```python
import torch

def _softmax(x, axis=-1):
    return torch.softmax(x, dim=axis)
def _gelu(x):
    return torch.nn.functional.gelu(x, approximate="tanh")
def _silu(x):
    return torch.nn.functional.silu(x)
def _sigmoid(x):
    return torch.sigmoid(x)
def _layernorm(x, axis=-1, eps=1e-5):
    m = x.mean(dim=axis, keepdim=True)
    v = x.var(dim=axis, keepdim=True, unbiased=False)
    return (x - m) / torch.sqrt(v + eps)
def _scatter_add(tgt, idx, src, axis=0):
    return tgt.clone().index_add_(axis, idx, src)
def _segment_sum(src, seg, n):
    out = torch.zeros((n,) + src.shape[1:], dtype=src.dtype)
    return out.index_add_(0, seg, src)
def _pad(x, pw):
    flat = tuple(v for pair in reversed(pw) for v in pair)
    return torch.nn.functional.pad(x, flat)

kernel_name = 'standalone__gqa_block_cached__near_ridge'

def setup_inputs():

    _w = (lambda a: (lambda t: t.to(torch.bfloat16) if t.is_floating_point() else t)(torch.as_tensor(a)))
    B, Sq, D = 16, 64, 512
    Hq, Hkv, Sk, Dh = 32, 32, 64, 128
    assert Hq % Hkv == 0, (Hq, Hkv)
    return {
        "x": _w((lambda *s: torch.randn(*s, dtype=torch.bfloat16) * 0.8)(B, Sq, D)),
        "k_cache": _w((lambda *s: torch.randn(*s, dtype=torch.bfloat16) * 0.8)(B, Hkv, Sk, Dh)),
        "v_cache": _w((lambda *s: torch.randn(*s, dtype=torch.bfloat16) * 0.05)(B, Hkv, Sk, Dh)),
        "wq": _w((lambda *s: torch.randn(*s, dtype=torch.bfloat16) * 0.8)(D, Hq * Dh) * (D ** -0.5)),
        "wo": _w((lambda *s: torch.randn(*s, dtype=torch.bfloat16) * 0.05)(Hq * Dh, D) * ((Hq * Dh) ** -0.5)),
    }

def reference(x, k_cache, v_cache, wq, wo):
    B, Sq, D = x.shape
    Hq, Dh = 32, 128
    Hkv = k_cache.shape[1]
    rep = Hq // Hkv
    q = (lambda x, *d: x.permute(*d))((x @ wq).reshape(B, Sq, Hq, Dh), 0, 2, 1, 3)
    k = (lambda x, n, axis: torch.repeat_interleave(x, n, dim=axis))(k_cache, rep, axis=1)
    v = (lambda x, n, axis: torch.repeat_interleave(x, n, dim=axis))(v_cache, rep, axis=1)
    scores = torch.einsum("bhqd,bhkd->bhqk", q, k) * (Dh ** -0.5)
    p = _softmax(scores, axis=-1)
    o = torch.einsum("bhqk,bhkd->bhqd", p, v)
    return (lambda x, *d: x.permute(*d))(o, 0, 2, 1, 3).reshape(B, Sq, Hq * Dh) @ wo
```

```cpp
#include <hip/hip_runtime.h>
#include <hip/hip_bf16.h>
typedef unsigned short u16;
__device__ inline float bf2f(u16 v){ return __uint_as_float(((unsigned)v)<<16); }
__device__ inline u16 f2bf(float f){ unsigned u=__float_as_uint(f); u += 0x7fff + ((u>>16)&1); return (u16)(u>>16); }
__global__ __launch_bounds__(256) void k1(const u16* x, const u16* kc, const u16* vc, const u16* wq, u16* o) {
  int bh = blockIdx.x; int b = bh/32, h = bh%32;
  __shared__ float q[64][129]; __shared__ float s[64][65];
  int t = threadIdx.x;
  for (int i = t; i < 64*128; i += 256) { int r=i/128, c=i%128; float acc=0;
    for (int k=0;k<512;k++) acc += bf2f(x[(b*64+r)*512+k]) * bf2f(wq[k*4096 + h*128 + c]);
    q[r][c] = bf2f(f2bf(acc)); }
  __syncthreads();
  for (int i = t; i < 64*64; i += 256) { int r=i/64, c=i%64; float acc=0;
    for (int k=0;k<128;k++) acc += q[r][k] * bf2f(kc[((size_t)bh*64+c)*128+k]);
    s[r][c] = acc * 0.08838834764831845f; }
  __syncthreads();
  if (t < 64) { float m=-1e30f; for (int c=0;c<64;c++) m=fmaxf(m,s[t][c]); float sum=0; for (int c=0;c<64;c++){ float e=__expf(s[t][c]-m); s[t][c]=e; sum+=e;}
    for (int c=0;c<64;c++) s[t][c] = bf2f(f2bf(s[t][c]/sum)); }
  __syncthreads();
  for (int i = t; i < 64*128; i += 256) { int r=i/128, c=i%128; float acc=0;
    for (int k=0;k<64;k++) acc += s[r][k] * bf2f(vc[((size_t)bh*64+k)*128+c]);
    o[(size_t)(b*64+r)*4096 + h*128 + c] = f2bf(acc); }
}
__global__ __launch_bounds__(256) void k2(const u16* o, const u16* wo, u16* out) {
  int i = blockIdx.x*256+threadIdx.x; int r=i/512, c=i%512; float acc=0;
  for (int k=0;k<4096;k++) acc += bf2f(o[(size_t)r*4096+k]) * bf2f(wo[k*512+c]);
  out[i]=f2bf(acc);
}
extern "C" void kernel_launch(void* const* d_in, const int* in_sizes, int n_in, void* d_out, int out_size, void* d_ws, size_t ws_size, hipStream_t stream) {
  const u16* x=(const u16*)d_in[0]; const u16* kc=(const u16*)d_in[1]; const u16* vc=(const u16*)d_in[2]; const u16* wq=(const u16*)d_in[3]; const u16* wo=(const u16*)d_in[4];
  u16* o=(u16*)d_ws;
  k1<<<512,256,0,stream>>>(x,kc,vc,wq,o);
  k2<<<2048,256,0,stream>>>(o,wo,(u16*)d_out);
}
```

```cpp
#include <hip/hip_runtime.h>
#include <hip/hip_bf16.h>

typedef unsigned short u16;
typedef unsigned long long u64;
typedef short s16x4 __attribute__((ext_vector_type(4)));
typedef short s16x8 __attribute__((ext_vector_type(8)));
typedef __bf16 bf16x8 __attribute__((ext_vector_type(8)));
typedef __bf16 bf16x2 __attribute__((ext_vector_type(2)));
typedef float f32x2 __attribute__((ext_vector_type(2)));
typedef float f32x16 __attribute__((ext_vector_type(16)));
typedef unsigned u32x4 __attribute__((ext_vector_type(4)));
#define LAS __attribute__((address_space(3)))
#define GAS __attribute__((address_space(1)))
#define DI __device__ __forceinline__
#define RLX_AGENT __ATOMIC_RELAXED, __HIP_MEMORY_SCOPE_AGENT

#define STAMPS 0

constexpr size_t WS_O     = 0;
constexpr size_t WS_P     = 8u << 20;
constexpr size_t WS_FLAG1 = 16u << 20;
constexpr size_t WS_FLAG2 = WS_FLAG1 + (128u << 10);
constexpr size_t WS_EPOCH = WS_FLAG2 + (128u << 10);
constexpr size_t WS_STAMP = 32u << 20;

constexpr unsigned L_RING = 0;
constexpr unsigned L_K    = 96u << 10;
constexpr unsigned L_V    = 128u << 10;
constexpr unsigned LDS_BYTES = 160u << 10;

DI void dma16(const char* sbase, unsigned voff, unsigned lds) {
  asm volatile("s_mov_b32 m0, %2\n\ts_nop 0\n\tglobal_load_lds_dwordx4 %0, %1" ::"v"(voff), "s"(sbase), "s"(lds) : "memory");
}
DI void dma16nt(const char* sbase, unsigned voff, unsigned lds) {
  asm volatile("s_mov_b32 m0, %2\n\ts_nop 0\n\tglobal_load_lds_dwordx4 %0, %1 nt" ::"v"(voff), "s"(sbase), "s"(lds) : "memory");
}
template <int N> DI void wait_vm_bar() { asm volatile("s_waitcnt vmcnt(%0)\n\ts_barrier" ::"i"(N) : "memory"); }
DI void bar_only() { asm volatile("s_barrier" ::: "memory"); }
DI void lgkm_bar() { asm volatile("s_waitcnt lgkmcnt(0)\n\ts_barrier" ::: "memory"); }

DI u64 rtime() { u64 t; asm volatile("s_memrealtime %0\n\ts_waitcnt lgkmcnt(0)" : "=s"(t)::"memory"); return t; }

DI bf16x8 tr_frag(const LAS char* p) {
  s16x4 lo = __builtin_amdgcn_ds_read_tr16_b64_v4i16((LAS s16x4*)p);
  s16x4 hi = __builtin_amdgcn_ds_read_tr16_b64_v4i16((LAS s16x4*)(p + 1024));
  return __builtin_bit_cast(bf16x8, __builtin_shufflevector(lo, hi, 0, 1, 2, 3, 4, 5, 6, 7));
}
DI unsigned pk2(float a, float b) { f32x2 v = {a, b}; return __builtin_bit_cast(unsigned, __builtin_convertvector(v, bf16x2)); }
DI bf16x8 pack8(const f32x16& a, int s) {
  u32x4 r = {pk2(a[8 * s], a[8 * s + 1]), pk2(a[8 * s + 2], a[8 * s + 3]), pk2(a[8 * s + 4], a[8 * s + 5]), pk2(a[8 * s + 6], a[8 * s + 7])};
  return __builtin_bit_cast(bf16x8, r);
}
#define MFMA(a, b, c) __builtin_amdgcn_mfma_f32_32x32x16_bf16((a), (b), (c), 0, 0, 0)

DI void wait_flags(const GAS u64* p, int lane, int n, u64 tag) {
  const GAS u64* q = p + (lane < n ? lane : 0);
  for (unsigned spins = 0; spins < (1u << 22); ++spins) {
    u64 v = __hip_atomic_load(q, RLX_AGENT);
    if (__all(v == tag)) break;
    __builtin_amdgcn_s_sleep(1);
  }
  asm volatile("" ::: "memory");
}

__global__ __launch_bounds__(512, 2) void gqa_fused(const char* __restrict__ x, const char* __restrict__ kc, const char* __restrict__ vc,
                                                   const char* __restrict__ wq, const char* __restrict__ wo, char* __restrict__ out, char* __restrict__ ws) {
  __shared__ __attribute__((aligned(16))) char lds_raw[LDS_BYTES];
  const LAS char* lds = (const LAS char*)lds_raw;
  const unsigned lds0 = (unsigned)(size_t)lds;
  const int lane = threadIdx.x & 63;
  const int w8 = __builtin_amdgcn_readfirstlane(threadIdx.x >> 6);
  const int j = blockIdx.x & 7, bp = blockIdx.x >> 5, hh = (blockIdx.x >> 3) & 3, h = 4 * j + hh;

  if (w8 >= 4) {
    const int lw = w8 - 4;
    const int rot = (bp + 2 * hh + (j & 1)) & 7;
    const unsigned l4 = lane >> 4, l3 = lane >> 3;
    const unsigned vx0 = l3 * 1024 + (((lane & 7) ^ l4) << 4), vx1 = vx0 ^ 64;
    const unsigned ptr_ = (lane & 15) ^ (l4 << 2);
    const unsigned vwq = l4 * 8192 + (ptr_ << 4);
    const unsigned vv = l4 * 256 + (ptr_ << 4);
    const unsigned vwo = l4 * 1024 + ((ptr_ ^ (unsigned)(j << 1)) << 4);
    const unsigned vk = l4 * 256 + (((lane & 15) ^ l4) << 4);
    const char* xb = x + (size_t)(bp * 128 + lw * 32) * 1024;
    const char* wqb = wq + (size_t)(lw * 16) * 8192 + h * 256;
    const size_t kvoff = (size_t)((bp * 2 + (lw >> 1)) * 32 + h) * 16384 + (lw & 1) * 8192;
    const char* kb = kc + kvoff;
    const char* vb = vc + kvoff;
    const char* wob = wo + (size_t)(j * 512 + lw * 16) * 1024 + hh * 256;

    auto chunk = [&](int c) {
      const int cc = (c + rot) & 7;
      const unsigned slot = lds0 + L_RING + (c % 3) * 32768;
#pragma unroll
      for (int q = 0; q < 4; ++q) {
        dma16(wqb + (size_t)(cc * 64 + q * 4) * 8192, vwq, slot + 16384 + (lw * 16 + q * 4) * 256);
        dma16(xb + q * 8192 + cc * 128, (q & 1) ? vx1 : vx0, slot + (lw * 32 + q * 8) * 128);
      }
    };
    auto loadK = [&]() {
#pragma unroll
      for (int q = 0; q < 8; ++q) dma16nt(kb + q * 1024, vk ^ ((q & 3) << 6), lds0 + L_K + lw * 8192 + q * 1024);
    };
    auto loadV = [&]() {
#pragma unroll
      for (int q = 0; q < 8; ++q) dma16nt(vb + q * 1024, vv, lds0 + L_V + lw * 8192 + q * 1024);
    };
    auto loadWo = [&](int g, unsigned dst) {
      const char* b = wob + (size_t)((hh ^ g) * 128) * 1024;
#pragma unroll
      for (int q = 0; q < 8; ++q) dma16(b + (size_t)((q >> 2) * 64 + (q & 3) * 4) * 1024, vwo, dst + ((q >> 2) * 64 + lw * 16 + (q & 3) * 4) * 256);
    };
    chunk(0); chunk(1); chunk(2);
    wait_vm_bar<16>();
    wait_vm_bar<8>(); chunk(3);
    wait_vm_bar<8>(); chunk(4);
    wait_vm_bar<8>(); chunk(5);
    wait_vm_bar<8>(); chunk(6);
    wait_vm_bar<8>(); chunk(7);
    wait_vm_bar<8>(); loadK();
    wait_vm_bar<8>(); loadV();
    wait_vm_bar<8>();
    loadWo(0, lds0 + 0);
    wait_vm_bar<8>();
    loadWo(1, lds0 + 32768);
    wait_vm_bar<8>();
    loadWo(2, lds0 + 65536);
    loadWo(3, lds0 + 98304);
    wait_vm_bar<16>();
    wait_vm_bar<8>();
    wait_vm_bar<0>();
    return;
  }

  const int w = w8;
  const int r = lane & 31, hf = lane >> 5;
  const int grp = bp * 4 + w;
  const int me = (grp * 8 + j) * 4 + hh;
  const u64 tag = __hip_atomic_load((const GAS u64*)(ws + WS_EPOCH + grp * 128), RLX_AGENT) + 1;

  const unsigned q4 = (lane & 15) >> 2, p4 = lane & 3, blk = (lane >> 4) & 1;
  const unsigned trc = (8 * hf + q4) * 256 + 32 * blk + 8 * (((p4 & 1) << 1) | (p4 >> 1));
  unsigned trb[4];
#pragma unroll
  for (int c = 0; c < 4; ++c) trb[c] = trc + ((c ^ q4) << 6);
  const unsigned xsw = (r >> 1) & 7;
  unsigned xfb[4];
#pragma unroll
  for (int s = 0; s < 4; ++s) xfb[s] = (w * 32 + r) * 128 + (((2 * s + hf) ^ xsw) << 4);

  f32x16 acc[4];
#pragma unroll
  for (int c = 0; c < 4; ++c)
#pragma unroll
    for (int i = 0; i < 16; ++i) acc[c][i] = 0.f;

  {
    bf16x8 A[2][4], B[2];
    bar_only();
    auto rd = [&](int g, int buf) {
      const int c = g >> 2, s = g & 3;
      const unsigned slot = (c % 3) * 32768;
      B[buf] = *(const LAS bf16x8*)(lds + slot + xfb[s]);
#pragma unroll
      for (int t = 0; t < 4; ++t) A[buf][t] = tr_frag(lds + slot + 16384 + s * 4096 + trb[t]);
    };
    rd(0, 0);
#pragma unroll
    for (int g = 0; g < 32; ++g) {
      if (g + 1 < 32) {
        if (((g + 1) & 3) == 0) lgkm_bar();
        rd(g + 1, (g + 1) & 1);
      }
#pragma unroll
      for (int t = 0; t < 4; ++t) acc[t] = MFMA(A[g & 1][t], B[g & 1], acc[t]);
      __builtin_amdgcn_sched_barrier(0);
    }
  }
  bf16x8 f[8];
#pragma unroll
  for (int s = 0; s < 8; ++s) f[s] = pack8(acc[s >> 1], s & 1);
  lgkm_bar();
  f32x16 sc[2];
#pragma unroll
  for (int t = 0; t < 2; ++t)
#pragma unroll
    for (int i = 0; i < 16; ++i) sc[t][i] = 0.f;
  {
    const int kr = (r & 19) | ((r & 4) << 1) | ((r & 8) >> 1);
    const LAS char* kp = lds + L_K + (w >> 1) * 16384 + kr * 256;
#pragma unroll
    for (int s = 0; s < 8; ++s) {
      const unsigned o = ((2 * s + hf) ^ (kr & 15)) << 4;
      bf16x8 k0 = *(const LAS bf16x8*)(kp + o);
      bf16x8 k1 = *(const LAS bf16x8*)(kp + 8192 + o);
      sc[0] = MFMA(k0, f[s], sc[0]);
      sc[1] = MFMA(k1, f[s], sc[1]);
    }
  }
  {
    constexpr float C = 0.08838834764831845f * 1.4426950408889634f;
    float m = sc[0][0];
#pragma unroll
    for (int i = 1; i < 16; ++i) m = fmaxf(m, sc[0][i]);
#pragma unroll
    for (int i = 0; i < 16; ++i) m = fmaxf(m, sc[1][i]);
    auto rr = __builtin_amdgcn_permlane32_swap(__float_as_uint(m), __float_as_uint(m), false, false);
    m = fmaxf(__uint_as_float(rr[0]), __uint_as_float(rr[1]));
    const float mc = -m * C;
    float sum = 0.f;
#pragma unroll
    for (int t = 0; t < 2; ++t)
#pragma unroll
      for (int i = 0; i < 16; ++i) { sc[t][i] = __builtin_amdgcn_exp2f(fmaf(sc[t][i], C, mc)); sum += sc[t][i]; }
    auto r2 = __builtin_amdgcn_permlane32_swap(__float_as_uint(sum), __float_as_uint(sum), false, false);
    const float inv = __builtin_amdgcn_rcpf(__uint_as_float(r2[0]) + __uint_as_float(r2[1]));
#pragma unroll
    for (int t = 0; t < 2; ++t)
#pragma unroll
      for (int i = 0; i < 16; ++i) sc[t][i] *= inv;
#pragma unroll
    for (int s = 0; s < 4; ++s) f[s] = pack8(sc[s >> 1], s & 1);
  }
  lgkm_bar();
#pragma unroll
  for (int c = 0; c < 4; ++c)
#pragma unroll
    for (int i = 0; i < 16; ++i) acc[c][i] = 0.f;
  {
    const LAS char* vp = lds + L_V + (w >> 1) * 16384;
#pragma unroll
    for (int s = 0; s < 4; ++s)
#pragma unroll
      for (int t = 0; t < 4; ++t) acc[t] = MFMA(tr_frag(vp + s * 4096 + trb[t]), f[s], acc[t]);
  }
#pragma unroll
  for (int s = 0; s < 8; ++s) f[s] = pack8(acc[s >> 1], s & 1);
  const __amdgpu_buffer_rsrc_t wsr = __builtin_amdgcn_make_buffer_rsrc(ws, 0, 16u << 20, 0x00020000);
#pragma unroll
  for (int s = 0; s < 8; ++s) __builtin_amdgcn_raw_buffer_store_b128(__builtin_bit_cast(u32x4, f[s]), wsr, lane * 16 + s * 1024, me * 8192, 16);
#pragma unroll
  for (int c = 0; c < 4; ++c)
#pragma unroll
    for (int i = 0; i < 16; ++i) acc[c][i] = 0.f;
  asm volatile("s_waitcnt vmcnt(0)" ::: "memory");
  if (lane >= 1 && lane < 4)
    __hip_atomic_store((GAS u64*)(ws + WS_FLAG1 + (size_t)(me ^ lane) * 128) + (lane - 1), tag, RLX_AGENT);
  bar_only();
#pragma unroll
  for (int s = 0; s < 8; ++s)
#pragma unroll
    for (int t = 0; t < 4; ++t) acc[t] = MFMA(tr_frag(lds + s * 4096 + trb[t]), f[s], acc[t]);
  wait_flags((const GAS u64*)(ws + WS_FLAG1 + (size_t)me * 128), lane, 3, tag);
  bf16x8 of[3][8];
#pragma unroll
  for (int g = 1; g < 4; ++g)
#pragma unroll
    for (int s = 0; s < 8; ++s)
      of[g - 1][s] = __builtin_bit_cast(bf16x8, __builtin_amdgcn_raw_buffer_load_b128(wsr, lane * 16 + s * 1024, (me ^ g) * 8192, 16));
  __builtin_amdgcn_sched_barrier(0);
#pragma unroll
  for (int g = 1; g < 4; ++g) {
    bar_only();
#pragma unroll
    for (int s = 0; s < 8; ++s)
#pragma unroll
      for (int t = 0; t < 4; ++t) acc[t] = MFMA(tr_frag(lds + g * 32768 + s * 4096 + trb[t]), of[g - 1][s], acc[t]);
  }
#pragma unroll
  for (int d = 1; d < 8; ++d)
    __builtin_amdgcn_raw_buffer_store_b128(__builtin_bit_cast(u32x4, pack8(acc[d >> 1], d & 1)), wsr, lane * 16 + d * 1024, (unsigned)WS_P + (me ^ (d << 2)) * 8192, 16);
  asm volatile("s_waitcnt vmcnt(0)" ::: "memory");
  if (lane >= 1 && lane < 8)
    __hip_atomic_store((GAS u64*)(ws + WS_FLAG2 + (size_t)(me ^ (lane << 2)) * 128) + (lane - 1), tag, RLX_AGENT);
  wait_flags((const GAS u64*)(ws + WS_FLAG2 + (size_t)me * 128), lane, 7, tag);
  float o[8];
#pragma unroll
  for (int i = 0; i < 8; ++i) o[i] = acc[0][i];
  u32x4 pv[7];
#pragma unroll
  for (int d = 1; d < 8; ++d) pv[d - 1] = __builtin_amdgcn_raw_buffer_load_b128(wsr, lane * 16 + d * 1024, (unsigned)WS_P + me * 8192, 16);
  __builtin_amdgcn_sched_barrier(0);
#pragma unroll
  for (int d = 0; d < 7; ++d)
#pragma unroll
    for (int i = 0; i < 4; ++i) {
      o[2 * i] += __uint_as_float(pv[d][i] << 16);
      o[2 * i + 1] += __uint_as_float(pv[d][i] & 0xffff0000u);
    }
  u32x4 ov = {pk2(o[0], o[1]), pk2(o[2], o[3]), pk2(o[4], o[5]), pk2(o[6], o[7])};
  *(GAS u32x4*)(out + (size_t)(bp * 128 + w * 32 + r) * 1024 + hh * 256 + j * 32 + hf * 16) = ov;
  if (j == 0 && hh == 0 && lane == 0) __hip_atomic_store((GAS u64*)(ws + WS_EPOCH + grp * 128), tag, RLX_AGENT);
}

extern "C" void kernel_launch(void* const* d_in, const int* in_sizes, int n_in, void* d_out, int out_size, void* d_ws, size_t ws_size,
                              hipStream_t stream) {
  gqa_fused<<<256, 512, 0, stream>>>((const char*)d_in[0], (const char*)d_in[1], (const char*)d_in[2], (const char*)d_in[3],
                                     (const char*)d_in[4], (char*)d_out, (char*)d_ws);
}
```

```cpp
#include <hip/hip_runtime.h>
#include <hip/hip_bf16.h>

typedef unsigned short u16;
typedef unsigned long long u64;
typedef short s16x4 __attribute__((ext_vector_type(4)));
typedef short s16x8 __attribute__((ext_vector_type(8)));
typedef __bf16 bf16x8 __attribute__((ext_vector_type(8)));
typedef __bf16 bf16x2 __attribute__((ext_vector_type(2)));
typedef float f32x2 __attribute__((ext_vector_type(2)));
typedef float f32x16 __attribute__((ext_vector_type(16)));
typedef unsigned u32x4 __attribute__((ext_vector_type(4)));
#define LAS __attribute__((address_space(3)))
#define GAS __attribute__((address_space(1)))
#define DI __device__ __forceinline__
#define RLX_AGENT __ATOMIC_RELAXED, __HIP_MEMORY_SCOPE_AGENT

#define STAMPS 1
#define PROBE 1

constexpr size_t WS_O     = 0;
constexpr size_t WS_P     = 8u << 20;
constexpr size_t WS_FLAG1 = 16u << 20;
constexpr size_t WS_FLAG2 = WS_FLAG1 + (128u << 10);
constexpr size_t WS_EPOCH = WS_FLAG2 + (128u << 10);
constexpr size_t WS_STAMP = 32u << 20;

constexpr unsigned L_RING = 0;
constexpr unsigned L_K    = 96u << 10;
constexpr unsigned L_V    = 128u << 10;
constexpr unsigned LDS_BYTES = 160u << 10;

DI void dma16(const char* sbase, unsigned voff, unsigned lds) {
  asm volatile("s_mov_b32 m0, %2\n\ts_nop 0\n\tglobal_load_lds_dwordx4 %0, %1" ::"v"(voff), "s"(sbase), "s"(lds) : "memory");
}
DI void dma16nt(const char* sbase, unsigned voff, unsigned lds) {
  asm volatile("s_mov_b32 m0, %2\n\ts_nop 0\n\tglobal_load_lds_dwordx4 %0, %1 nt" ::"v"(voff), "s"(sbase), "s"(lds) : "memory");
}
template <int N> DI void wait_vm_bar() { asm volatile("s_waitcnt vmcnt(%0)\n\ts_barrier" ::"i"(N) : "memory"); }
DI void bar_only() { asm volatile("s_barrier" ::: "memory"); }
DI void lgkm_bar() { asm volatile("s_waitcnt lgkmcnt(0)\n\ts_barrier" ::: "memory"); }

DI u64 rtime() { u64 t; asm volatile("s_memrealtime %0\n\ts_waitcnt lgkmcnt(0)" : "=s"(t)::"memory"); return t; }

DI bf16x8 tr_frag(const LAS char* p) {
  s16x4 lo = __builtin_amdgcn_ds_read_tr16_b64_v4i16((LAS s16x4*)p);
  s16x4 hi = __builtin_amdgcn_ds_read_tr16_b64_v4i16((LAS s16x4*)(p + 1024));
  return __builtin_bit_cast(bf16x8, __builtin_shufflevector(lo, hi, 0, 1, 2, 3, 4, 5, 6, 7));
}
DI unsigned pk2(float a, float b) { f32x2 v = {a, b}; return __builtin_bit_cast(unsigned, __builtin_convertvector(v, bf16x2)); }
DI bf16x8 pack8(const f32x16& a, int s) {
  u32x4 r = {pk2(a[8 * s], a[8 * s + 1]), pk2(a[8 * s + 2], a[8 * s + 3]), pk2(a[8 * s + 4], a[8 * s + 5]), pk2(a[8 * s + 6], a[8 * s + 7])};
  return __builtin_bit_cast(bf16x8, r);
}
#define MFMA(a, b, c) __builtin_amdgcn_mfma_f32_32x32x16_bf16((a), (b), (c), 0, 0, 0)

DI void wait_flags(const GAS u64* p, int lane, int n, u64 tag) {
  const GAS u64* q = p + (lane < n ? lane : 0);
  for (unsigned spins = 0; spins < (1u << 22); ++spins) {
    u64 v = __hip_atomic_load(q, RLX_AGENT);
    if (__all(v == tag)) break;
    __builtin_amdgcn_s_sleep(1);
  }
  asm volatile("" ::: "memory");
}

__global__ __launch_bounds__(512, 2) void gqa_fused(const char* __restrict__ x, const char* __restrict__ kc, const char* __restrict__ vc,
                                                   const char* __restrict__ wq, const char* __restrict__ wo, char* __restrict__ out, char* __restrict__ ws) {
  __shared__ __attribute__((aligned(16))) char lds_raw[LDS_BYTES];
  const LAS char* lds = (const LAS char*)lds_raw;
  const unsigned lds0 = (unsigned)(size_t)lds;
  const int lane = threadIdx.x & 63;
  const int w8 = __builtin_amdgcn_readfirstlane(threadIdx.x >> 6);
  const int j = blockIdx.x & 7, bp = blockIdx.x >> 5, hh = (blockIdx.x >> 3) & 3, h = 4 * j + hh;
  u64 st[12];
  st[0] = rtime();

  if (w8 >= 4) {
    const int lw = w8 - 4;
    const int rot = (bp + 2 * hh + (j & 1)) & 7;
    const unsigned l4 = lane >> 4, l3 = lane >> 3;
    const unsigned vx0 = l3 * 1024 + (((lane & 7) ^ l4) << 4), vx1 = vx0 ^ 64;
    const unsigned ptr_ = (lane & 15) ^ (l4 << 2);
    const unsigned vwq = l4 * 8192 + (ptr_ << 4);
    const unsigned vv = l4 * 256 + (ptr_ << 4);
    const unsigned vwo = l4 * 1024 + ((ptr_ ^ (unsigned)(j << 1)) << 4);
    const unsigned vk = l4 * 256 + (((lane & 15) ^ l4) << 4);
    const char* xb = x + (size_t)(bp * 128 + lw * 32) * 1024;
    const char* wqb = wq + (size_t)(lw * 16) * 8192 + h * 256;
    const size_t kvoff = (size_t)((bp * 2 + (lw >> 1)) * 32 + h) * 16384 + (lw & 1) * 8192;
    const char* kb = kc + kvoff;
    const char* vb = vc + kvoff;
    const char* wob = wo + (size_t)(j * 512 + lw * 16) * 1024 + hh * 256;

    auto chunk = [&](int c) {
      const int cc = (c + rot) & 7;
      const unsigned slot = lds0 + L_RING + (c % 3) * 32768;
#pragma unroll
      for (int q = 0; q < 4; ++q) {
        dma16(wqb + (size_t)(cc * 64 + q * 4) * 8192, vwq, slot + 16384 + (lw * 16 + q * 4) * 256);
        dma16(xb + q * 8192 + cc * 128, (q & 1) ? vx1 : vx0, slot + (lw * 32 + q * 8) * 128);
      }
    };
    auto loadK = [&]() {
#pragma unroll
      for (int q = 0; q < 8; ++q) dma16nt(kb + q * 1024, vk ^ ((q & 3) << 6), lds0 + L_K + lw * 8192 + q * 1024);
    };
    auto loadV = [&]() {
#pragma unroll
      for (int q = 0; q < 8; ++q) dma16nt(vb + q * 1024, vv, lds0 + L_V + lw * 8192 + q * 1024);
    };
    auto loadWo = [&](int g, unsigned dst) {
      const char* b = wob + (size_t)((hh ^ g) * 128) * 1024;
#pragma unroll
      for (int q = 0; q < 8; ++q) dma16(b + (size_t)((q >> 2) * 64 + (q & 3) * 4) * 1024, vwo, dst + ((q >> 2) * 64 + lw * 16 + (q & 3) * 4) * 256);
    };
    chunk(0); chunk(1); chunk(2);
    wait_vm_bar<16>();
    wait_vm_bar<8>(); chunk(3);
    wait_vm_bar<8>(); chunk(4);
    wait_vm_bar<8>(); chunk(5);
    wait_vm_bar<8>(); chunk(6);
    wait_vm_bar<8>(); chunk(7);
    wait_vm_bar<8>(); loadK();
    wait_vm_bar<8>(); loadV();
    wait_vm_bar<8>();
    loadWo(0, lds0 + 0);
    wait_vm_bar<8>();
    loadWo(1, lds0 + 32768);
    wait_vm_bar<8>();
    loadWo(2, lds0 + 65536);
    loadWo(3, lds0 + 98304);
    wait_vm_bar<16>();
    wait_vm_bar<8>();
    wait_vm_bar<0>();
    st[1] = rtime();

    return;
  }

  const int w = w8;
  const int r = lane & 31, hf = lane >> 5;
  const int grp = bp * 4 + w;
  const int me = (grp * 8 + j) * 4 + hh;
  const u64 tag = __hip_atomic_load((const GAS u64*)(ws + WS_EPOCH + grp * 128), RLX_AGENT) + 1;

  const unsigned q4 = (lane & 15) >> 2, p4 = lane & 3, blk = (lane >> 4) & 1;
  const unsigned trc = (8 * hf + q4) * 256 + 32 * blk + 8 * (((p4 & 1) << 1) | (p4 >> 1));
  unsigned trb[4];
#pragma unroll
  for (int c = 0; c < 4; ++c) trb[c] = trc + ((c ^ q4) << 6);
  const unsigned xsw = (r >> 1) & 7;
  unsigned xfb[4];
#pragma unroll
  for (int s = 0; s < 4; ++s) xfb[s] = (w * 32 + r) * 128 + (((2 * s + hf) ^ xsw) << 4);

  f32x16 acc[4];
#pragma unroll
  for (int c = 0; c < 4; ++c)
#pragma unroll
    for (int i = 0; i < 16; ++i) acc[c][i] = 0.f;

  {
    bf16x8 A[2][4], B[2];
    bar_only();
    st[1] = rtime();
    auto rd = [&](int g, int buf) {
      const int c = g >> 2, s = g & 3;
      const unsigned slot = (c % 3) * 32768;
      B[buf] = *(const LAS bf16x8*)(lds + slot + xfb[s]);
#pragma unroll
      for (int t = 0; t < 4; ++t) A[buf][t] = tr_frag(lds + slot + 16384 + s * 4096 + trb[t]);
    };
    rd(0, 0);
#pragma unroll
    for (int g = 0; g < 32; ++g) {
      if (g + 1 < 32) {
        if (((g + 1) & 3) == 0) lgkm_bar();
        rd(g + 1, (g + 1) & 1);
      }
#pragma unroll
      for (int t = 0; t < 4; ++t) acc[t] = MFMA(A[g & 1][t], B[g & 1], acc[t]);
      __builtin_amdgcn_sched_barrier(0);
    }
  }
  st[2] = rtime();
  bf16x8 f[8];
#pragma unroll
  for (int s = 0; s < 8; ++s) f[s] = pack8(acc[s >> 1], s & 1);
  lgkm_bar();
  st[3] = rtime();
  f32x16 sc[2];
#pragma unroll
  for (int t = 0; t < 2; ++t)
#pragma unroll
    for (int i = 0; i < 16; ++i) sc[t][i] = 0.f;
  {
    const int kr = (r & 19) | ((r & 4) << 1) | ((r & 8) >> 1);
    const LAS char* kp = lds + L_K + (w >> 1) * 16384 + kr * 256;
#pragma unroll
    for (int s = 0; s < 8; ++s) {
      const unsigned o = ((2 * s + hf) ^ (kr & 15)) << 4;
      bf16x8 k0 = *(const LAS bf16x8*)(kp + o);
      bf16x8 k1 = *(const LAS bf16x8*)(kp + 8192 + o);
      sc[0] = MFMA(k0, f[s], sc[0]);
      sc[1] = MFMA(k1, f[s], sc[1]);
    }
  }
  {
    constexpr float C = 0.08838834764831845f * 1.4426950408889634f;
    float m = sc[0][0];
#pragma unroll
    for (int i = 1; i < 16; ++i) m = fmaxf(m, sc[0][i]);
#pragma unroll
    for (int i = 0; i < 16; ++i) m = fmaxf(m, sc[1][i]);
    auto rr = __builtin_amdgcn_permlane32_swap(__float_as_uint(m), __float_as_uint(m), false, false);
    m = fmaxf(__uint_as_float(rr[0]), __uint_as_float(rr[1]));
    const float mc = -m * C;
    float sum = 0.f;
#pragma unroll
    for (int t = 0; t < 2; ++t)
#pragma unroll
      for (int i = 0; i < 16; ++i) { sc[t][i] = __builtin_amdgcn_exp2f(fmaf(sc[t][i], C, mc)); sum += sc[t][i]; }
    auto r2 = __builtin_amdgcn_permlane32_swap(__float_as_uint(sum), __float_as_uint(sum), false, false);
    const float inv = __builtin_amdgcn_rcpf(__uint_as_float(r2[0]) + __uint_as_float(r2[1]));
#pragma unroll
    for (int t = 0; t < 2; ++t)
#pragma unroll
      for (int i = 0; i < 16; ++i) sc[t][i] *= inv;
#pragma unroll
    for (int s = 0; s < 4; ++s) f[s] = pack8(sc[s >> 1], s & 1);
  }
  st[4] = rtime();
  lgkm_bar();
  st[5] = rtime();
#pragma unroll
  for (int c = 0; c < 4; ++c)
#pragma unroll
    for (int i = 0; i < 16; ++i) acc[c][i] = 0.f;
  {
    const LAS char* vp = lds + L_V + (w >> 1) * 16384;
#pragma unroll
    for (int s = 0; s < 4; ++s)
#pragma unroll
      for (int t = 0; t < 4; ++t) acc[t] = MFMA(tr_frag(vp + s * 4096 + trb[t]), f[s], acc[t]);
  }
#pragma unroll
  for (int s = 0; s < 8; ++s) f[s] = pack8(acc[s >> 1], s & 1);
  const __amdgpu_buffer_rsrc_t wsr = __builtin_amdgcn_make_buffer_rsrc(ws, 0, 16u << 20, 0x00020000);
#pragma unroll
  for (int s = 0; s < 8; ++s) __builtin_amdgcn_raw_buffer_store_b128(__builtin_bit_cast(u32x4, f[s]), wsr, lane * 16 + s * 1024, me * 8192, 16);
  st[6] = rtime();
#pragma unroll
  for (int c = 0; c < 4; ++c)
#pragma unroll
    for (int i = 0; i < 16; ++i) acc[c][i] = 0.f;
  asm volatile("s_waitcnt vmcnt(0)" ::: "memory");
  if (lane >= 1 && lane < 4)
    __hip_atomic_store((GAS u64*)(ws + WS_FLAG1 + (size_t)(me ^ lane) * 128) + (lane - 1), tag, RLX_AGENT);
  st[7] = rtime();
  bar_only();
#pragma unroll
  for (int s = 0; s < 8; ++s)
#pragma unroll
    for (int t = 0; t < 4; ++t) acc[t] = MFMA(tr_frag(lds + s * 4096 + trb[t]), f[s], acc[t]);
  wait_flags((const GAS u64*)(ws + WS_FLAG1 + (size_t)me * 128), lane, 3, tag);
  st[8] = rtime();
  bf16x8 of[3][8];
#pragma unroll
  for (int g = 1; g < 4; ++g)
#pragma unroll
    for (int s = 0; s < 8; ++s)
      of[g - 1][s] = __builtin_bit_cast(bf16x8, __builtin_amdgcn_raw_buffer_load_b128(wsr, lane * 16 + s * 1024, (me ^ g) * 8192, 16));
  __builtin_amdgcn_sched_barrier(0);
#pragma unroll
  for (int g = 1; g < 4; ++g) {
    bar_only();
#pragma unroll
    for (int s = 0; s < 8; ++s)
#pragma unroll
      for (int t = 0; t < 4; ++t) acc[t] = MFMA(tr_frag(lds + g * 32768 + s * 4096 + trb[t]), of[g - 1][s], acc[t]);
  }
  st[9] = rtime();
#pragma unroll
  for (int d = 1; d < 8; ++d)
    __builtin_amdgcn_raw_buffer_store_b128(__builtin_bit_cast(u32x4, pack8(acc[d >> 1], d & 1)), wsr, lane * 16 + d * 1024, (unsigned)WS_P + (me ^ (d << 2)) * 8192, 16);
  asm volatile("s_waitcnt vmcnt(0)" ::: "memory");
  if (lane >= 1 && lane < 8)
    __hip_atomic_store((GAS u64*)(ws + WS_FLAG2 + (size_t)(me ^ (lane << 2)) * 128) + (lane - 1), tag, RLX_AGENT);
  st[10] = rtime();
  wait_flags((const GAS u64*)(ws + WS_FLAG2 + (size_t)me * 128), lane, 7, tag);
  float o[8];
#pragma unroll
  for (int i = 0; i < 8; ++i) o[i] = acc[0][i];
  u32x4 pv[7];
#pragma unroll
  for (int d = 1; d < 8; ++d) pv[d - 1] = __builtin_amdgcn_raw_buffer_load_b128(wsr, lane * 16 + d * 1024, (unsigned)WS_P + me * 8192, 16);
  __builtin_amdgcn_sched_barrier(0);
#pragma unroll
  for (int d = 0; d < 7; ++d)
#pragma unroll
    for (int i = 0; i < 4; ++i) {
      o[2 * i] += __uint_as_float(pv[d][i] << 16);
      o[2 * i + 1] += __uint_as_float(pv[d][i] & 0xffff0000u);
    }
  u32x4 ov = {pk2(o[0], o[1]), pk2(o[2], o[3]), pk2(o[4], o[5]), pk2(o[6], o[7])};
  *(GAS u32x4*)(out + (size_t)(bp * 128 + w * 32 + r) * 1024 + hh * 256 + j * 32 + hf * 16) = ov;
  if (j == 0 && hh == 0 && lane == 0) __hip_atomic_store((GAS u64*)(ws + WS_EPOCH + grp * 128), tag, RLX_AGENT);
  st[11] = rtime();
  { u64 until = st[0] + 50 * (st[PROBE] - st[0]); while (rtime() < until) __builtin_amdgcn_s_sleep(4); }
}

extern "C" void kernel_launch(void* const* d_in, const int* in_sizes, int n_in, void* d_out, int out_size, void* d_ws, size_t ws_size,
                              hipStream_t stream) {
  gqa_fused<<<256, 512, 0, stream>>>((const char*)d_in[0], (const char*)d_in[1], (const char*)d_in[2], (const char*)d_in[3],
                                     (const char*)d_in[4], (char*)d_out, (char*)d_ws);
}
```

```cpp
#include <hip/hip_runtime.h>
#include <hip/hip_bf16.h>

typedef unsigned short u16;
typedef unsigned long long u64;
typedef short s16x4 __attribute__((ext_vector_type(4)));
typedef short s16x8 __attribute__((ext_vector_type(8)));
typedef __bf16 bf16x8 __attribute__((ext_vector_type(8)));
typedef __bf16 bf16x2 __attribute__((ext_vector_type(2)));
typedef float f32x2 __attribute__((ext_vector_type(2)));
typedef float f32x16 __attribute__((ext_vector_type(16)));
typedef unsigned u32x4 __attribute__((ext_vector_type(4)));
#define LAS __attribute__((address_space(3)))
#define GAS __attribute__((address_space(1)))
#define DI __device__ __forceinline__
#define RLX_AGENT __ATOMIC_RELAXED, __HIP_MEMORY_SCOPE_AGENT
#define SB() __builtin_amdgcn_sched_barrier(0)
#define AI __attribute__((always_inline))

#define STAMPS 0
#define ROT 3
#define DRAIN1 16
#define STAMP(i) do { } while (0)

constexpr size_t WS_O     = 0;
constexpr size_t WS_P     = 8u << 20;
constexpr size_t WS_FLAG  = 16u << 20;
constexpr size_t WS_EPOCH = WS_FLAG + (256u << 10);
constexpr size_t WS_STAMP = 32u << 20;

constexpr unsigned L_K = 0, L_V = 4u * 32768;
constexpr unsigned LDS_BYTES = 160u << 10;
constexpr unsigned cslot(int c) { return (c < 5 ? c : c - 4) * 32768u; }

DI void dma16(const char* sbase, unsigned voff, unsigned lds) {
  asm volatile("s_mov_b32 m0, %2\n\ts_nop 0\n\tglobal_load_lds_dwordx4 %0, %1" ::"v"(voff), "s"(sbase), "s"(lds) : "memory");
}
DI void dma16nt(const char* sbase, unsigned voff, unsigned lds) {
  asm volatile("s_mov_b32 m0, %2\n\ts_nop 0\n\tglobal_load_lds_dwordx4 %0, %1 nt" ::"v"(voff), "s"(sbase), "s"(lds) : "memory");
}
DI void dma4(const char* sbase, unsigned voff, unsigned lds) {
  asm volatile("s_mov_b32 m0, %2\n\ts_nop 0\n\tglobal_load_lds_dword %0, %1" ::"v"(voff), "s"(sbase), "s"(lds) : "memory");
}
DI void pin(float& v) { asm volatile("" : "+v"(v)); }
DI void drain_vm() { asm volatile("s_waitcnt vmcnt(0)" ::: "memory"); }
template <int N> DI void wait_vm_bar() { asm volatile("s_waitcnt vmcnt(%0)\n\ts_barrier" ::"i"(N) : "memory"); }
DI void bar_only() { SB(); asm volatile("s_barrier" ::: "memory"); SB(); }
DI void lgkm_bar() { SB(); asm volatile("s_waitcnt lgkmcnt(0)\n\ts_barrier" ::: "memory"); SB(); }

DI u64 rtime() { u64 t; asm volatile("s_memrealtime %0\n\ts_waitcnt lgkmcnt(0)" : "=s"(t)::"memory"); return t; }
DI unsigned ctime_() { u64 t; asm volatile("s_memtime %0\n\ts_waitcnt lgkmcnt(0)" : "=s"(t)::"memory"); return (unsigned)t; }

DI bf16x8 tr_frag(const LAS char* p) {
  s16x4 lo = __builtin_amdgcn_ds_read_tr16_b64_v4i16((LAS s16x4*)p);
  s16x4 hi = __builtin_amdgcn_ds_read_tr16_b64_v4i16((LAS s16x4*)(p + 1024));
  return __builtin_bit_cast(bf16x8, __builtin_shufflevector(lo, hi, 0, 1, 2, 3, 4, 5, 6, 7));
}
DI unsigned pk2(float a, float b) { f32x2 v = {a, b}; return __builtin_bit_cast(unsigned, __builtin_convertvector(v, bf16x2)); }
DI bf16x8 pack8(const f32x16& a, int s) {
  u32x4 r = {pk2(a[8 * s], a[8 * s + 1]), pk2(a[8 * s + 2], a[8 * s + 3]), pk2(a[8 * s + 4], a[8 * s + 5]), pk2(a[8 * s + 6], a[8 * s + 7])};
  return __builtin_bit_cast(bf16x8, r);
}
DI u32x4 pack8s(const f32x16& a, int s, float k) {
  float t[8];
#pragma unroll
  for (int i = 0; i < 8; ++i) t[i] = a[8 * s + i] * k;
  SB();
  return u32x4{pk2(t[0], t[1]), pk2(t[2], t[3]), pk2(t[4], t[5]), pk2(t[6], t[7])};
}
template <int I> struct IC { static constexpr int v = I; };
template <int B, int E, class F> DI void sfor(F&& f) { if constexpr (B < E) { f(IC<B>{}); sfor<B + 1, E>(f); } }
#define MFMA(a, b, c) __builtin_amdgcn_mfma_f32_32x32x16_bf16((a), (b), (c), 0, 0, 0)
#define BF(x) __builtin_bit_cast(bf16x8, x)
constexpr unsigned SPIN_CAP = 1u << 19;

DI u64 ld_flag(const GAS u64* q) { return __hip_atomic_load(q, RLX_AGENT); }
DI unsigned flag_mask(u64 v, u64 tag, unsigned all) { return (unsigned)__ballot(v == tag) & all; }
DI unsigned wait_flags(const GAS u64* q, u64 tag, unsigned all, unsigned want) {
  unsigned m = 0;
#pragma clang loop unroll(disable)
  for (unsigned spins = 0; spins < SPIN_CAP; ++spins) {
    m = flag_mask(ld_flag(q), tag, all);
    if ((m & want) == want) break;
    __builtin_amdgcn_s_sleep(1);
  }
  asm volatile("" ::: "memory");
  return m;
}
#define POLL3(TEST)                                                                                         \
  "global_load_dwordx2 %[pa], %[q], off sc1\n\ts_sleep 3\n\t"                                               \
  "global_load_dwordx2 %[pb], %[q], off sc1\n\ts_sleep 3\n\t"                                               \
  "global_load_dwordx2 %[pc], %[q], off sc1\n\t"                                                            \
  "s_mov_b32 %[n], %[cap]\n"                                                                                \
  "1:\n\t"                                                                                                  \
  "s_waitcnt vmcnt(2)\n\tv_cmp_eq_u64 vcc, %[tag], %[pa]\n\t" TEST "s_cbranch_scc1 2f\n\t"                  \
  "global_load_dwordx2 %[pa], %[q], off sc1\n\t"                                                            \
  "s_waitcnt vmcnt(2)\n\tv_cmp_eq_u64 vcc, %[tag], %[pb]\n\t" TEST "s_cbranch_scc1 2f\n\t"                  \
  "global_load_dwordx2 %[pb], %[q], off sc1\n\t"                                                            \
  "s_waitcnt vmcnt(2)\n\tv_cmp_eq_u64 vcc, %[tag], %[pc]\n\t" TEST "s_cbranch_scc1 2f\n\t"                  \
  "global_load_dwordx2 %[pc], %[q], off sc1\n\t"                                                            \
  "s_sub_u32 %[n], %[n], 1\n\ts_cmp_lg_u32 %[n], 0\n\ts_cbranch_scc1 1b\n\t"                                \
  "s_mov_b32 %[m], 1\n"                                                                                     \
  "2:\n\t"

__global__ __launch_bounds__(512, 2) void gqa_fused(const char* __restrict__ x, const char* __restrict__ kc, const char* __restrict__ vc,
                                                   const char* __restrict__ wq, const char* __restrict__ wo, char* __restrict__ out, char* __restrict__ ws) {
  __shared__ __attribute__((aligned(16))) char lds_raw[LDS_BYTES];
  const LAS char* lds = (const LAS char*)lds_raw;
  const unsigned lds0 = (unsigned)(size_t)lds;
  const int lane = threadIdx.x & 63;
  const int w8 = __builtin_amdgcn_readfirstlane(threadIdx.x >> 6);
  const int j = blockIdx.x & 7, bp = blockIdx.x >> 5, hh = (blockIdx.x >> 3) & 3, h = 4 * j + hh;

  if (w8 >= 4) {
    const int lw = w8 - 4;
    const int rot = ROT == 0 ? 0 : ROT == 1 ? (((bp ^ hh) & 1) << 2) : ROT == 2 ? (((bp + hh) & 3) << 1) : ((bp + 2 * hh + (j & 1)) & 7);
    const unsigned l4 = lane >> 4, l3 = lane >> 3;
    const unsigned vx0 = l3 * 1024 + (((lane & 7) ^ l4) << 4), vx1 = vx0 ^ 64;
    const unsigned ptr_ = (lane & 15) ^ (l4 << 2);
    const unsigned vwq = l4 * 8192 + (ptr_ << 4);
    const unsigned vv = l4 * 256 + (ptr_ << 4);
    const unsigned vwo = l4 * 1024 + ((ptr_ ^ (unsigned)(j << 1)) << 4);
    const unsigned vk = l4 * 256 + (((lane & 15) ^ l4) << 4);
    const char* xb = x + (size_t)(bp * 128 + lw * 32) * 1024;
    const char* wqb = wq + (size_t)(lw * 16) * 8192 + h * 256;
    const size_t kvoff = (size_t)((bp * 2 + (lw >> 1)) * 32 + h) * 16384 + (lw & 1) * 8192;
    const char* kb = kc + kvoff;
    const char* vb = vc + kvoff;
    const char* wob = wo + (size_t)(j * 512 + lw * 16) * 1024 + hh * 256;

    auto chunk = [&](int c) AI {
      const int cc = (c + rot) & 7;
      const unsigned slot = lds0 + cslot(c);
#pragma unroll
      for (int q = 0; q < 4; ++q) {
        dma16(wqb + (size_t)(cc * 64 + q * 4) * 8192, vwq, slot + 16384 + (lw * 16 + q * 4) * 256);
        dma16(xb + q * 8192 + cc * 128, (q & 1) ? vx1 : vx0, slot + (lw * 32 + q * 8) * 128);
      }
    };
    auto loadK = [&]() AI {
#pragma unroll
      for (int i = 0; i < 8; ++i) dma16nt(kb + i * 1024, vk ^ ((i & 3) << 6), lds0 + L_K + lw * 8192 + i * 1024);
    };
    auto loadV = [&]() AI {
      dma4(wob + (size_t)(bp * 64) * 1024, ((lane & 31) >> 1) * 1024 + (lane & 1) * 128, lds0 + L_V + lw * 8192);
#pragma unroll
      for (int i = 0; i < 8; ++i) dma16nt(vb + i * 1024, vv, lds0 + L_V + lw * 8192 + i * 1024);
    };
    auto loadWo = [&](int g) AI {
      const char* b = wob + (size_t)((hh ^ g) * 128) * 1024;
#pragma unroll
      for (int q = 0; q < 8; ++q)
        dma16(b + (size_t)((q >> 2) * 64 + (q & 3) * 4) * 1024, vwo, lds0 + ((g + 1) & 3) * 32768 + ((q >> 2) * 64 + lw * 16 + (q & 3) * 4) * 256);
    };
    chunk(0); chunk(1); chunk(2);
    wait_vm_bar<8>(); chunk(3); chunk(4);
    wait_vm_bar<0>(); loadK(); chunk(5);
    wait_vm_bar<0>(); chunk(6); chunk(7);
    wait_vm_bar<8>(); loadV();
    wait_vm_bar<9>(); loadWo(0); loadWo(1);
    wait_vm_bar<8>(); loadWo(2); loadWo(3);
    wait_vm_bar<0>();
    return;
  }

  const int w = w8;
  const int r = lane & 31, hf = lane >> 5;
  const int grp = bp * 4 + w;
  const int me = (grp * 8 + j) * 4 + hh;
  const u64 ep = __hip_atomic_load((const GAS u64*)(ws + WS_EPOCH + grp * 128), RLX_AGENT);
  const u64 tag = (((u64)__builtin_amdgcn_readfirstlane((unsigned)(ep >> 32)) << 32) | (unsigned)__builtin_amdgcn_readfirstlane((unsigned)ep)) + 1;
  const u32x4 wsd = {(unsigned)(size_t)ws, (unsigned)((size_t)ws >> 32), 16u << 20, 0x00020000u};
  const __amdgpu_buffer_rsrc_t wsr = __builtin_amdgcn_make_buffer_rsrc(ws, 0, 16u << 20, 0x00020000);
  const GAS u64* myflag = (const GAS u64*)(ws + WS_FLAG + (size_t)me * 256);
  { u64 warm = ld_flag(myflag + (lane & 1) * 16); asm volatile("" ::"v"(warm)); }

  const unsigned q4 = (lane & 15) >> 2, p4 = lane & 3, blk = (lane >> 4) & 1;
  const unsigned trc = (8 * hf + q4) * 256 + 32 * blk + 8 * (((p4 & 1) << 1) | (p4 >> 1));
  unsigned trb[4];
#pragma unroll
  for (int c = 0; c < 4; ++c) trb[c] = trc + ((c ^ q4) << 6);
  const unsigned xsw = (r >> 1) & 7;
  unsigned xfb[4];
#pragma unroll
  for (int s = 0; s < 4; ++s) xfb[s] = (w * 32 + r) * 128 + (((2 * s + hf) ^ xsw) << 4);
  const int kr = (r & 19) | ((r & 4) << 1) | ((r & 8) >> 1);
  const LAS char* kp = lds + L_K + (w >> 1) * 16384 + kr * 256;

  f32x16 acc[4], sc[2];
#pragma unroll
  for (int c = 0; c < 4; ++c)
#pragma unroll
    for (int i = 0; i < 16; ++i) acc[c][i] = 0.f;
#pragma unroll
  for (int t = 0; t < 2; ++t)
#pragma unroll
    for (int i = 0; i < 16; ++i) sc[t][i] = 0.f;

  bf16x8 A[8];
  bf16x8 f[8];
  bf16x8 kf[16];
  auto s_t = [](int m) AI { return (m < 6 || m == 8 || m == 9) ? 0 : 1; };
  auto s_s = [](int m) AI { return m < 6 ? m : m < 8 ? m - 6 : m < 10 ? m - 2 : m - 8; };
  auto rdk = [&](int i) AI { kf[i] = *(const LAS bf16x8*)(kp + s_t(i) * 8192 + (((2 * s_s(i) + hf) ^ (kr & 15)) << 4)); };
  {
    bf16x8 B[8];
    auto stp = [](int m) AI { return m < 112 ? (m >> 2) & 3 : m & 3; };
    auto til = [](int m) AI { return m < 112 ? m & 3 : (m >> 2) & 3; };
    auto rd = [&](int m) AI {
      const int c = m >> 4, s = stp(m), t = til(m);
      if (t == 0) B[(c & 1) * 4 + s] = *(const LAS bf16x8*)(lds + cslot(c) + xfb[s]);
      A[m & 7] = tr_frag(lds + cslot(c) + 16384 + s * 4096 + trb[t]);
    };
    bar_only();
    STAMP(1);
#pragma unroll
    for (int m = 0; m < 6; ++m) { rd(m); SB(); }
    sfor<0, 128>([&](auto ic) AI {
      constexpr int m = decltype(ic)::v;
      acc[til(m)] = MFMA(A[m & 7], B[((m >> 4) & 1) * 4 + stp(m)], acc[til(m)]);
      SB();
      constexpr int k = m & 15, c = m >> 4;
      if constexpr (c == 1 || c == 4 || c == 5 || c == 6) {
        if (k < 10) rd(m + 6);
        else if (k == 12) { lgkm_bar(); rd(m + 4); rd(m + 5); }
        else if (k == 13) { rd(m + 5); rd(m + 6); }
        else if (k > 13) rd(m + 6);
      } else {
        if (m + 6 < 128) rd(m + 6);
        if constexpr (c == 7) {
          if constexpr (k >= 10) { rdk(2 * k - 20); rdk(2 * k - 19); }
          if constexpr (k >= 5 && (k & 1)) f[(k - 5) >> 1] = pack8(acc[(k - 5) >> 2], ((k - 5) >> 1) & 1);
        }
      }
      SB();
    });
  }
  STAMP(2);
  constexpr float C = 0.08838834764831845f * 1.4426950408889634f;
  float m0, sum0 = 0.f;
  sfor<0, 16>([&](auto ic) AI {
    constexpr int m = decltype(ic)::v;
    sc[s_t(m)] = MFMA(kf[m], f[s_s(m)], sc[s_t(m)]);
    SB();
    if constexpr (m < 4) rdk(12 + m);
    if constexpr (m == 1) f[6] = pack8(acc[3], 0);
    if constexpr (m == 2) f[7] = pack8(acc[3], 1);
    if constexpr (m == 11) {
      m0 = sc[0][0];
#pragma unroll
      for (int i = 1; i < 16; ++i) m0 = fmaxf(m0, sc[0][i]);
      m0 *= C;
    }
    if constexpr (m >= 12) {
#pragma unroll
      for (int i = 4 * (m - 12); i < 4 * (m - 11); ++i) sc[0][i] = __builtin_amdgcn_exp2f(fmaf(sc[0][i], C, -m0));
    }
    SB();
  });
#pragma unroll
  for (int i = 0; i < 16; ++i) sum0 += sc[0][i];
  pin(sum0);
  STAMP(3);
  float inv;
  {
    float mx = sc[1][0];
#pragma unroll
    for (int i = 1; i < 16; ++i) mx = fmaxf(mx, sc[1][i]);
    mx = fmaxf(mx * C, m0);
    auto rr = __builtin_amdgcn_permlane32_swap(__float_as_uint(mx), __float_as_uint(mx), false, false);
    mx = fmaxf(__uint_as_float(rr[0]), __uint_as_float(rr[1]));
    const float a0 = __builtin_amdgcn_exp2f(m0 - mx);
    float sum = sum0 * a0;
#pragma unroll
    for (int i = 0; i < 16; ++i) sc[1][i] = __builtin_amdgcn_exp2f(fmaf(sc[1][i], C, -mx));
    f[0] = BF(pack8s(sc[0], 0, a0));
    f[1] = BF(pack8s(sc[0], 1, a0));
#pragma unroll
    for (int i = 0; i < 16; ++i) sum += sc[1][i];
    f[2] = pack8(sc[1], 0);
    f[3] = pack8(sc[1], 1);
    auto r2 = __builtin_amdgcn_permlane32_swap(__float_as_uint(sum), __float_as_uint(sum), false, false);
    inv = __builtin_amdgcn_rcpf(__uint_as_float(r2[0]) + __uint_as_float(r2[1]));
  }
  asm volatile("" : "+v"(f[0]), "+v"(f[1]), "+v"(f[2]), "+v"(f[3]), "+v"(inv));
  STAMP(4);
  bar_only();
  STAMP(5);
#pragma unroll
  for (int c = 0; c < 4; ++c)
#pragma unroll
    for (int i = 0; i < 16; ++i) acc[c][i] = 0.f;
  u32x4 of[4][8];
  {
    const LAS char* vp = lds + L_V + (w >> 1) * 16384;
    auto rd = [&](int m) AI {
      if (m < 16) A[m & 7] = tr_frag(vp + (m & 3) * 4096 + trb[m >> 2]);
      else A[m & 7] = tr_frag(lds + 32768 + ((m - 16) >> 2) * 4096 + trb[m & 3]);
    };
    auto put = [&](int s) AI {
      of[0][s] = pack8s(acc[s >> 1], s & 1, inv);
      __builtin_amdgcn_raw_buffer_store_b128(of[0][s], wsr, lane * 16 + s * 1024, me * 8192, 16);
    };
#pragma unroll
    for (int m = 0; m < 7; ++m) { rd(m); SB(); }
    sfor<0, 16>([&](auto ic) AI {
      constexpr int m = decltype(ic)::v;
      acc[m >> 2] = MFMA(A[m & 7], f[m & 3], acc[m >> 2]);
      SB();
      rd(m + 7);
      if constexpr (m >= 5 && (m & 1)) put((m - 5) >> 1);
      SB();
    });
    put(6); SB(); put(7); SB();
  }
  STAMP(6);
#pragma unroll
  for (int c = 0; c < 4; ++c)
#pragma unroll
    for (int i = 0; i < 16; ++i) acc[c][i] = 0.f;
  sfor<0, 32>([&](auto ic) AI {
    constexpr int m = decltype(ic)::v;
    acc[m & 3] = MFMA(A[m & 7], BF(of[0][m >> 2]), acc[m & 3]);
    SB();
    if constexpr (m + 7 < 32) A[(m + 7) & 7] = tr_frag(lds + 32768 + ((m + 7) >> 2) * 4096 + trb[(m + 7) & 3]);
    if constexpr (m == DRAIN1) {
      drain_vm();
      if (lane >= 1 && lane < 4)
        __hip_atomic_store((GAS u64*)(ws + WS_FLAG + (size_t)(me ^ lane) * 256) + (lane - 1), tag, RLX_AGENT);
    }
    SB();
  });
  STAMP(7);
  bar_only();
  const GAS u64* fq = myflag + (lane < 3 ? lane : 0);
  auto fetch = [&](int k, int g, int s) AI { of[k][s] = __builtin_amdgcn_raw_buffer_load_b128(wsr, lane * 16 + s * 1024, (me ^ g) * 8192, 16); };
  int g1;
  u64 fl, pa, pb, pc;
  const unsigned vo0 = lane * 16, vo1 = lane * 16 + 4096;
  {
    unsigned n_, m_;
    asm volatile(POLL3("s_and_b32 %[m], vcc_lo, 7\n\t")
                 "s_ff1_i32_b32 %[g], %[m]\n\ts_add_u32 %[g], %[g], 1\n\ts_xor_b32 %[n], %[me], %[g]\n\ts_lshl_b32 %[n], %[n], 13\n\t"
                 "buffer_load_dwordx4 %[f0], %[v0], %[rs], %[n] offen sc1\n\t"
                 "buffer_load_dwordx4 %[f1], %[v0], %[rs], %[n] offen offset:1024 sc1\n\t"
                 "buffer_load_dwordx4 %[f2], %[v0], %[rs], %[n] offen offset:2048 sc1\n\t"
                 "buffer_load_dwordx4 %[f3], %[v0], %[rs], %[n] offen offset:3072 sc1\n\t"
                 "buffer_load_dwordx4 %[f4], %[v1], %[rs], %[n] offen sc1\n\t"
                 "buffer_load_dwordx4 %[f5], %[v1], %[rs], %[n] offen offset:1024 sc1\n\t"
                 "buffer_load_dwordx4 %[f6], %[v1], %[rs], %[n] offen offset:2048 sc1\n\t"
                 "buffer_load_dwordx4 %[f7], %[v1], %[rs], %[n] offen offset:3072 sc1\n\t"
                 "global_load_dwordx2 %[fl], %[q], off sc1"
                 : [f0] "=&v"(of[1][0]), [f1] "=&v"(of[1][1]), [f2] "=&v"(of[1][2]), [f3] "=&v"(of[1][3]), [f4] "=&v"(of[1][4]), [f5] "=&v"(of[1][5]),
                   [f6] "=&v"(of[1][6]), [f7] "=&v"(of[1][7]), [fl] "=&v"(fl), [pa] "=&v"(pa), [pb] "=&v"(pb), [pc] "=&v"(pc), [g] "=&s"(g1),
                   [n] "=&s"(n_), [m] "=&s"(m_)
                 : [q] "v"(fq), [tag] "s"(tag), [cap] "i"(SPIN_CAP), [me] "s"(me), [v0] "v"(vo0), [v1] "v"(vo1), [rs] "s"(wsd)
                 : "vcc", "scc", "memory");
  }
  SB();
  STAMP(8);
  unsigned tb[3][4];
#pragma unroll
  for (int t = 0; t < 4; ++t) tb[0][t] = trb[t] + ((g1 + 1) & 3) * 32768;
  auto rd2 = [&](int m) AI {
    if (m < 64) A[m & 7] = tr_frag(lds + ((m >> 2) & 7) * 4096 + tb[m >> 5][m & 3]);
    else A[m & 7] = tr_frag(lds + (m & 7) * 4096 + tb[2][3 - ((m - 64) >> 3)]);
  };
#pragma unroll
  for (int m = 0; m < 7; ++m) { rd2(m); SB(); }
  asm volatile("s_waitcnt vmcnt(0)"
               : "+v"(of[1][0]), "+v"(of[1][1]), "+v"(of[1][2]), "+v"(of[1][3]), "+v"(of[1][4]), "+v"(of[1][5]), "+v"(of[1][6]), "+v"(of[1][7]),
                 "+v"(fl), "+v"(pa), "+v"(pb), "+v"(pc)::"memory");
  const unsigned rem = 7u & ~(1u << (g1 - 1));
  unsigned seen = flag_mask(fl, tag, 7);
  const int g2 = __builtin_ctz((seen & rem) ? (seen & rem) : rem) + 1, g3 = 6 - g1 - g2;
#pragma unroll
  for (int t = 0; t < 4; ++t) { tb[1][t] = trb[t] + ((g2 + 1) & 3) * 32768; tb[2][t] = trb[t] + ((g3 + 1) & 3) * 32768; }
  SB();
  sfor<0, 96>([&](auto ic) AI {
    constexpr int m = decltype(ic)::v;
    if constexpr (m < 64) acc[m & 3] = MFMA(A[m & 7], BF(of[1 + (m >> 5)][(m >> 2) & 7]), acc[m & 3]);
    else acc[3 - ((m - 64) >> 3)] = MFMA(A[m & 7], BF(of[3][m & 7]), acc[3 - ((m - 64) >> 3)]);
    SB();
    if constexpr (m + 7 < 96) rd2(m + 7);
    if constexpr (m < 16 && (m & 1) == 0) fetch(2, g2, m >> 1);
    if constexpr (m == 18) fl = ld_flag(fq);
    if constexpr (m == 31) {
      const bool ok2 = (seen >> (g2 - 1)) & 1;
      seen = flag_mask(fl, tag, 7);
      if (__builtin_expect(!ok2, 0)) {
        seen = wait_flags(fq, tag, 7, 1u << (g2 - 1));
#pragma unroll
        for (int s = 0; s < 8; ++s) fetch(2, g2, s);
      }
      asm volatile("" ::: "memory");
    }
    if constexpr (m >= 32 && m < 48 && (m & 1) == 0) fetch(3, g3, (m - 32) >> 1);
    if constexpr (m == 63) {
      if (__builtin_expect(!((seen >> (g3 - 1)) & 1), 0)) {
        wait_flags(fq, tag, 7, 1u << (g3 - 1));
#pragma unroll
        for (int s = 0; s < 8; ++s) fetch(3, g3, s);
      }
    }
    if constexpr (m >= 72 && ((m & 7) == 1 || (m & 7) == 3)) {
      constexpr int d = 2 * (3 - ((m - 72) >> 3)) + ((m & 7) == 1 ? 1 : 0);
      __builtin_amdgcn_raw_buffer_store_b128(__builtin_bit_cast(u32x4, pack8(acc[d >> 1], d & 1)), wsr, lane * 16 + d * 1024,
                                             (unsigned)WS_P + (me ^ (d << 2)) * 8192, 16);
    }
    SB();
  });
  __builtin_amdgcn_raw_buffer_store_b128(__builtin_bit_cast(u32x4, pack8(acc[0], 1)), wsr, lane * 16 + 1024, (unsigned)WS_P + (me ^ 4) * 8192, 16);
  STAMP(9);
  drain_vm();
  if (lane >= 1 && lane < 8)
    __hip_atomic_store((GAS u64*)(ws + WS_FLAG + (size_t)(me ^ (lane << 2)) * 256) + 16 + (lane - 1), tag, RLX_AGENT);
  STAMP(10);
  u32x4 pv[7];
  {
    unsigned n_, m_;
    asm volatile(POLL3("s_cmp_eq_u64 vcc, -1\n\t")
                 "buffer_load_dwordx4 %[f1], %[v0], %[rs], %[so] offen offset:1024 sc1\n\t"
                 "buffer_load_dwordx4 %[f2], %[v0], %[rs], %[so] offen offset:2048 sc1\n\t"
                 "buffer_load_dwordx4 %[f3], %[v0], %[rs], %[so] offen offset:3072 sc1\n\t"
                 "buffer_load_dwordx4 %[f4], %[v1], %[rs], %[so] offen sc1\n\t"
                 "buffer_load_dwordx4 %[f5], %[v1], %[rs], %[so] offen offset:1024 sc1\n\t"
                 "buffer_load_dwordx4 %[f6], %[v1], %[rs], %[so] offen offset:2048 sc1\n\t"
                 "buffer_load_dwordx4 %[f7], %[v1], %[rs], %[so] offen offset:3072 sc1\n\t"
                 "s_waitcnt vmcnt(0)"
                 : [f1] "=&v"(pv[0]), [f2] "=&v"(pv[1]), [f3] "=&v"(pv[2]), [f4] "=&v"(pv[3]), [f5] "=&v"(pv[4]), [f6] "=&v"(pv[5]), [f7] "=&v"(pv[6]),
                   [pa] "=&v"(pa), [pb] "=&v"(pb), [pc] "=&v"(pc), [n] "=&s"(n_), [m] "=&s"(m_)
                 : [q] "v"(myflag + 16 + (lane < 7 ? lane : 0)), [tag] "s"(tag), [cap] "i"(SPIN_CAP), [so] "s"((unsigned)WS_P + me * 8192), [v0] "v"(vo0),
                   [v1] "v"(vo1), [rs] "s"(wsd)
                 : "vcc", "scc", "memory");
  }
  float o[8];
#pragma unroll
  for (int i = 0; i < 8; ++i) o[i] = acc[0][i];
#pragma unroll
  for (int d = 0; d < 7; ++d)
#pragma unroll
    for (int i = 0; i < 4; ++i) {
      o[2 * i] += __uint_as_float(pv[d][i] << 16);
      o[2 * i + 1] += __uint_as_float(pv[d][i] & 0xffff0000u);
    }
  u32x4 ov = {pk2(o[0], o[1]), pk2(o[2], o[3]), pk2(o[4], o[5]), pk2(o[6], o[7])};
  *(GAS u32x4*)(out + (size_t)(bp * 128 + w * 32 + r) * 1024 + hh * 256 + j * 32 + hf * 16) = ov;
  if (j == 0 && hh == 0 && lane == 0) __hip_atomic_store((GAS u64*)(ws + WS_EPOCH + grp * 128), tag, RLX_AGENT);
}

extern "C" void kernel_launch(void* const* d_in, const int* in_sizes, int n_in, void* d_out, int out_size, void* d_ws, size_t ws_size,
                              hipStream_t stream) {
  gqa_fused<<<256, 512, 0, stream>>>((const char*)d_in[0], (const char*)d_in[1], (const char*)d_in[2], (const char*)d_in[3],
                                     (const char*)d_in[4], (char*)d_out, (char*)d_ws);
}
```

```cpp
#include <hip/hip_runtime.h>
#include <hip/hip_bf16.h>

typedef unsigned short u16;
typedef unsigned long long u64;
typedef short s16x4 __attribute__((ext_vector_type(4)));
typedef short s16x8 __attribute__((ext_vector_type(8)));
typedef __bf16 bf16x8 __attribute__((ext_vector_type(8)));
typedef __bf16 bf16x2 __attribute__((ext_vector_type(2)));
typedef float f32x2 __attribute__((ext_vector_type(2)));
typedef float f32x16 __attribute__((ext_vector_type(16)));
typedef unsigned u32x4 __attribute__((ext_vector_type(4)));
#define LAS __attribute__((address_space(3)))
#define GAS __attribute__((address_space(1)))
#define DI __device__ __forceinline__
#define RLX_AGENT __ATOMIC_RELAXED, __HIP_MEMORY_SCOPE_AGENT
#define SB() __builtin_amdgcn_sched_barrier(0)
#define AI __attribute__((always_inline))

#define STAMPS 0
#define ROT 3
#define STG 0
#define WOP 1
#define DRAIN1 16
#define STAMP(i) do { } while (0)

constexpr size_t WS_O     = 0;
constexpr size_t WS_P     = 8u << 20;
constexpr size_t WS_FLAG  = 16u << 20;
constexpr size_t WS_EPOCH = WS_FLAG + (256u << 10);
constexpr size_t WS_STAMP = 32u << 20;

constexpr unsigned L_K = 3u * 32768, L_V = 4u * 32768;
constexpr unsigned LDS_BYTES = 160u << 10;

DI void dma16(const char* sbase, unsigned voff, unsigned lds) {
  asm volatile("s_mov_b32 m0, %2\n\ts_nop 0\n\tglobal_load_lds_dwordx4 %0, %1" ::"v"(voff), "s"(sbase), "s"(lds) : "memory");
}
DI void dma16nt(const char* sbase, unsigned voff, unsigned lds) {
  asm volatile("s_mov_b32 m0, %2\n\ts_nop 0\n\tglobal_load_lds_dwordx4 %0, %1 nt" ::"v"(voff), "s"(sbase), "s"(lds) : "memory");
}
DI void dma4(const char* sbase, unsigned voff, unsigned lds) {
  asm volatile("s_mov_b32 m0, %2\n\ts_nop 0\n\tglobal_load_lds_dword %0, %1" ::"v"(voff), "s"(sbase), "s"(lds) : "memory");
}
DI void pin(float& v) { asm volatile("" : "+v"(v)); }
DI void drain_vm() { asm volatile("s_waitcnt vmcnt(0)" ::: "memory"); }
template <int N> DI void wait_vm_bar() { asm volatile("s_waitcnt vmcnt(%0)\n\ts_barrier" ::"i"(N) : "memory"); }
DI void bar_only() { SB(); asm volatile("s_barrier" ::: "memory"); SB(); }
DI void lgkm_bar() { SB(); asm volatile("s_waitcnt lgkmcnt(0)\n\ts_barrier" ::: "memory"); SB(); }

DI u64 rtime() { u64 t; asm volatile("s_memrealtime %0\n\ts_waitcnt lgkmcnt(0)" : "=s"(t)::"memory"); return t; }
DI unsigned ctime_() { u64 t; asm volatile("s_memtime %0\n\ts_waitcnt lgkmcnt(0)" : "=s"(t)::"memory"); return (unsigned)t; }

DI bf16x8 tr_frag(const LAS char* p) {
  s16x4 lo = __builtin_amdgcn_ds_read_tr16_b64_v4i16((LAS s16x4*)p);
  s16x4 hi = __builtin_amdgcn_ds_read_tr16_b64_v4i16((LAS s16x4*)(p + 1024));
  return __builtin_bit_cast(bf16x8, __builtin_shufflevector(lo, hi, 0, 1, 2, 3, 4, 5, 6, 7));
}
DI unsigned pk2(float a, float b) { f32x2 v = {a, b}; return __builtin_bit_cast(unsigned, __builtin_convertvector(v, bf16x2)); }
DI bf16x8 pack8(const f32x16& a, int s) {
  u32x4 r = {pk2(a[8 * s], a[8 * s + 1]), pk2(a[8 * s + 2], a[8 * s + 3]), pk2(a[8 * s + 4], a[8 * s + 5]), pk2(a[8 * s + 6], a[8 * s + 7])};
  return __builtin_bit_cast(bf16x8, r);
}
DI u32x4 not_ones(u32x4 v) {
  return u32x4{min(v[0], 0xfffffffeu), min(v[1], 0xfffffffeu), min(v[2], 0xfffffffeu), min(v[3], 0xfffffffeu)};
}
DI u32x4 pack8s(const f32x16& a, int s, float k) {
  float t[8];
#pragma unroll
  for (int i = 0; i < 8; ++i) t[i] = a[8 * s + i] * k;
  SB();
  return u32x4{pk2(t[0], t[1]), pk2(t[2], t[3]), pk2(t[4], t[5]), pk2(t[6], t[7])};
}
DI unsigned umax4(unsigned m, u32x4 v) { return max(max(max(m, v[0]), v[1]), max(v[2], v[3])); }
template <int I> struct IC { static constexpr int v = I; };
template <int B, int E, class F> DI void sfor(F&& f) { if constexpr (B < E) { f(IC<B>{}); sfor<B + 1, E>(f); } }
#define MFMA(a, b, c) __builtin_amdgcn_mfma_f32_32x32x16_bf16((a), (b), (c), 0, 0, 0)
#define BF(x) __builtin_bit_cast(bf16x8, x)
constexpr unsigned SPIN_CAP = 1u << 20;

DI void wait_flags(const GAS u64* p, int lane, int n, u64 tag) {
  const GAS u64* q = p + (lane < n ? lane : 0);
#pragma clang loop unroll(disable)
  for (unsigned spins = 0; spins < SPIN_CAP; ++spins) {
    u64 v = __hip_atomic_load(q, RLX_AGENT);
    if (__all(v == tag)) break;
    __builtin_amdgcn_s_sleep(1);
  }
  asm volatile("" ::: "memory");
}

__global__ __launch_bounds__(512, 2) void gqa_fused(const char* __restrict__ x, const char* __restrict__ kc, const char* __restrict__ vc,
                                                   const char* __restrict__ wq, const char* __restrict__ wo, char* __restrict__ out, char* __restrict__ ws) {
  __shared__ __attribute__((aligned(16))) char lds_raw[LDS_BYTES];
  const LAS char* lds = (const LAS char*)lds_raw;
  const unsigned lds0 = (unsigned)(size_t)lds;
  const int lane = threadIdx.x & 63;
  const int w8 = __builtin_amdgcn_readfirstlane(threadIdx.x >> 6);
  const int j = blockIdx.x & 7, bp = blockIdx.x >> 5, hh = (blockIdx.x >> 3) & 3, h = 4 * j + hh;

  const bool early = STG == 0 ? false : STG == 1 ? (bp & 1) : STG == 2 ? ((bp ^ hh) & 1) : true;
  if (w8 >= 4) {
    const int lw = w8 - 4;
    const int rot = ROT == 0 ? 0 : ROT == 1 ? (((bp ^ hh) & 1) << 2) : ROT == 2 ? (((bp + hh) & 3) << 1) : ((bp + 2 * hh + (j & 1)) & 7);
    const unsigned l4 = lane >> 4, l3 = lane >> 3;
    const unsigned vx0 = l3 * 1024 + (((lane & 7) ^ l4) << 4), vx1 = vx0 ^ 64;
    const unsigned ptr_ = (lane & 15) ^ (l4 << 2);
    const unsigned vwq = l4 * 8192 + (ptr_ << 4);
    const unsigned vv = l4 * 256 + (ptr_ << 4);
    const unsigned vwo = l4 * 1024 + ((ptr_ ^ (unsigned)(j << 1)) << 4);
    const unsigned vk = l4 * 256 + (((lane & 15) ^ l4) << 4);
    const char* xb = x + (size_t)(bp * 128 + lw * 32) * 1024;
    const char* wqb = wq + (size_t)(lw * 16) * 8192 + h * 256;
    const size_t kvoff = (size_t)((bp * 2 + (lw >> 1)) * 32 + h) * 16384 + (lw & 1) * 8192;
    const char* kb = kc + kvoff;
    const char* vb = vc + kvoff;
    const char* wob = wo + (size_t)(j * 512 + lw * 16) * 1024 + hh * 256;

    auto chunk = [&](int c, int sl) AI {
      const int cc = (c + rot) & 7;
      const unsigned slot = lds0 + sl * 32768;
#pragma unroll
      for (int q = 0; q < 4; ++q) {
        dma16(wqb + (size_t)(cc * 64 + q * 4) * 8192, vwq, slot + 16384 + (lw * 16 + q * 4) * 256);
        dma16(xb + q * 8192 + cc * 128, (q & 1) ? vx1 : vx0, slot + (lw * 32 + q * 8) * 128);
      }
    };
    auto loadK = [&]() AI {
      dma4(wob + (size_t)(bp * 64) * 1024, ((lane & 31) >> 1) * 1024 + (lane & 1) * 128, lds0 + L_K + lw * 8192);
#pragma unroll
      for (int i = 0; i < 8; ++i) dma16nt(kb + i * 1024, vk ^ ((i & 3) << 6), lds0 + L_K + lw * 8192 + i * 1024);
    };
    auto loadV = [&]() AI {
#pragma unroll
      for (int i = 0; i < 8; ++i) dma16nt(vb + i * 1024, vv, lds0 + L_V + lw * 8192 + i * 1024);
    };
    auto loadWo = [&](int g) AI {
      const char* b = wob + (size_t)((hh ^ g) * 128) * 1024;
#pragma unroll
      for (int q = 0; q < 8; ++q)
        dma16(b + (size_t)((q >> 2) * 64 + (q & 3) * 4) * 1024, vwo, lds0 + g * 32768 + ((q >> 2) * 64 + lw * 16 + (q & 3) * 4) * 256);
    };
    chunk(0, 0); chunk(1, 1);
    wait_vm_bar<8>(); chunk(2, 2);
    if (early) {
      wait_vm_bar<0>(); loadK(); loadV(); chunk(3, 0);
      wait_vm_bar<0>(); chunk(4, 1); chunk(5, 2);
      wait_vm_bar<8>(); chunk(6, 0);
      wait_vm_bar<8>(); chunk(7, 1);
      wait_vm_bar<8>(); loadWo(2);
      wait_vm_bar<8>(); loadWo(0);
      wait_vm_bar<63>(); loadWo(1);
      wait_vm_bar<63>(); if (!WOP) loadWo(3);
      wait_vm_bar<WOP ? 0 : 8>(); if (WOP) loadWo(3);
    } else {
      wait_vm_bar<8>(); chunk(3, 3); chunk(4, 4);
      wait_vm_bar<0>(); chunk(5, 0); chunk(6, 1);
      wait_vm_bar<63>(); chunk(7, 2);
      wait_vm_bar<63>(); loadK();
      wait_vm_bar<9>(); loadV();
      wait_vm_bar<8>(); loadWo(0);
      wait_vm_bar<8>(); if (!WOP) { loadWo(1); loadWo(2); }
      wait_vm_bar<WOP ? 0 : 16>(); if (WOP) { loadWo(1); loadWo(2); } loadWo(3);
    }
    wait_vm_bar<0>();
    return;
  }

  const int w = w8;
  const int r = lane & 31, hf = lane >> 5;
  const int grp = bp * 4 + w;
  const int me = (grp * 8 + j) * 4 + hh;
  const u64 tag = __hip_atomic_load((const GAS u64*)(ws + WS_EPOCH + grp * 128), RLX_AGENT) + 1;
  const __amdgpu_buffer_rsrc_t wsr = __builtin_amdgcn_make_buffer_rsrc(ws, 0, 16u << 20, 0x00020000);
  const GAS u64* myflag = (const GAS u64*)(ws + WS_FLAG + (size_t)me * 256);
  { u64 warm = __hip_atomic_load(myflag + (lane & 1) * 16, RLX_AGENT); asm volatile("" ::"v"(warm)); }

  const unsigned q4 = (lane & 15) >> 2, p4 = lane & 3, blk = (lane >> 4) & 1;
  const unsigned trc = (8 * hf + q4) * 256 + 32 * blk + 8 * (((p4 & 1) << 1) | (p4 >> 1));
  unsigned trb[4];
#pragma unroll
  for (int c = 0; c < 4; ++c) trb[c] = trc + ((c ^ q4) << 6);
  const unsigned xsw = (r >> 1) & 7;
  unsigned xfb[4];
#pragma unroll
  for (int s = 0; s < 4; ++s) xfb[s] = (w * 32 + r) * 128 + (((2 * s + hf) ^ xsw) << 4);
  const int kr = (r & 19) | ((r & 4) << 1) | ((r & 8) >> 1);
  const LAS char* kp = lds + L_K + (w >> 1) * 16384 + kr * 256;

  f32x16 acc[4], sc[2];
#pragma unroll
  for (int c = 0; c < 4; ++c)
#pragma unroll
    for (int i = 0; i < 16; ++i) acc[c][i] = 0.f;
#pragma unroll
  for (int t = 0; t < 2; ++t)
#pragma unroll
    for (int i = 0; i < 16; ++i) sc[t][i] = 0.f;

  bf16x8 A[8];
  bf16x8 f[8];
  bf16x8 kf[16];
  {
    bf16x8 B[8];
    auto stp = [](int m) AI { return m < 112 ? (m >> 2) & 3 : m & 3; };
    auto til = [](int m) AI { return m < 112 ? m & 3 : (m >> 2) & 3; };
    auto rd = [&](int m) AI {
      const int c = m >> 4, s = stp(m), t = til(m);
      const unsigned slot = c < 3 ? c * 32768u : early ? (c % 3) * 32768u : (c % 5) * 32768u;
      if (t == 0) B[(c & 1) * 4 + s] = *(const LAS bf16x8*)(lds + slot + xfb[s]);
      A[m & 7] = tr_frag(lds + slot + 16384 + s * 4096 + trb[t]);
    };
    bar_only();
    STAMP(1);
#pragma unroll
    for (int m = 0; m < 6; ++m) rd(m);
    SB();
    sfor<0, 128>([&](auto ic) AI {
      constexpr int m = decltype(ic)::v;
      acc[til(m)] = MFMA(A[m & 7], B[((m >> 4) & 1) * 4 + stp(m)], acc[til(m)]);
      SB();
      constexpr int k = m & 15;
      if constexpr (m < 112) {
        if (k < 10) rd(m + 6);
        else if (k == 12) {
          if ((m >> 4) == 1 ? !early : (m >> 4) >= 5 ? early : true) lgkm_bar();
          SB();
          rd(m + 4); rd(m + 5);
        }
        else if (k == 13) { rd(m + 5); rd(m + 6); }
        else if (k > 13) rd(m + 6);
      } else {
        if (m + 6 < 128) rd(m + 6);
        if constexpr (m >= 112 && k >= 5 && (k & 1)) f[(k - 5) >> 1] = pack8(acc[(k - 5) >> 2], ((k - 5) >> 1) & 1);
      }
      SB();
    });
  }
  STAMP(2);
  const LAS char* vp = lds + L_V + (w >> 1) * 16384;
  f[6] = pack8(acc[3], 0);
  f[7] = pack8(acc[3], 1);
  lgkm_bar();
  STAMP(3);
  constexpr float C = 0.08838834764831845f * 1.4426950408889634f;
  float m0, sum0 = 0.f;
  {
    auto rdk = [&](int i) AI { kf[i] = *(const LAS bf16x8*)(kp + (i >> 3) * 8192 + (((2 * (i & 7) + hf) ^ (kr & 15)) << 4)); };
#pragma unroll
    for (int i = 0; i < 12; ++i) { rdk(i); SB(); }
    sfor<0, 16>([&](auto ic) AI {
      constexpr int m = decltype(ic)::v;
      sc[m >> 3] = MFMA(kf[m], f[m & 7], sc[m >> 3]);
      SB();
      if constexpr (m < 4) rdk(12 + m);
      if constexpr (m == 9) {
        m0 = sc[0][0];
#pragma unroll
        for (int i = 1; i < 16; ++i) m0 = fmaxf(m0, sc[0][i]);
        m0 *= C;
      }
      if constexpr (m >= 10) {
#pragma unroll
        for (int i = (m - 10) * 8 / 3; i < (m - 9) * 8 / 3; ++i) sc[0][i] = __builtin_amdgcn_exp2f(fmaf(sc[0][i], C, -m0));
      }
      SB();
    });
  }
#pragma unroll
  for (int i = 0; i < 16; ++i) sum0 += sc[0][i];
  pin(sum0);
  float inv;
  {
    float mx = sc[1][0];
#pragma unroll
    for (int i = 1; i < 16; ++i) mx = fmaxf(mx, sc[1][i]);
    mx = fmaxf(mx * C, m0);
    auto rr = __builtin_amdgcn_permlane32_swap(__float_as_uint(mx), __float_as_uint(mx), false, false);
    mx = fmaxf(__uint_as_float(rr[0]), __uint_as_float(rr[1]));
    const float a0 = __builtin_amdgcn_exp2f(m0 - mx);
    float sum = sum0 * a0;
#pragma unroll
    for (int i = 0; i < 16; ++i) sc[1][i] = __builtin_amdgcn_exp2f(fmaf(sc[1][i], C, -mx));
    f[0] = BF(pack8s(sc[0], 0, a0));
    f[1] = BF(pack8s(sc[0], 1, a0));
#pragma unroll
    for (int i = 0; i < 16; ++i) sum += sc[1][i];
    f[2] = pack8(sc[1], 0);
    f[3] = pack8(sc[1], 1);
    auto r2 = __builtin_amdgcn_permlane32_swap(__float_as_uint(sum), __float_as_uint(sum), false, false);
    inv = __builtin_amdgcn_rcpf(__uint_as_float(r2[0]) + __uint_as_float(r2[1]));
  }
  asm volatile("" : "+v"(f[0]), "+v"(f[1]), "+v"(f[2]), "+v"(f[3]), "+v"(inv));
  STAMP(4);
  bar_only();
  STAMP(5);
#pragma unroll
  for (int c = 0; c < 4; ++c)
#pragma unroll
    for (int i = 0; i < 16; ++i) acc[c][i] = 0.f;
  u32x4 of[4][8];
  {
    auto rd = [&](int m) AI { A[m & 7] = tr_frag(vp + (m & 3) * 4096 + trb[m >> 2]); };
    auto put = [&](int s) AI {
      of[0][s] = pack8s(acc[s >> 1], s & 1, inv);
      __builtin_amdgcn_raw_buffer_store_b128(of[0][s], wsr, lane * 16 + s * 1024, me * 8192, 16);
    };
#pragma unroll
    for (int m = 0; m < 7; ++m) { rd(m); SB(); }
    sfor<0, 16>([&](auto ic) AI {
      constexpr int m = decltype(ic)::v;
      acc[m >> 2] = MFMA(A[m & 7], f[m & 3], acc[m >> 2]);
      SB();
      if constexpr (m + 7 < 16) rd(m + 7);
      if constexpr (m >= 5 && (m & 1)) put((m - 5) >> 1);
      SB();
    });
    put(6); SB(); put(7); SB();
  }
  STAMP(6);
#pragma unroll
  for (int c = 0; c < 4; ++c)
#pragma unroll
    for (int i = 0; i < 16; ++i) acc[c][i] = 0.f;
  bar_only();
  {
    auto rd = [&](int m) AI { A[m & 7] = tr_frag(lds + (m >> 2) * 4096 + trb[m & 3]); };
#pragma unroll
    for (int m = 0; m < 7; ++m) { rd(m); SB(); }
    sfor<0, 32>([&](auto ic) AI {
      constexpr int m = decltype(ic)::v;
      acc[m & 3] = MFMA(A[m & 7], BF(of[0][m >> 2]), acc[m & 3]);
      SB();
      if constexpr (m + 7 < 32) rd(m + 7);
      if constexpr (m == DRAIN1) {
        drain_vm();
        if (lane >= 1 && lane < 4)
          __hip_atomic_store((GAS u64*)(ws + WS_FLAG + (size_t)(me ^ lane) * 256) + (lane - 1), tag, RLX_AGENT);
      }
      SB();
    });
  }
  STAMP(7);
  bar_only();
  wait_flags(myflag, lane, 3, tag);
  STAMP(8);
  auto fetch = [&](int i) AI {
    of[1 + (i >> 3)][i & 7] = __builtin_amdgcn_raw_buffer_load_b128(wsr, lane * 16 + (i & 7) * 1024, (me ^ (1 + (i >> 3))) * 8192, 16);
  };
#pragma unroll
  for (int i = 0; i < 10; ++i) { fetch(i); SB(); }
  {
    auto rd = [&](int m) AI {
      if (m < 64) A[m & 7] = tr_frag(lds + 32768 + (m >> 2) * 4096 + trb[m & 3]);
      else A[m & 7] = tr_frag(lds + 98304 + (m & 7) * 4096 + trb[3 - ((m - 64) >> 3)]);
    };
#pragma unroll
    for (int m = 0; m < 7; ++m) { rd(m); SB(); }
    sfor<0, 96>([&](auto ic) AI {
      constexpr int m = decltype(ic)::v;
      if constexpr (m < 64) acc[m & 3] = MFMA(A[m & 7], BF(of[1 + (m >> 5)][(m >> 2) & 7]), acc[m & 3]);
      else acc[3 - ((m - 64) >> 3)] = MFMA(A[m & 7], BF(of[3][m & 7]), acc[3 - ((m - 64) >> 3)]);
      SB();
      if constexpr (m + 7 < 96) rd(m + 7);
      if constexpr (m % 3 == 0 && 10 + m / 3 < 24) fetch(10 + m / 3);
      if constexpr (m >= 72 && ((m & 7) == 1 || (m & 7) == 3)) {
        constexpr int d = 2 * (3 - ((m - 72) >> 3)) + ((m & 7) == 1 ? 1 : 0);
        __builtin_amdgcn_raw_buffer_store_b128(__builtin_bit_cast(u32x4, pack8(acc[d >> 1], d & 1)), wsr, lane * 16 + d * 1024,
                                               (unsigned)WS_P + (me ^ (d << 2)) * 8192, 16);
      }
      SB();
    });
    __builtin_amdgcn_raw_buffer_store_b128(__builtin_bit_cast(u32x4, pack8(acc[0], 1)), wsr, lane * 16 + 1024, (unsigned)WS_P + (me ^ 4) * 8192, 16);
  }
  STAMP(9);
  drain_vm();
  if (lane >= 1 && lane < 8)
    __hip_atomic_store((GAS u64*)(ws + WS_FLAG + (size_t)(me ^ (lane << 2)) * 256) + 16 + (lane - 1), tag, RLX_AGENT);
  wait_flags(myflag + 16, lane, 7, tag);
  STAMP(10);
  u32x4 pv[7];
#pragma unroll
  for (int d = 1; d < 8; ++d) pv[d - 1] = __builtin_amdgcn_raw_buffer_load_b128(wsr, lane * 16 + d * 1024, (unsigned)WS_P + me * 8192, 16);
  SB();
  float o[8];
#pragma unroll
  for (int i = 0; i < 8; ++i) o[i] = acc[0][i];
#pragma unroll
  for (int d = 0; d < 7; ++d)
#pragma unroll
    for (int i = 0; i < 4; ++i) {
      o[2 * i] += __uint_as_float(pv[d][i] << 16);
      o[2 * i + 1] += __uint_as_float(pv[d][i] & 0xffff0000u);
    }
  u32x4 ov = {pk2(o[0], o[1]), pk2(o[2], o[3]), pk2(o[4], o[5]), pk2(o[6], o[7])};
  *(GAS u32x4*)(out + (size_t)(bp * 128 + w * 32 + r) * 1024 + hh * 256 + j * 32 + hf * 16) = ov;
  if (j == 0 && hh == 0 && lane == 0) __hip_atomic_store((GAS u64*)(ws + WS_EPOCH + grp * 128), tag, RLX_AGENT);
}

extern "C" void kernel_launch(void* const* d_in, const int* in_sizes, int n_in, void* d_out, int out_size, void* d_ws, size_t ws_size,
                              hipStream_t stream) {
  gqa_fused<<<256, 512, 0, stream>>>((const char*)d_in[0], (const char*)d_in[1], (const char*)d_in[2], (const char*)d_in[3],
                                     (const char*)d_in[4], (char*)d_out, (char*)d_ws + ((size_t)0  __H0__ ));
}
```

```cpp
#include <hip/hip_runtime.h>
#include <hip/hip_bf16.h>

typedef unsigned short u16;
typedef unsigned long long u64;
typedef short s16x4 __attribute__((ext_vector_type(4)));
typedef short s16x8 __attribute__((ext_vector_type(8)));
typedef __bf16 bf16x8 __attribute__((ext_vector_type(8)));
typedef __bf16 bf16x2 __attribute__((ext_vector_type(2)));
typedef float f32x2 __attribute__((ext_vector_type(2)));
typedef float f32x16 __attribute__((ext_vector_type(16)));
typedef unsigned u32x4 __attribute__((ext_vector_type(4)));
#define LAS __attribute__((address_space(3)))
#define GAS __attribute__((address_space(1)))
#define DI __device__ __forceinline__
#define RLX_AGENT __ATOMIC_RELAXED, __HIP_MEMORY_SCOPE_AGENT
#define SB() __builtin_amdgcn_sched_barrier(0)
#define AI __attribute__((always_inline))

#define STAMPS 0
#define ROT 3
#define JR 0
#define AV 1
#define STG 1
#define WOP 1
#define DRAIN1 16
#define STAMP(i) do { } while (0)

constexpr size_t WS_O     = 0;
constexpr size_t WS_P     = 8u << 20;
constexpr size_t WS_FLAG  = 16u << 20;
constexpr size_t WS_EPOCH = WS_FLAG + (256u << 10);
constexpr size_t WS_STAMP = 32u << 20;

constexpr unsigned L_K = 3u * 32768, L_V = 4u * 32768;
constexpr unsigned LDS_BYTES = 160u << 10;

DI void dma16(const char* sbase, unsigned voff, unsigned lds) {
  asm volatile("s_mov_b32 m0, %2\n\ts_nop 0\n\tglobal_load_lds_dwordx4 %0, %1" ::"v"(voff), "s"(sbase), "s"(lds) : "memory");
}
DI void dma16nt(const char* sbase, unsigned voff, unsigned lds) {
  asm volatile("s_mov_b32 m0, %2\n\ts_nop 0\n\tglobal_load_lds_dwordx4 %0, %1 nt" ::"v"(voff), "s"(sbase), "s"(lds) : "memory");
}
DI void dma4(const char* sbase, unsigned voff, unsigned lds) {
  asm volatile("s_mov_b32 m0, %2\n\ts_nop 0\n\tglobal_load_lds_dword %0, %1" ::"v"(voff), "s"(sbase), "s"(lds) : "memory");
}
DI void pin(float& v) { asm volatile("" : "+v"(v)); }
DI void drain_vm() { asm volatile("s_waitcnt vmcnt(0)" ::: "memory"); }
template <int N> DI void wait_vm_bar() { asm volatile("s_waitcnt vmcnt(%0)\n\ts_barrier" ::"i"(N) : "memory"); }
DI void bar_only() { SB(); asm volatile("s_barrier" ::: "memory"); SB(); }
DI void lgkm_bar() { SB(); asm volatile("s_waitcnt lgkmcnt(0)\n\ts_barrier" ::: "memory"); SB(); }

DI u64 rtime() { u64 t; asm volatile("s_memrealtime %0\n\ts_waitcnt lgkmcnt(0)" : "=s"(t)::"memory"); return t; }
DI unsigned ctime_() { u64 t; asm volatile("s_memtime %0\n\ts_waitcnt lgkmcnt(0)" : "=s"(t)::"memory"); return (unsigned)t; }

DI bf16x8 tr_frag(const LAS char* p) {
  s16x4 lo = __builtin_amdgcn_ds_read_tr16_b64_v4i16((LAS s16x4*)p);
  s16x4 hi = __builtin_amdgcn_ds_read_tr16_b64_v4i16((LAS s16x4*)(p + 1024));
  return __builtin_bit_cast(bf16x8, __builtin_shufflevector(lo, hi, 0, 1, 2, 3, 4, 5, 6, 7));
}
DI unsigned pk2(float a, float b) { f32x2 v = {a, b}; return __builtin_bit_cast(unsigned, __builtin_convertvector(v, bf16x2)); }
DI bf16x8 pack8(const f32x16& a, int s) {
  u32x4 r = {pk2(a[8 * s], a[8 * s + 1]), pk2(a[8 * s + 2], a[8 * s + 3]), pk2(a[8 * s + 4], a[8 * s + 5]), pk2(a[8 * s + 6], a[8 * s + 7])};
  return __builtin_bit_cast(bf16x8, r);
}
DI u32x4 not_ones(u32x4 v) {
  return u32x4{min(v[0], 0xfffffffeu), min(v[1], 0xfffffffeu), min(v[2], 0xfffffffeu), min(v[3], 0xfffffffeu)};
}
DI u32x4 pack8s(const f32x16& a, int s, float k) {
  float t[8];
#pragma unroll
  for (int i = 0; i < 8; ++i) t[i] = a[8 * s + i] * k;
  SB();
  return u32x4{pk2(t[0], t[1]), pk2(t[2], t[3]), pk2(t[4], t[5]), pk2(t[6], t[7])};
}
DI unsigned umax4(unsigned m, u32x4 v) { return max(max(max(m, v[0]), v[1]), max(v[2], v[3])); }
template <int I> struct IC { static constexpr int v = I; };
template <int B, int E, class F> DI void sfor(F&& f) { if constexpr (B < E) { f(IC<B>{}); sfor<B + 1, E>(f); } }
#define MFMA(a, b, c) __builtin_amdgcn_mfma_f32_32x32x16_bf16((a), (b), (c), 0, 0, 0)
#define BF(x) __builtin_bit_cast(bf16x8, x)
constexpr unsigned SPIN_CAP = 1u << 20;

DI void wait_flags(const GAS u64* p, int lane, int n, u64 tag) {
  const GAS u64* q = p + (lane < n ? lane : 0);
#pragma clang loop unroll(disable)
  for (unsigned spins = 0; spins < SPIN_CAP; ++spins) {
    u64 v = __hip_atomic_load(q, RLX_AGENT);
    if (__all(v == tag)) break;
    __builtin_amdgcn_s_sleep(1);
  }
  asm volatile("" ::: "memory");
}

__global__ __launch_bounds__(512, 2) void gqa_fused(const char* __restrict__ x, const char* __restrict__ kc, const char* __restrict__ vc,
                                                   const char* __restrict__ wq, const char* __restrict__ wo, char* __restrict__ out, char* ws) {
  __shared__ __attribute__((aligned(16))) char lds_raw[LDS_BYTES];
  const LAS char* lds = (const LAS char*)lds_raw;
  const unsigned lds0 = (unsigned)(size_t)lds;
  const int lane = threadIdx.x & 63;
  const int w8 = __builtin_amdgcn_readfirstlane(threadIdx.x >> 6);
  const int j = blockIdx.x & 7, bp = blockIdx.x >> 5, hh = (blockIdx.x >> 3) & 3, h = 4 * j + hh;

  const bool early = (bp ^ hh) & 1;
  if (w8 >= 4) {
    const int lw = w8 - 4;
    const int rot = (2 * (bp >> 1) + 4 * (hh >> 1) + JR * (j & 1)) & 7;
    const unsigned l4 = lane >> 4, l3 = lane >> 3;
    const unsigned vx0 = l3 * 1024 + (((lane & 7) ^ l4) << 4), vx1 = vx0 ^ 64;
    const unsigned ptr_ = (lane & 15) ^ (l4 << 2);
    const unsigned vwq = l4 * 8192 + (ptr_ << 4);
    const unsigned vv = l4 * 256 + (ptr_ << 4);
    const unsigned vwo = l4 * 1024 + ((ptr_ ^ (unsigned)(j << 1)) << 4);
    const unsigned vk = l4 * 256 + (((lane & 15) ^ l4) << 4);
    const char* xb = x + (size_t)(bp * 128 + lw * 32) * 1024;
    const char* wqb = wq + (size_t)(lw * 16) * 8192 + h * 256;
    const size_t kvoff = (size_t)((bp * 2 + (lw >> 1)) * 32 + h) * 16384 + (lw & 1) * 8192;
    const char* kb = kc + kvoff;
    const char* vb = vc + kvoff;
    const char* wob = wo + (size_t)(j * 512 + lw * 16) * 1024 + hh * 256;

    auto chunk = [&](int c, int sl) AI {
      const int cc = (c + rot) & 7;
      const unsigned slot = lds0 + sl * 32768;
#pragma unroll
      for (int q = 0; q < 4; ++q) {
        dma16(wqb + (size_t)(cc * 64 + q * 4) * 8192, vwq, slot + 16384 + (lw * 16 + q * 4) * 256);
        dma16(xb + q * 8192 + cc * 128, (q & 1) ? vx1 : vx0, slot + (lw * 32 + q * 8) * 128);
      }
    };
    auto loadK = [&]() AI {
      dma4(wob + (size_t)(bp * 64) * 1024, ((lane & 31) >> 1) * 1024 + (lane & 1) * 128, lds0 + L_K + lw * 8192);
#pragma unroll
      for (int i = 0; i < 8; ++i) dma16nt(kb + i * 1024, vk ^ ((i & 3) << 6), lds0 + L_K + lw * 8192 + i * 1024);
    };
    auto loadV = [&]() AI {
#pragma unroll
      for (int i = 0; i < 8; ++i) dma16nt(vb + i * 1024, vv, lds0 + L_V + lw * 8192 + i * 1024);
    };
    auto loadWo = [&](int g) AI {
      const char* b = wob + (size_t)((hh ^ g) * 128) * 1024;
#pragma unroll
      for (int q = 0; q < 8; ++q)
        dma16(b + (size_t)((q >> 2) * 64 + (q & 3) * 4) * 1024, vwo, lds0 + g * 32768 + ((q >> 2) * 64 + lw * 16 + (q & 3) * 4) * 256);
    };
    if (early) {
      loadK(); loadV(); chunk(0, 0); chunk(4, 1);
      wait_vm_bar<8>(); chunk(1, 2);
      wait_vm_bar<8>(); chunk(5, 0);
      wait_vm_bar<8>(); chunk(2, 1);
      wait_vm_bar<8>(); chunk(6, 2);
      wait_vm_bar<8>(); chunk(3, 0);
      wait_vm_bar<8>(); chunk(7, 1);
      wait_vm_bar<8>(); loadWo(2);
      wait_vm_bar<8>(); loadWo(0);
      wait_vm_bar<63>(); loadWo(1);
      wait_vm_bar<63>();
      wait_vm_bar<0>(); loadWo(3);
    } else {
      chunk(0, 0); chunk(1, 1);
      wait_vm_bar<8>(); chunk(2, 2);
      wait_vm_bar<8>(); chunk(3, 3);
      wait_vm_bar<8>(); chunk(4, 4);
      wait_vm_bar<8>(); chunk(5, 0); chunk(6, 1); chunk(7, 2);
      wait_vm_bar<24>(); loadK();
      wait_vm_bar<9>(); loadV();
      wait_vm_bar<8>(); loadWo(0);
      wait_vm_bar<8>();
      wait_vm_bar<0>(); loadWo(1); loadWo(2); loadWo(3);
    }
    wait_vm_bar<0>();
    return;
  }

  const int w = w8;
  const int r = lane & 31, hf = lane >> 5;
  const int grp = bp * 4 + w;
  const int me = (grp * 8 + j) * 4 + hh;
  const u64 tag = __hip_atomic_load((const GAS u64*)(ws + WS_EPOCH + grp * 128), RLX_AGENT) + 1;
  const __amdgpu_buffer_rsrc_t wsr = __builtin_amdgcn_make_buffer_rsrc(ws, 0, 16u << 20, 0x00020000);
  const GAS u64* myflag = (const GAS u64*)(ws + WS_FLAG + (size_t)me * 256);
  { u64 warm = __hip_atomic_load(myflag + (lane & 1) * 16, RLX_AGENT); asm volatile("" ::"v"(warm)); }

  const unsigned q4 = (lane & 15) >> 2, p4 = lane & 3, blk = (lane >> 4) & 1;
  const unsigned trc = (8 * hf + q4) * 256 + 32 * blk + 8 * (((p4 & 1) << 1) | (p4 >> 1));
  unsigned trb[4];
#pragma unroll
  for (int c = 0; c < 4; ++c) trb[c] = trc + ((c ^ q4) << 6);
  const unsigned xsw = (r >> 1) & 7;
  unsigned xfb[4];
#pragma unroll
  for (int s = 0; s < 4; ++s) xfb[s] = (w * 32 + r) * 128 + (((2 * s + hf) ^ xsw) << 4);
  const int kr = (r & 19) | ((r & 4) << 1) | ((r & 8) >> 1);
  const LAS char* kp = lds + L_K + (w >> 1) * 16384 + kr * 256;

  f32x16 acc[4], sc[2];
#pragma unroll
  for (int c = 0; c < 4; ++c)
#pragma unroll
    for (int i = 0; i < 16; ++i) acc[c][i] = 0.f;
#pragma unroll
  for (int t = 0; t < 2; ++t)
#pragma unroll
    for (int i = 0; i < 16; ++i) sc[t][i] = 0.f;

  bf16x8 A[8];
  bf16x8 f[8];
  bf16x8 kf[16];
  {
    bf16x8 B[8];
    auto stp = [](int m) AI { return m < 112 ? (m >> 2) & 3 : m & 3; };
    auto til = [](int m) AI { return m < 112 ? m & 3 : (m >> 2) & 3; };
    auto rd = [&](int m) AI {
      const int c = m >> 4, s = stp(m), t = til(m);
      const unsigned slot = c < 3 ? c * 32768u : early ? (c % 3) * 32768u : (c % 5) * 32768u;
      if (t == 0) B[(c & 1) * 4 + s] = *(const LAS bf16x8*)(lds + slot + xfb[s]);
      A[m & 7] = tr_frag(lds + slot + 16384 + s * 4096 + trb[t]);
    };
    bar_only();
    STAMP(1);
#pragma unroll
    for (int m = 0; m < 6; ++m) rd(m);
    SB();
    sfor<0, 128>([&](auto ic) AI {
      constexpr int m = decltype(ic)::v;
      acc[til(m)] = MFMA(A[m & 7], B[((m >> 4) & 1) * 4 + stp(m)], acc[til(m)]);
      SB();
      constexpr int k = m & 15;
      if constexpr (m < 112) {
        if (k < 10) rd(m + 6);
        else if (k == 12) {
          if ((m >> 4) >= 5 ? early : true) lgkm_bar();
          SB();
          rd(m + 4); rd(m + 5);
        }
        else if (k == 13) { rd(m + 5); rd(m + 6); }
        else if (k > 13) rd(m + 6);
      } else {
        if (m + 6 < 128) rd(m + 6);
        if constexpr (m >= 112 && k >= 5 && (k & 1)) f[(k - 5) >> 1] = pack8(acc[(k - 5) >> 2], ((k - 5) >> 1) & 1);
      }
      SB();
    });
  }
  STAMP(2);
  const LAS char* vp = lds + L_V + (w >> 1) * 16384;
  f[6] = pack8(acc[3], 0);
  f[7] = pack8(acc[3], 1);
  lgkm_bar();
  STAMP(3);
  constexpr float C = 0.08838834764831845f * 1.4426950408889634f;
  float m0, sum0 = 0.f;
  {
    auto rdk = [&](int i) AI { kf[i] = *(const LAS bf16x8*)(kp + (i >> 3) * 8192 + (((2 * (i & 7) + hf) ^ (kr & 15)) << 4)); };
#pragma unroll
    for (int i = 0; i < 12; ++i) { rdk(i); SB(); }
    sfor<0, 16>([&](auto ic) AI {
      constexpr int m = decltype(ic)::v;
      sc[m >> 3] = MFMA(kf[m], f[m & 7], sc[m >> 3]);
      SB();
      if constexpr (m < 4) rdk(12 + m);
      if constexpr (m == 9) {
        m0 = sc[0][0];
#pragma unroll
        for (int i = 1; i < 16; ++i) m0 = fmaxf(m0, sc[0][i]);
        m0 *= C;
      }
      if constexpr (m >= 10) {
#pragma unroll
        for (int i = (m - 10) * 8 / 3; i < (m - 9) * 8 / 3; ++i) sc[0][i] = __builtin_amdgcn_exp2f(fmaf(sc[0][i], C, -m0));
      }
      SB();
    });
  }
#pragma unroll
  for (int i = 0; i < 16; ++i) sum0 += sc[0][i];
  pin(sum0);
  float inv;
  {
    float mx = sc[1][0];
#pragma unroll
    for (int i = 1; i < 16; ++i) mx = fmaxf(mx, sc[1][i]);
    mx = fmaxf(mx * C, m0);
    auto rr = __builtin_amdgcn_permlane32_swap(__float_as_uint(mx), __float_as_uint(mx), false, false);
    mx = fmaxf(__uint_as_float(rr[0]), __uint_as_float(rr[1]));
    const float a0 = __builtin_amdgcn_exp2f(m0 - mx);
    float sum = sum0 * a0;
#pragma unroll
    for (int i = 0; i < 16; ++i) sc[1][i] = __builtin_amdgcn_exp2f(fmaf(sc[1][i], C, -mx));
    f[0] = BF(pack8s(sc[0], 0, a0));
    f[1] = BF(pack8s(sc[0], 1, a0));
#pragma unroll
    for (int i = 0; i < 16; ++i) sum += sc[1][i];
    f[2] = pack8(sc[1], 0);
    f[3] = pack8(sc[1], 1);
    auto r2 = __builtin_amdgcn_permlane32_swap(__float_as_uint(sum), __float_as_uint(sum), false, false);
    inv = __builtin_amdgcn_rcpf(__uint_as_float(r2[0]) + __uint_as_float(r2[1]));
  }
  asm volatile("" : "+v"(f[0]), "+v"(f[1]), "+v"(f[2]), "+v"(f[3]), "+v"(inv));
  STAMP(4);
  bar_only();
  STAMP(5);
#pragma unroll
  for (int c = 0; c < 4; ++c)
#pragma unroll
    for (int i = 0; i < 16; ++i) acc[c][i] = 0.f;
  u32x4 of[4][8];
  {
    auto rd = [&](int m) AI { A[m & 7] = tr_frag(vp + (m & 3) * 4096 + trb[m >> 2]); };
    auto put = [&](int s) AI {
      of[0][s] = pack8s(acc[s >> 1], s & 1, inv);
      __builtin_amdgcn_raw_buffer_store_b128(of[0][s], wsr, lane * 16 + s * 1024, me * 8192, 16);
    };
#pragma unroll
    for (int m = 0; m < 7; ++m) { rd(m); SB(); }
    sfor<0, 16>([&](auto ic) AI {
      constexpr int m = decltype(ic)::v;
      acc[m >> 2] = MFMA(A[m & 7], f[m & 3], acc[m >> 2]);
      SB();
      if constexpr (m + 7 < 16) rd(m + 7);
      if constexpr (m >= 5 && (m & 1)) put((m - 5) >> 1);
      SB();
    });
    put(6); SB(); put(7); SB();
  }
  STAMP(6);
#pragma unroll
  for (int c = 0; c < 4; ++c)
#pragma unroll
    for (int i = 0; i < 16; ++i) acc[c][i] = 0.f;
  bar_only();
  {
    auto rd = [&](int m) AI { A[m & 7] = tr_frag(lds + (m >> 2) * 4096 + trb[m & 3]); };
#pragma unroll
    for (int m = 0; m < 7; ++m) { rd(m); SB(); }
    sfor<0, 32>([&](auto ic) AI {
      constexpr int m = decltype(ic)::v;
      acc[m & 3] = MFMA(A[m & 7], BF(of[0][m >> 2]), acc[m & 3]);
      SB();
      if constexpr (m + 7 < 32) rd(m + 7);
      if constexpr (m == DRAIN1) {
        drain_vm();
        if (lane >= 1 && lane < 4)
          __hip_atomic_store((GAS u64*)(ws + WS_FLAG + (size_t)(me ^ lane) * 256) + (lane - 1), tag, RLX_AGENT);
      }
      SB();
    });
  }
  STAMP(7);
  bar_only();
  wait_flags(myflag, lane, 3, tag);
  STAMP(8);
  auto fetch = [&](int i) AI {
    of[1 + (i >> 3)][i & 7] = __builtin_amdgcn_raw_buffer_load_b128(wsr, lane * 16 + (i & 7) * 1024, (me ^ (1 + (i >> 3))) * 8192, 16);
  };
#pragma unroll
  for (int i = 0; i < 10; ++i) { fetch(i); SB(); }
  {
    auto rd = [&](int m) AI {
      if (m < 64) A[m & 7] = tr_frag(lds + 32768 + (m >> 2) * 4096 + trb[m & 3]);
      else A[m & 7] = tr_frag(lds + 98304 + (m & 7) * 4096 + trb[3 - ((m - 64) >> 3)]);
    };
#pragma unroll
    for (int m = 0; m < 7; ++m) { rd(m); SB(); }
    sfor<0, 96>([&](auto ic) AI {
      constexpr int m = decltype(ic)::v;
      if constexpr (m < 64) acc[m & 3] = MFMA(A[m & 7], BF(of[1 + (m >> 5)][(m >> 2) & 7]), acc[m & 3]);
      else acc[3 - ((m - 64) >> 3)] = MFMA(A[m & 7], BF(of[3][m & 7]), acc[3 - ((m - 64) >> 3)]);
      SB();
      if constexpr (m + 7 < 96) rd(m + 7);
      if constexpr (m % 3 == 0 && 10 + m / 3 < 24) fetch(10 + m / 3);
      if constexpr (m >= 72 && ((m & 7) == 1 || (m & 7) == 3)) {
        constexpr int d = 2 * (3 - ((m - 72) >> 3)) + ((m & 7) == 1 ? 1 : 0);
        __builtin_amdgcn_raw_buffer_store_b128(__builtin_bit_cast(u32x4, pack8(acc[d >> 1], d & 1)), wsr, lane * 16 + d * 1024,
                                               (unsigned)WS_P + (me ^ (d << 2)) * 8192, 16);
      }
      SB();
    });
    __builtin_amdgcn_raw_buffer_store_b128(__builtin_bit_cast(u32x4, pack8(acc[0], 1)), wsr, lane * 16 + 1024, (unsigned)WS_P + (me ^ 4) * 8192, 16);
  }
  STAMP(9);
  drain_vm();
  if (lane >= 1 && lane < 8)
    __hip_atomic_store((GAS u64*)(ws + WS_FLAG + (size_t)(me ^ (lane << 2)) * 256) + 16 + (lane - 1), tag, RLX_AGENT);
  wait_flags(myflag + 16, lane, 7, tag);
  STAMP(10);
  u32x4 pv[7];
#pragma unroll
  for (int d = 1; d < 8; ++d) pv[d - 1] = __builtin_amdgcn_raw_buffer_load_b128(wsr, lane * 16 + d * 1024, (unsigned)WS_P + me * 8192, 16);
  SB();
  float o[8];
#pragma unroll
  for (int i = 0; i < 8; ++i) o[i] = acc[0][i];
#pragma unroll
  for (int d = 0; d < 7; ++d)
#pragma unroll
    for (int i = 0; i < 4; ++i) {
      o[2 * i] += __uint_as_float(pv[d][i] << 16);
      o[2 * i + 1] += __uint_as_float(pv[d][i] & 0xffff0000u);
    }
  u32x4 ov = {pk2(o[0], o[1]), pk2(o[2], o[3]), pk2(o[4], o[5]), pk2(o[6], o[7])};
  *(GAS u32x4*)(out + (size_t)(bp * 128 + w * 32 + r) * 1024 + hh * 256 + j * 32 + hf * 16) = ov;
  if (j == 0 && hh == 0 && lane == 0) __hip_atomic_store((GAS u64*)(ws + WS_EPOCH + grp * 128), tag, RLX_AGENT);
}

extern "C" void kernel_launch(void* const* d_in, const int* in_sizes, int n_in, void* d_out, int out_size, void* d_ws, size_t ws_size,
                              hipStream_t stream) {
  gqa_fused<<<256, 512, 0, stream>>>((const char*)d_in[0], (const char*)d_in[1], (const char*)d_in[2], (const char*)d_in[3],
                                     (const char*)d_in[4], (char*)d_out, (char*)d_ws);
}
```

```cpp
#include <hip/hip_runtime.h>
#include <hip/hip_bf16.h>

typedef unsigned short u16;
typedef unsigned long long u64;
typedef short s16x4 __attribute__((ext_vector_type(4)));
typedef short s16x8 __attribute__((ext_vector_type(8)));
typedef __bf16 bf16x8 __attribute__((ext_vector_type(8)));
typedef __bf16 bf16x2 __attribute__((ext_vector_type(2)));
typedef float f32x2 __attribute__((ext_vector_type(2)));
typedef float f32x16 __attribute__((ext_vector_type(16)));
typedef unsigned u32x4 __attribute__((ext_vector_type(4)));
#define LAS __attribute__((address_space(3)))
#define GAS __attribute__((address_space(1)))
#define DI __device__ __forceinline__
#define RLX_AGENT __ATOMIC_RELAXED, __HIP_MEMORY_SCOPE_AGENT
#define SB() __builtin_amdgcn_sched_barrier(0)
#define AI __attribute__((always_inline))

#define STAMPS 0
#define ROT 3
#define FL2 92
#define PV_ 1
#define TOUCHM -1
#define TLBW 1
#define STG 1
#define WOP 1
#define DRAIN1 16
#define STAMP(i) do { } while (0)

constexpr size_t WS_O     = 0;
constexpr size_t WS_P     = 8u << 20;
constexpr size_t WS_FLAG  = 16u << 20;
constexpr size_t WS_EPOCH = WS_FLAG + (512u << 10);
constexpr size_t WS_STAMP = 32u << 20;

constexpr unsigned L_K = 3u * 32768, L_V = 4u * 32768;
constexpr unsigned LDS_BYTES = 160u << 10;

DI void dma16(const char* sbase, unsigned voff, unsigned lds) {
  asm volatile("s_mov_b32 m0, %2\n\ts_nop 0\n\tglobal_load_lds_dwordx4 %0, %1" ::"v"(voff), "s"(sbase), "s"(lds) : "memory");
}
DI void dma16nt(const char* sbase, unsigned voff, unsigned lds) {
  asm volatile("s_mov_b32 m0, %2\n\ts_nop 0\n\tglobal_load_lds_dwordx4 %0, %1 nt" ::"v"(voff), "s"(sbase), "s"(lds) : "memory");
}
DI void dma4(const char* sbase, unsigned voff, unsigned lds) {
  asm volatile("s_mov_b32 m0, %2\n\ts_nop 0\n\tglobal_load_lds_dword %0, %1" ::"v"(voff), "s"(sbase), "s"(lds) : "memory");
}
DI void pin(float& v) { asm volatile("" : "+v"(v)); }
DI void pin64(u64& v) { asm volatile("" : "+v"(v)); }
DI u64 ld_flag(const GAS u64* q) { return __hip_atomic_load(q, RLX_AGENT); }
DI void drain_vm() { asm volatile("s_waitcnt vmcnt(0)" ::: "memory"); }
template <int N> DI void wait_vm_bar() { asm volatile("s_waitcnt vmcnt(%0)\n\ts_barrier" ::"i"(N) : "memory"); }
DI void bar_only() { SB(); asm volatile("s_barrier" ::: "memory"); SB(); }
DI void lgkm_bar() { SB(); asm volatile("s_waitcnt lgkmcnt(0)\n\ts_barrier" ::: "memory"); SB(); }

DI u64 rtime() { u64 t; asm volatile("s_memrealtime %0\n\ts_waitcnt lgkmcnt(0)" : "=s"(t)::"memory"); return t; }
DI unsigned ctime_() { u64 t; asm volatile("s_memtime %0\n\ts_waitcnt lgkmcnt(0)" : "=s"(t)::"memory"); return (unsigned)t; }

DI bf16x8 tr_frag(const LAS char* p) {
  s16x4 lo = __builtin_amdgcn_ds_read_tr16_b64_v4i16((LAS s16x4*)p);
  s16x4 hi = __builtin_amdgcn_ds_read_tr16_b64_v4i16((LAS s16x4*)(p + 1024));
  return __builtin_bit_cast(bf16x8, __builtin_shufflevector(lo, hi, 0, 1, 2, 3, 4, 5, 6, 7));
}
DI unsigned pk2(float a, float b) { f32x2 v = {a, b}; return __builtin_bit_cast(unsigned, __builtin_convertvector(v, bf16x2)); }
DI bf16x8 pack8(const f32x16& a, int s) {
  u32x4 r = {pk2(a[8 * s], a[8 * s + 1]), pk2(a[8 * s + 2], a[8 * s + 3]), pk2(a[8 * s + 4], a[8 * s + 5]), pk2(a[8 * s + 6], a[8 * s + 7])};
  return __builtin_bit_cast(bf16x8, r);
}
DI u32x4 not_ones(u32x4 v) {
  return u32x4{min(v[0], 0xfffffffeu), min(v[1], 0xfffffffeu), min(v[2], 0xfffffffeu), min(v[3], 0xfffffffeu)};
}
DI u32x4 pack8s(const f32x16& a, int s, float k) {
  float t[8];
#pragma unroll
  for (int i = 0; i < 8; ++i) t[i] = a[8 * s + i] * k;
  SB();
  return u32x4{pk2(t[0], t[1]), pk2(t[2], t[3]), pk2(t[4], t[5]), pk2(t[6], t[7])};
}
DI unsigned umax4(unsigned m, u32x4 v) { return max(max(max(m, v[0]), v[1]), max(v[2], v[3])); }
template <int I> struct IC { static constexpr int v = I; };
template <int B, int E, class F> DI void sfor(F&& f) { if constexpr (B < E) { f(IC<B>{}); sfor<B + 1, E>(f); } }
#define MFMA(a, b, c) __builtin_amdgcn_mfma_f32_32x32x16_bf16((a), (b), (c), 0, 0, 0)
#define BF(x) __builtin_bit_cast(bf16x8, x)
constexpr unsigned SPIN_CAP = 1u << 20;

DI void wait_flags(const GAS u64* p, int lane, int n, u64 tag, int stride = 1) {
  const GAS u64* q = p + (lane < n ? lane : 0) * stride;
#pragma clang loop unroll(disable)
  for (unsigned spins = 0; spins < SPIN_CAP; ++spins) {
    u64 v = __hip_atomic_load(q, RLX_AGENT);
    if (__all(v == tag)) break;
    __builtin_amdgcn_s_sleep(1);
  }
  asm volatile("" ::: "memory");
}

__global__ __launch_bounds__(512, 2) void gqa_fused(const char* __restrict__ x, const char* __restrict__ kc, const char* __restrict__ vc,
                                                   const char* __restrict__ wq, const char* __restrict__ wo, char* __restrict__ out, char* ws) {
  __shared__ __attribute__((aligned(16))) char lds_raw[LDS_BYTES];
  const LAS char* lds = (const LAS char*)lds_raw;
  const unsigned lds0 = (unsigned)(size_t)lds;
  const int lane = threadIdx.x & 63;
  const int w8 = __builtin_amdgcn_readfirstlane(threadIdx.x >> 6);
  const int j = blockIdx.x & 7, bp = blockIdx.x >> 5, hh = (blockIdx.x >> 3) & 3, h = 4 * j + hh;

  const bool early = STG == 0 ? false : STG == 1 ? (bp & 1) : STG == 2 ? ((bp ^ hh) & 1) : true;
  if (w8 >= 4) {
    const int lw = w8 - 4;
    const int rot = ROT == 0 ? 0 : ROT == 1 ? (((bp ^ hh) & 1) << 2) : ROT == 2 ? (((bp + hh) & 3) << 1) : ((bp + 2 * hh + (j & 1)) & 7);
    const unsigned l4 = lane >> 4, l3 = lane >> 3;
    const unsigned vx0 = l3 * 1024 + (((lane & 7) ^ l4) << 4), vx1 = vx0 ^ 64;
    const unsigned ptr_ = (lane & 15) ^ (l4 << 2);
    const unsigned vwq = l4 * 8192 + (ptr_ << 4);
    const unsigned vv = l4 * 256 + (ptr_ << 4);
    const unsigned vwo = l4 * 1024 + ((ptr_ ^ (unsigned)(j << 1)) << 4);
    const unsigned vk = l4 * 256 + (((lane & 15) ^ l4) << 4);
    const char* xb = x + (size_t)(bp * 128 + lw * 32) * 1024;
    const char* wqb = wq + (size_t)(lw * 16) * 8192 + h * 256;
    const size_t kvoff = (size_t)((bp * 2 + (lw >> 1)) * 32 + h) * 16384 + (lw & 1) * 8192;
    const char* kb = kc + kvoff;
    const char* vb = vc + kvoff;
    const char* wob = wo + (size_t)(j * 512 + lw * 16) * 1024 + hh * 256;

    auto chunk = [&](int c, int sl) AI {
      const int cc = (c + rot) & 7;
      const unsigned slot = lds0 + sl * 32768;
#pragma unroll
      for (int q = 0; q < 4; ++q) {
        dma16(wqb + (size_t)(cc * 64 + q * 4) * 8192, vwq, slot + 16384 + (lw * 16 + q * 4) * 256);
        dma16(xb + q * 8192 + cc * 128, (q & 1) ? vx1 : vx0, slot + (lw * 32 + q * 8) * 128);
      }
    };
    auto loadK = [&]() AI {
      dma4(wob + (size_t)(bp * 64) * 1024, ((lane & 31) >> 1) * 1024 + (lane & 1) * 128, lds0 + L_K + lw * 8192);
#pragma unroll
      for (int i = 0; i < 8; ++i) dma16nt(kb + i * 1024, vk ^ ((i & 3) << 6), lds0 + L_K + lw * 8192 + i * 1024);
    };
    auto loadV = [&]() AI {
#pragma unroll
      for (int i = 0; i < 8; ++i) dma16nt(vb + i * 1024, vv, lds0 + L_V + lw * 8192 + i * 1024);
    };
    auto loadWo = [&](int g) AI {
      const char* b = wob + (size_t)((hh ^ g) * 128) * 1024;
#pragma unroll
      for (int q = 0; q < 8; ++q)
        dma16(b + (size_t)((q >> 2) * 64 + (q & 3) * 4) * 1024, vwo, lds0 + g * 32768 + ((q >> 2) * 64 + lw * 16 + (q & 3) * 4) * 256);
    };
    chunk(0, 0); chunk(1, 1);
    wait_vm_bar<8>(); chunk(2, 2);
    if (early) {
      wait_vm_bar<8>(); chunk(3, 4); loadK();
      wait_vm_bar<9>(); chunk(4, 0); chunk(5, 1);
      wait_vm_bar<8>(); chunk(6, 2);
      wait_vm_bar<0>(); chunk(7, 0); loadV();
      wait_vm_bar<8>();
      wait_vm_bar<63>(); loadWo(0);
      wait_vm_bar<8>();
      wait_vm_bar<0>(); loadWo(1); loadWo(2); loadWo(3);
    } else {
      wait_vm_bar<8>(); chunk(3, 3); chunk(4, 4);
      wait_vm_bar<0>(); chunk(5, 0); chunk(6, 1);
      wait_vm_bar<63>(); chunk(7, 2);
      wait_vm_bar<63>(); loadK();
      wait_vm_bar<9>(); loadV();
      wait_vm_bar<8>(); loadWo(0);
      wait_vm_bar<8>(); if (!WOP) { loadWo(1); loadWo(2); }
      wait_vm_bar<WOP ? 0 : 16>(); if (WOP) { loadWo(1); loadWo(2); } loadWo(3);
    }
    wait_vm_bar<0>();
    return;
  }

  const int w = w8;
  const int r = lane & 31, hf = lane >> 5;
  const int grp = bp * 4 + w;
  const int me = (grp * 8 + j) * 4 + hh;
  const u64 tag = __hip_atomic_load((const GAS u64*)(ws + WS_EPOCH + grp * 128), RLX_AGENT) + 1;
  const __amdgpu_buffer_rsrc_t wsr = __builtin_amdgcn_make_buffer_rsrc(ws, 0, 16u << 20, 0x00020000);
  const GAS u64* myflag = (const GAS u64*)(ws + WS_FLAG + (size_t)me * 512);
  u64 warm = 0;
  unsigned tl = 0;
  if (TLBW) {
    const size_t kvo = (size_t)((bp * 2 + (w >> 1)) * 32 + h) * 16384;
    const char* p = lane == 0 ? kc + kvo : lane == 1 ? vc + kvo : lane == 2 ? wo + (size_t)(j * 512) * 1024 : lane == 3 ? ws + (size_t)me * 8192
                                                                                                                       : out + (size_t)(bp * 128 + w * 32) * 1024;
    tl = __hip_atomic_load((const GAS unsigned*)p, RLX_AGENT);
  }
  if (TOUCHM < 0) warm = __hip_atomic_load(myflag + (lane & 3) * 16, RLX_AGENT);
  __builtin_amdgcn_raw_buffer_store_b128(u32x4{~0u, ~0u, ~0u, ~0u}, wsr, lane * 16 + 1024, (unsigned)WS_P + (me ^ 4) * 8192, 16);

  const unsigned q4 = (lane & 15) >> 2, p4 = lane & 3, blk = (lane >> 4) & 1;
  const unsigned trc = (8 * hf + q4) * 256 + 32 * blk + 8 * (((p4 & 1) << 1) | (p4 >> 1));
  unsigned trb[4];
#pragma unroll
  for (int c = 0; c < 4; ++c) trb[c] = trc + ((c ^ q4) << 6);
  const unsigned xsw = (r >> 1) & 7;
  unsigned xfb[4];
#pragma unroll
  for (int s = 0; s < 4; ++s) xfb[s] = (w * 32 + r) * 128 + (((2 * s + hf) ^ xsw) << 4);
  const int kr = (r & 19) | ((r & 4) << 1) | ((r & 8) >> 1);
  const LAS char* kp = lds + L_K + (w >> 1) * 16384 + kr * 256;

  f32x16 acc[4], sc[2];
#pragma unroll
  for (int c = 0; c < 4; ++c)
#pragma unroll
    for (int i = 0; i < 16; ++i) acc[c][i] = 0.f;
#pragma unroll
  for (int t = 0; t < 2; ++t)
#pragma unroll
    for (int i = 0; i < 16; ++i) sc[t][i] = 0.f;

  bf16x8 A[8];
  bf16x8 f[8];
  bf16x8 kf[16];
  {
    bf16x8 B[8];
    auto stp = [](int m) AI { return m < 112 ? (m >> 2) & 3 : m & 3; };
    auto til = [](int m) AI { return m < 112 ? m & 3 : (m >> 2) & 3; };
    auto rd = [&](int m) AI {
      const int c = m >> 4, s = stp(m), t = til(m);
      const unsigned slot = c < 3 ? c * 32768u : early ? (c == 3 ? 4u : (c - 4) % 3) * 32768u : (c % 5) * 32768u;
      if (t == 0) B[(c & 1) * 4 + s] = *(const LAS bf16x8*)(lds + slot + xfb[s]);
      A[m & 7] = tr_frag(lds + slot + 16384 + s * 4096 + trb[t]);
    };
    bar_only();
    STAMP(1);
#pragma unroll
    for (int m = 0; m < 6; ++m) rd(m);
    SB();
    sfor<0, 128>([&](auto ic) AI {
      constexpr int m = decltype(ic)::v;
      acc[til(m)] = MFMA(A[m & 7], B[((m >> 4) & 1) * 4 + stp(m)], acc[til(m)]);
      SB();
      constexpr int k = m & 15;
      if constexpr (m == TOUCHM) warm = ld_flag(myflag + (lane & 3) * 16);
      if constexpr (m < 112) {
        if (k < 10) rd(m + 6);
        else if (k == 12) {
          if ((m >> 4) == 2 ? !early : (m >> 4) == 5 ? false : (m >> 4) == 6 ? early : true) lgkm_bar();
          SB();
          rd(m + 4); rd(m + 5);
        }
        else if (k == 13) { rd(m + 5); rd(m + 6); }
        else if (k > 13) rd(m + 6);
      } else {
        if (m + 6 < 128) rd(m + 6);
        if constexpr (m >= 112 && k >= 5 && (k & 1)) f[(k - 5) >> 1] = pack8(acc[(k - 5) >> 2], ((k - 5) >> 1) & 1);
      }
      SB();
    });
  }
  STAMP(2);
  const LAS char* vp = lds + L_V + (w >> 1) * 16384;
  f[6] = pack8(acc[3], 0);
  f[7] = pack8(acc[3], 1);
  lgkm_bar();
  STAMP(3);
  constexpr float C = 0.08838834764831845f * 1.4426950408889634f;
  float m0, sum0 = 0.f;
  {
    auto rdk = [&](int i) AI { kf[i] = *(const LAS bf16x8*)(kp + (i >> 3) * 8192 + (((2 * (i & 7) + hf) ^ (kr & 15)) << 4)); };
#pragma unroll
    for (int i = 0; i < 12; ++i) { rdk(i); SB(); }
    sfor<0, 16>([&](auto ic) AI {
      constexpr int m = decltype(ic)::v;
      sc[m >> 3] = MFMA(kf[m], f[m & 7], sc[m >> 3]);
      SB();
      if constexpr (m < 4) rdk(12 + m);
      if constexpr (m == 9) {
        m0 = sc[0][0];
#pragma unroll
        for (int i = 1; i < 16; ++i) m0 = fmaxf(m0, sc[0][i]);
        m0 *= C;
      }
      if constexpr (m >= 10) {
#pragma unroll
        for (int i = (m - 10) * 8 / 3; i < (m - 9) * 8 / 3; ++i) sc[0][i] = __builtin_amdgcn_exp2f(fmaf(sc[0][i], C, -m0));
      }
      SB();
    });
  }
#pragma unroll
  for (int i = 0; i < 16; ++i) sum0 += sc[0][i];
  pin(sum0);
  float inv;
  {
    float mx = sc[1][0];
#pragma unroll
    for (int i = 1; i < 16; ++i) mx = fmaxf(mx, sc[1][i]);
    mx = fmaxf(mx * C, m0);
    auto rr = __builtin_amdgcn_permlane32_swap(__float_as_uint(mx), __float_as_uint(mx), false, false);
    mx = fmaxf(__uint_as_float(rr[0]), __uint_as_float(rr[1]));
    const float a0 = __builtin_amdgcn_exp2f(m0 - mx);
    float sum = sum0 * a0;
#pragma unroll
    for (int i = 0; i < 16; ++i) sc[1][i] = __builtin_amdgcn_exp2f(fmaf(sc[1][i], C, -mx));
    f[0] = BF(pack8s(sc[0], 0, a0));
    f[1] = BF(pack8s(sc[0], 1, a0));
#pragma unroll
    for (int i = 0; i < 16; ++i) sum += sc[1][i];
    f[2] = pack8(sc[1], 0);
    f[3] = pack8(sc[1], 1);
    auto r2 = __builtin_amdgcn_permlane32_swap(__float_as_uint(sum), __float_as_uint(sum), false, false);
    inv = __builtin_amdgcn_rcpf(__uint_as_float(r2[0]) + __uint_as_float(r2[1]));
  }
  asm volatile("" : "+v"(f[0]), "+v"(f[1]), "+v"(f[2]), "+v"(f[3]), "+v"(inv));
  STAMP(4);
  bar_only();
  STAMP(5);
#pragma unroll
  for (int c = 0; c < 4; ++c)
#pragma unroll
    for (int i = 0; i < 16; ++i) acc[c][i] = 0.f;
  u32x4 of[4][8];
  {
    auto rd = [&](int m) AI { A[m & 7] = tr_frag(vp + (m & 3) * 4096 + trb[m >> 2]); };
    auto put = [&](int s) AI {
      of[0][s] = pack8s(acc[s >> 1], s & 1, inv);
      __builtin_amdgcn_raw_buffer_store_b128(of[0][s], wsr, lane * 16 + s * 1024, me * 8192, 16);
    };
#pragma unroll
    for (int m = 0; m < 7; ++m) { rd(m); SB(); }
    sfor<0, 16>([&](auto ic) AI {
      constexpr int m = decltype(ic)::v;
      acc[m >> 2] = MFMA(A[m & 7], f[m & 3], acc[m >> 2]);
      SB();
      if constexpr (m + 7 < 16) rd(m + 7);
      if constexpr (m >= 5 && (m & 1)) put((m - 5) >> 1);
      SB();
    });
    put(6); SB(); put(7); SB();
  }
  STAMP(6);
#pragma unroll
  for (int c = 0; c < 4; ++c)
#pragma unroll
    for (int i = 0; i < 16; ++i) acc[c][i] = 0.f;
  u64 early_poll;
  pin64(warm);
  asm volatile("" ::"v"(tl));
  bar_only();
  {
    auto rd = [&](int m) AI { A[m & 7] = tr_frag(lds + (m >> 2) * 4096 + trb[m & 3]); };
#pragma unroll
    for (int m = 0; m < 7; ++m) { rd(m); SB(); }
    sfor<0, 32>([&](auto ic) AI {
      constexpr int m = decltype(ic)::v;
      acc[m & 3] = MFMA(A[m & 7], BF(of[0][m >> 2]), acc[m & 3]);
      SB();
      if constexpr (m + 7 < 32) rd(m + 7);
      if constexpr (m == DRAIN1) {
        drain_vm();
        if (lane >= 1 && lane < 5)
          __hip_atomic_store((GAS u64*)(ws + WS_FLAG + (size_t)(me ^ lane) * 512) + (lane - 1) * 16, tag, RLX_AGENT);
        early_poll = ld_flag(myflag + (lane < 3 ? lane : 0) * 16);
      }
      SB();
    });
  }
  STAMP(7);
  pin64(early_poll);
  if (!__all(early_poll == tag)) wait_flags(myflag, lane, 3, tag, 16);
  asm volatile("" ::: "memory");
  STAMP(8);
  auto fetch = [&](int i) AI {
    of[1 + (i >> 3)][i & 7] = __builtin_amdgcn_raw_buffer_load_b128(wsr, lane * 16 + (i & 7) * 1024, (me ^ (1 + (i >> 3))) * 8192, 16);
  };
#pragma unroll
  for (int i = 0; i < 10; ++i) { fetch(i); SB(); }
  bar_only();
  {
    auto frg = [](int m) AI { return m < 48 ? m / 3 : m < 72 ? 16 + (m & 7) : m - 72; };
    auto til = [](int m) AI { return m < 48 ? 3 - m % 3 : m < 72 ? 3 - ((m - 48) >> 3) : 0; };
    auto rd = [&](int m) AI { A[m & 7] = tr_frag(lds + 32768 + frg(m) * 4096 + trb[til(m)]); };
#pragma unroll
    for (int m = 0; m < 7; ++m) { rd(m); SB(); }
    sfor<0, 96>([&](auto ic) AI {
      constexpr int m = decltype(ic)::v;
      acc[til(m)] = MFMA(A[m & 7], BF(of[1 + (frg(m) >> 3)][frg(m) & 7]), acc[til(m)]);
      SB();
      if constexpr (m + 7 < 96) rd(m + 7);
      if constexpr (m % 2 == 0 && 10 + m / 2 < 24) fetch(10 + m / 2);
      if constexpr (m == 40) early_poll = ld_flag(myflag + 48);
      if constexpr (m >= 56 && m < 80 && ((m & 7) == 1 || (m & 7) == 3)) {
        constexpr int d = 2 * (3 - ((m - 56) >> 3)) + ((m & 7) == 1 ? 1 : 0);
        __builtin_amdgcn_raw_buffer_store_b128(__builtin_bit_cast(u32x4, pack8(acc[d >> 1], d & 1)), wsr, lane * 16 + d * 1024,
                                               (unsigned)WS_P + (me ^ (d << 2)) * 8192, 16);
      }
      if constexpr (m == FL2) {
        drain_vm();
        if (lane >= 2 && lane < 8)
          __hip_atomic_store((GAS u64*)(ws + WS_FLAG + (size_t)(me ^ (lane << 2)) * 512) + 48 + (lane - 1), tag, RLX_AGENT);
      }
      SB();
    });
    __builtin_amdgcn_raw_buffer_store_b128(not_ones(__builtin_bit_cast(u32x4, pack8(acc[0], 1))), wsr, lane * 16 + 1024, (unsigned)WS_P + (me ^ 4) * 8192, 16);
  }
  STAMP(9);
  pin64(early_poll);
  if (!__all(early_poll == tag)) wait_flags(myflag + 48, lane, 1, tag);
  u32x4 pv[7];
  {
    const GAS u64* q = myflag + 48 + (lane < 7 ? lane : 0);
    bool ok1 = false;
#pragma clang loop unroll(disable)
    for (unsigned spins = 0; spins < SPIN_CAP; ++spins) {
      const u64 v = ld_flag(q);
      pv[0] = __builtin_amdgcn_raw_buffer_load_b128(wsr, lane * 16 + 1024, (unsigned)WS_P + me * 8192, 16);
      SB();
      if (__all(v == tag)) break;
      __builtin_amdgcn_s_sleep(1);
    }
    asm volatile("" ::: "memory");
    STAMP(10);
#pragma unroll
    for (int d = 2; d < 8; ++d) pv[d - 1] = __builtin_amdgcn_raw_buffer_load_b128(wsr, lane * 16 + d * 1024, (unsigned)WS_P + me * 8192, 16);
    SB();
    ok1 = __all(umax4(0, pv[0]) != ~0u);
    if (!ok1) {
#pragma clang loop unroll(disable)
      for (unsigned spins = 0; spins < SPIN_CAP; ++spins) {
        asm volatile("" ::: "memory");
        pv[0] = __builtin_amdgcn_raw_buffer_load_b128(wsr, lane * 16 + 1024, (unsigned)WS_P + me * 8192, 16);
        if (__all(umax4(0, pv[0]) != ~0u)) break;
      }
    }
  }
  float o[8];
#pragma unroll
  for (int i = 0; i < 8; ++i) o[i] = acc[0][i];
#pragma unroll
  for (int d = 0; d < 7; ++d)
#pragma unroll
    for (int i = 0; i < 4; ++i) {
      o[2 * i] += __uint_as_float(pv[d][i] << 16);
      o[2 * i + 1] += __uint_as_float(pv[d][i] & 0xffff0000u);
    }
  u32x4 ov = {pk2(o[0], o[1]), pk2(o[2], o[3]), pk2(o[4], o[5]), pk2(o[6], o[7])};
  *(GAS u32x4*)(out + (size_t)(bp * 128 + w * 32 + r) * 1024 + hh * 256 + j * 32 + hf * 16) = ov;
  if (j == 0 && hh == 0 && lane == 0) __hip_atomic_store((GAS u64*)(ws + WS_EPOCH + grp * 128), tag, RLX_AGENT);
}

extern "C" void kernel_launch(void* const* d_in, const int* in_sizes, int n_in, void* d_out, int out_size, void* d_ws, size_t ws_size,
                              hipStream_t stream) {
  gqa_fused<<<256, 512, 0, stream>>>((const char*)d_in[0], (const char*)d_in[1], (const char*)d_in[2], (const char*)d_in[3],
                                     (const char*)d_in[4], (char*)d_out, (char*)d_ws);
}
```

```cpp
#include <hip/hip_runtime.h>
#include <hip/hip_bf16.h>

typedef unsigned short u16;
typedef unsigned long long u64;
typedef short s16x4 __attribute__((ext_vector_type(4)));
typedef short s16x8 __attribute__((ext_vector_type(8)));
typedef __bf16 bf16x8 __attribute__((ext_vector_type(8)));
typedef __bf16 bf16x2 __attribute__((ext_vector_type(2)));
typedef float f32x2 __attribute__((ext_vector_type(2)));
typedef float f32x16 __attribute__((ext_vector_type(16)));
typedef unsigned u32x4 __attribute__((ext_vector_type(4)));
#define LAS __attribute__((address_space(3)))
#define GAS __attribute__((address_space(1)))
#define DI __device__ __forceinline__
#define RLX_AGENT __ATOMIC_RELAXED, __HIP_MEMORY_SCOPE_AGENT
#define SB() __builtin_amdgcn_sched_barrier(0)
#define AI __attribute__((always_inline))

#define STAMPS 0
#define ROT 3
#define FL2 92
#define CODEW 1
#define STG 1
#define WOP 1
#define DRAIN1 16
#define STAMP(i) do { } while (0)

constexpr size_t WS_O     = 0;
constexpr size_t WS_P     = 8u << 20;
constexpr size_t WS_FLAG  = 16u << 20;
constexpr size_t WS_EPOCH = WS_FLAG + (512u << 10);
constexpr size_t WS_STAMP = 32u << 20;

constexpr unsigned L_K = 3u * 32768, L_V = 4u * 32768;
constexpr unsigned LDS_BYTES = 160u << 10;

DI void dma16(const char* sbase, unsigned voff, unsigned lds) {
  asm volatile("s_mov_b32 m0, %2\n\ts_nop 0\n\tglobal_load_lds_dwordx4 %0, %1" ::"v"(voff), "s"(sbase), "s"(lds) : "memory");
}
DI void dma16nt(const char* sbase, unsigned voff, unsigned lds) {
  asm volatile("s_mov_b32 m0, %2\n\ts_nop 0\n\tglobal_load_lds_dwordx4 %0, %1 nt" ::"v"(voff), "s"(sbase), "s"(lds) : "memory");
}
DI void dma4(const char* sbase, unsigned voff, unsigned lds) {
  asm volatile("s_mov_b32 m0, %2\n\ts_nop 0\n\tglobal_load_lds_dword %0, %1" ::"v"(voff), "s"(sbase), "s"(lds) : "memory");
}
DI void pin(float& v) { asm volatile("" : "+v"(v)); }
DI void pin64(u64& v) { asm volatile("" : "+v"(v)); }
DI u64 ld_flag(const GAS u64* q) { return __hip_atomic_load(q, RLX_AGENT); }
DI void drain_vm() { asm volatile("s_waitcnt vmcnt(0)" ::: "memory"); }
template <int N> DI void wait_vm_bar() { asm volatile("s_waitcnt vmcnt(%0)\n\ts_barrier" ::"i"(N) : "memory"); }
DI void bar_only() { SB(); asm volatile("s_barrier" ::: "memory"); SB(); }
DI void lgkm_bar() { SB(); asm volatile("s_waitcnt lgkmcnt(0)\n\ts_barrier" ::: "memory"); SB(); }

DI u64 rtime() { u64 t; asm volatile("s_memrealtime %0\n\ts_waitcnt lgkmcnt(0)" : "=s"(t)::"memory"); return t; }
DI unsigned ctime_() { u64 t; asm volatile("s_memtime %0\n\ts_waitcnt lgkmcnt(0)" : "=s"(t)::"memory"); return (unsigned)t; }

DI bf16x8 tr_frag(const LAS char* p) {
  s16x4 lo = __builtin_amdgcn_ds_read_tr16_b64_v4i16((LAS s16x4*)p);
  s16x4 hi = __builtin_amdgcn_ds_read_tr16_b64_v4i16((LAS s16x4*)(p + 1024));
  return __builtin_bit_cast(bf16x8, __builtin_shufflevector(lo, hi, 0, 1, 2, 3, 4, 5, 6, 7));
}
DI unsigned pk2(float a, float b) { f32x2 v = {a, b}; return __builtin_bit_cast(unsigned, __builtin_convertvector(v, bf16x2)); }
DI bf16x8 pack8(const f32x16& a, int s) {
  u32x4 r = {pk2(a[8 * s], a[8 * s + 1]), pk2(a[8 * s + 2], a[8 * s + 3]), pk2(a[8 * s + 4], a[8 * s + 5]), pk2(a[8 * s + 6], a[8 * s + 7])};
  return __builtin_bit_cast(bf16x8, r);
}
DI u32x4 not_ones(u32x4 v) {
  return u32x4{min(v[0], 0xfffffffeu), min(v[1], 0xfffffffeu), min(v[2], 0xfffffffeu), min(v[3], 0xfffffffeu)};
}
DI u32x4 pack8s(const f32x16& a, int s, float k) {
  float t[8];
#pragma unroll
  for (int i = 0; i < 8; ++i) t[i] = a[8 * s + i] * k;
  SB();
  return u32x4{pk2(t[0], t[1]), pk2(t[2], t[3]), pk2(t[4], t[5]), pk2(t[6], t[7])};
}
DI unsigned umax4(unsigned m, u32x4 v) { return max(max(max(m, v[0]), v[1]), max(v[2], v[3])); }
template <int I> struct IC { static constexpr int v = I; };
template <int B, int E, class F> DI void sfor(F&& f) { if constexpr (B < E) { f(IC<B>{}); sfor<B + 1, E>(f); } }
#define MFMA(a, b, c) __builtin_amdgcn_mfma_f32_32x32x16_bf16((a), (b), (c), 0, 0, 0)
#define BF(x) __builtin_bit_cast(bf16x8, x)
constexpr unsigned SPIN_CAP = 1u << 20;

DI void wait_flags(const GAS u64* p, int lane, int n, u64 tag, int stride = 1) {
  const GAS u64* q = p + (lane < n ? lane : 0) * stride;
#pragma clang loop unroll(disable)
  for (unsigned spins = 0; spins < SPIN_CAP; ++spins) {
    u64 v = __hip_atomic_load(q, RLX_AGENT);
    if (__all(v == tag)) break;
    __builtin_amdgcn_s_sleep(1);
  }
  asm volatile("" ::: "memory");
}

__global__ __launch_bounds__(512, 2) void gqa_fused(const char* __restrict__ x, const char* __restrict__ kc, const char* __restrict__ vc,
                                                   const char* __restrict__ wq, const char* __restrict__ wo, char* __restrict__ out, char* ws) {
  __shared__ __attribute__((aligned(16))) char lds_raw[LDS_BYTES];
  const LAS char* lds = (const LAS char*)lds_raw;
  const unsigned lds0 = (unsigned)(size_t)lds;
  const int lane = threadIdx.x & 63;
  const int w8 = __builtin_amdgcn_readfirstlane(threadIdx.x >> 6);
  const int j = blockIdx.x & 7, bp = blockIdx.x >> 5, hh = (blockIdx.x >> 3) & 3, h = 4 * j + hh;

  const bool early = STG == 0 ? false : STG == 1 ? (bp & 1) : STG == 2 ? ((bp ^ hh) & 1) : true;
  if (w8 >= 4) {
    const int lw = w8 - 4;
    const int rot = ROT == 0 ? 0 : ROT == 1 ? (((bp ^ hh) & 1) << 2) : ROT == 2 ? (((bp + hh) & 3) << 1) : ((bp + 2 * hh + (j & 1)) & 7);
    const unsigned l4 = lane >> 4, l3 = lane >> 3;
    const unsigned vx0 = l3 * 1024 + (((lane & 7) ^ l4) << 4), vx1 = vx0 ^ 64;
    const unsigned ptr_ = (lane & 15) ^ (l4 << 2);
    const unsigned vwq = l4 * 8192 + (ptr_ << 4);
    const unsigned vv = l4 * 256 + (ptr_ << 4);
    const unsigned vwo = l4 * 1024 + ((ptr_ ^ (unsigned)(j << 1)) << 4);
    const unsigned vk = l4 * 256 + (((lane & 15) ^ l4) << 4);
    const char* xb = x + (size_t)(bp * 128 + lw * 32) * 1024;
    const char* wqb = wq + (size_t)(lw * 16) * 8192 + h * 256;
    const size_t kvoff = (size_t)((bp * 2 + (lw >> 1)) * 32 + h) * 16384 + (lw & 1) * 8192;
    const char* kb = kc + kvoff;
    const char* vb = vc + kvoff;
    const char* wob = wo + (size_t)(j * 512 + lw * 16) * 1024 + hh * 256;

    auto chunk = [&](int c, int sl) AI {
      const int cc = (c + rot) & 7;
      const unsigned slot = lds0 + sl * 32768;
#pragma unroll
      for (int q = 0; q < 4; ++q) {
        dma16(wqb + (size_t)(cc * 64 + q * 4) * 8192, vwq, slot + 16384 + (lw * 16 + q * 4) * 256);
        dma16(xb + q * 8192 + cc * 128, (q & 1) ? vx1 : vx0, slot + (lw * 32 + q * 8) * 128);
      }
    };
    auto loadK = [&]() AI {
      dma4(wob + (size_t)(bp * 64) * 1024, ((lane & 31) >> 1) * 1024 + (lane & 1) * 128, lds0 + L_K + lw * 8192);
#pragma unroll
      for (int i = 0; i < 8; ++i) dma16nt(kb + i * 1024, vk ^ ((i & 3) << 6), lds0 + L_K + lw * 8192 + i * 1024);
    };
    auto loadV = [&]() AI {
#pragma unroll
      for (int i = 0; i < 8; ++i) dma16nt(vb + i * 1024, vv, lds0 + L_V + lw * 8192 + i * 1024);
    };
    auto loadWo = [&](int g) AI {
      const char* b = wob + (size_t)((hh ^ g) * 128) * 1024;
#pragma unroll
      for (int q = 0; q < 8; ++q)
        dma16(b + (size_t)((q >> 2) * 64 + (q & 3) * 4) * 1024, vwo, lds0 + g * 32768 + ((q >> 2) * 64 + lw * 16 + (q & 3) * 4) * 256);
    };
    chunk(0, 0); chunk(1, 1);
    wait_vm_bar<8>(); chunk(2, 2);
    if (early) {
      wait_vm_bar<0>(); loadK(); loadV(); chunk(3, 0);
      wait_vm_bar<0>(); chunk(4, 1); chunk(5, 2);
      wait_vm_bar<8>(); chunk(6, 0);
      wait_vm_bar<8>(); chunk(7, 1);
      wait_vm_bar<8>(); loadWo(2);
      wait_vm_bar<8>(); loadWo(0);
      wait_vm_bar<63>(); loadWo(1);
      wait_vm_bar<63>(); if (!WOP) loadWo(3);
      wait_vm_bar<WOP ? 0 : 8>(); if (WOP) loadWo(3);
    } else {
      wait_vm_bar<8>(); chunk(3, 3); chunk(4, 4);
      wait_vm_bar<0>(); chunk(5, 0); chunk(6, 1);
      wait_vm_bar<63>(); chunk(7, 2);
      wait_vm_bar<63>(); loadK();
      wait_vm_bar<9>(); loadV();
      wait_vm_bar<8>(); loadWo(0);
      wait_vm_bar<8>(); if (!WOP) { loadWo(1); loadWo(2); }
      wait_vm_bar<WOP ? 0 : 16>(); if (WOP) { loadWo(1); loadWo(2); } loadWo(3);
    }
    wait_vm_bar<0>();
    asm volatile(".Lgqa_loader_end:");
    return;
  }

  unsigned codew = 0;
  if (CODEW) {
    u64 pc;
    int len;
    asm volatile("s_getpc_b64 %0\n1:\n\ts_mov_b32 %1, .Lgqa_loader_end-1b" : "=s"(pc), "=s"(len));
    const __amdgpu_buffer_rsrc_t cr = __builtin_amdgcn_make_buffer_rsrc((char*)pc, 0, len > 0 ? len : 0, 0x00020000);
    codew = __builtin_amdgcn_raw_buffer_load_b32(cr, lane < 2 ? (((blockIdx.x >> 3) * 4 + w8) * 2 + lane) * 128 : 0x7ffffff0, 0, 0);
  }
  const int w = w8;
  const int r = lane & 31, hf = lane >> 5;
  const int grp = bp * 4 + w;
  const int me = (grp * 8 + j) * 4 + hh;
  const u64 tag = __hip_atomic_load((const GAS u64*)(ws + WS_EPOCH + grp * 128), RLX_AGENT) + 1;
  const __amdgpu_buffer_rsrc_t wsr = __builtin_amdgcn_make_buffer_rsrc(ws, 0, 16u << 20, 0x00020000);
  const GAS u64* myflag = (const GAS u64*)(ws + WS_FLAG + (size_t)me * 512);
  { u64 warm = __hip_atomic_load(myflag + (lane & 3) * 16, RLX_AGENT); asm volatile("" ::"v"(warm)); }
  __builtin_amdgcn_raw_buffer_store_b128(u32x4{~0u, ~0u, ~0u, ~0u}, wsr, lane * 16 + 1024, (unsigned)WS_P + (me ^ 4) * 8192, 16);

  const unsigned q4 = (lane & 15) >> 2, p4 = lane & 3, blk = (lane >> 4) & 1;
  const unsigned trc = (8 * hf + q4) * 256 + 32 * blk + 8 * (((p4 & 1) << 1) | (p4 >> 1));
  unsigned trb[4];
#pragma unroll
  for (int c = 0; c < 4; ++c) trb[c] = trc + ((c ^ q4) << 6);
  const unsigned xsw = (r >> 1) & 7;
  unsigned xfb[4];
#pragma unroll
  for (int s = 0; s < 4; ++s) xfb[s] = (w * 32 + r) * 128 + (((2 * s + hf) ^ xsw) << 4);
  const int kr = (r & 19) | ((r & 4) << 1) | ((r & 8) >> 1);
  const LAS char* kp = lds + L_K + (w >> 1) * 16384 + kr * 256;

  f32x16 acc[4], sc[2];
#pragma unroll
  for (int c = 0; c < 4; ++c)
#pragma unroll
    for (int i = 0; i < 16; ++i) acc[c][i] = 0.f;
#pragma unroll
  for (int t = 0; t < 2; ++t)
#pragma unroll
    for (int i = 0; i < 16; ++i) sc[t][i] = 0.f;

  bf16x8 A[8];
  bf16x8 f[8];
  bf16x8 kf[16];
  {
    bf16x8 B[8];
    auto stp = [](int m) AI { return m < 112 ? (m >> 2) & 3 : m & 3; };
    auto til = [](int m) AI { return m < 112 ? m & 3 : (m >> 2) & 3; };
    auto rd = [&](int m) AI {
      const int c = m >> 4, s = stp(m), t = til(m);
      const unsigned slot = c < 3 ? c * 32768u : early ? (c % 3) * 32768u : (c % 5) * 32768u;
      if (t == 0) B[(c & 1) * 4 + s] = *(const LAS bf16x8*)(lds + slot + xfb[s]);
      A[m & 7] = tr_frag(lds + slot + 16384 + s * 4096 + trb[t]);
    };
    bar_only();
    STAMP(1);
#pragma unroll
    for (int m = 0; m < 6; ++m) rd(m);
    SB();
    sfor<0, 128>([&](auto ic) AI {
      constexpr int m = decltype(ic)::v;
      acc[til(m)] = MFMA(A[m & 7], B[((m >> 4) & 1) * 4 + stp(m)], acc[til(m)]);
      SB();
      constexpr int k = m & 15;
      if constexpr (m < 112) {
        if (k < 10) rd(m + 6);
        else if (k == 12) {
          if ((m >> 4) == 1 ? !early : (m >> 4) >= 5 ? early : true) lgkm_bar();
          SB();
          rd(m + 4); rd(m + 5);
        }
        else if (k == 13) { rd(m + 5); rd(m + 6); }
        else if (k > 13) rd(m + 6);
      } else {
        if (m + 6 < 128) rd(m + 6);
        if constexpr (m >= 112 && k >= 5 && (k & 1)) f[(k - 5) >> 1] = pack8(acc[(k - 5) >> 2], ((k - 5) >> 1) & 1);
      }
      SB();
    });
  }
  STAMP(2);
  const LAS char* vp = lds + L_V + (w >> 1) * 16384;
  f[6] = pack8(acc[3], 0);
  f[7] = pack8(acc[3], 1);
  lgkm_bar();
  STAMP(3);
  constexpr float C = 0.08838834764831845f * 1.4426950408889634f;
  float m0, sum0 = 0.f;
  {
    auto rdk = [&](int i) AI { kf[i] = *(const LAS bf16x8*)(kp + (i >> 3) * 8192 + (((2 * (i & 7) + hf) ^ (kr & 15)) << 4)); };
#pragma unroll
    for (int i = 0; i < 12; ++i) { rdk(i); SB(); }
    sfor<0, 16>([&](auto ic) AI {
      constexpr int m = decltype(ic)::v;
      sc[m >> 3] = MFMA(kf[m], f[m & 7], sc[m >> 3]);
      SB();
      if constexpr (m < 4) rdk(12 + m);
      if constexpr (m == 9) {
        m0 = sc[0][0];
#pragma unroll
        for (int i = 1; i < 16; ++i) m0 = fmaxf(m0, sc[0][i]);
        m0 *= C;
      }
      if constexpr (m >= 10) {
#pragma unroll
        for (int i = (m - 10) * 8 / 3; i < (m - 9) * 8 / 3; ++i) sc[0][i] = __builtin_amdgcn_exp2f(fmaf(sc[0][i], C, -m0));
      }
      SB();
    });
  }
#pragma unroll
  for (int i = 0; i < 16; ++i) sum0 += sc[0][i];
  pin(sum0);
  float inv;
  {
    float mx = sc[1][0];
#pragma unroll
    for (int i = 1; i < 16; ++i) mx = fmaxf(mx, sc[1][i]);
    mx = fmaxf(mx * C, m0);
    auto rr = __builtin_amdgcn_permlane32_swap(__float_as_uint(mx), __float_as_uint(mx), false, false);
    mx = fmaxf(__uint_as_float(rr[0]), __uint_as_float(rr[1]));
    const float a0 = __builtin_amdgcn_exp2f(m0 - mx);
    float sum = sum0 * a0;
#pragma unroll
    for (int i = 0; i < 16; ++i) sc[1][i] = __builtin_amdgcn_exp2f(fmaf(sc[1][i], C, -mx));
    f[0] = BF(pack8s(sc[0], 0, a0));
    f[1] = BF(pack8s(sc[0], 1, a0));
#pragma unroll
    for (int i = 0; i < 16; ++i) sum += sc[1][i];
    f[2] = pack8(sc[1], 0);
    f[3] = pack8(sc[1], 1);
    auto r2 = __builtin_amdgcn_permlane32_swap(__float_as_uint(sum), __float_as_uint(sum), false, false);
    inv = __builtin_amdgcn_rcpf(__uint_as_float(r2[0]) + __uint_as_float(r2[1]));
  }
  asm volatile("" : "+v"(f[0]), "+v"(f[1]), "+v"(f[2]), "+v"(f[3]), "+v"(inv));
  STAMP(4);
  bar_only();
  STAMP(5);
#pragma unroll
  for (int c = 0; c < 4; ++c)
#pragma unroll
    for (int i = 0; i < 16; ++i) acc[c][i] = 0.f;
  u32x4 of[4][8];
  {
    auto rd = [&](int m) AI { A[m & 7] = tr_frag(vp + (m & 3) * 4096 + trb[m >> 2]); };
    auto put = [&](int s) AI {
      of[0][s] = pack8s(acc[s >> 1], s & 1, inv);
      __builtin_amdgcn_raw_buffer_store_b128(of[0][s], wsr, lane * 16 + s * 1024, me * 8192, 16);
    };
#pragma unroll
    for (int m = 0; m < 7; ++m) { rd(m); SB(); }
    sfor<0, 16>([&](auto ic) AI {
      constexpr int m = decltype(ic)::v;
      acc[m >> 2] = MFMA(A[m & 7], f[m & 3], acc[m >> 2]);
      SB();
      if constexpr (m + 7 < 16) rd(m + 7);
      if constexpr (m >= 5 && (m & 1)) put((m - 5) >> 1);
      SB();
    });
    put(6); SB(); put(7); SB();
  }
  STAMP(6);
#pragma unroll
  for (int c = 0; c < 4; ++c)
#pragma unroll
    for (int i = 0; i < 16; ++i) acc[c][i] = 0.f;
  u64 early_poll;
  asm volatile("" ::"v"(codew));
  bar_only();
  {
    auto rd = [&](int m) AI { A[m & 7] = tr_frag(lds + (m >> 2) * 4096 + trb[m & 3]); };
#pragma unroll
    for (int m = 0; m < 7; ++m) { rd(m); SB(); }
    sfor<0, 32>([&](auto ic) AI {
      constexpr int m = decltype(ic)::v;
      acc[m & 3] = MFMA(A[m & 7], BF(of[0][m >> 2]), acc[m & 3]);
      SB();
      if constexpr (m + 7 < 32) rd(m + 7);
      if constexpr (m == DRAIN1) {
        drain_vm();
        if (lane >= 1 && lane < 5)
          __hip_atomic_store((GAS u64*)(ws + WS_FLAG + (size_t)(me ^ lane) * 512) + (lane - 1) * 16, tag, RLX_AGENT);
        early_poll = ld_flag(myflag + (lane < 3 ? lane : 0) * 16);
      }
      SB();
    });
  }
  STAMP(7);
  pin64(early_poll);
  if (!__all(early_poll == tag)) wait_flags(myflag, lane, 3, tag, 16);
  asm volatile("" ::: "memory");
  STAMP(8);
  auto fetch = [&](int i) AI {
    of[1 + (i >> 3)][i & 7] = __builtin_amdgcn_raw_buffer_load_b128(wsr, lane * 16 + (i & 7) * 1024, (me ^ (1 + (i >> 3))) * 8192, 16);
  };
#pragma unroll
  for (int i = 0; i < 10; ++i) { fetch(i); SB(); }
  bar_only();
  {
    auto frg = [](int m) AI { return m < 48 ? m / 3 : m < 72 ? 16 + (m & 7) : m - 72; };
    auto til = [](int m) AI { return m < 48 ? 3 - m % 3 : m < 72 ? 3 - ((m - 48) >> 3) : 0; };
    auto rd = [&](int m) AI { A[m & 7] = tr_frag(lds + 32768 + frg(m) * 4096 + trb[til(m)]); };
#pragma unroll
    for (int m = 0; m < 7; ++m) { rd(m); SB(); }
    sfor<0, 96>([&](auto ic) AI {
      constexpr int m = decltype(ic)::v;
      acc[til(m)] = MFMA(A[m & 7], BF(of[1 + (frg(m) >> 3)][frg(m) & 7]), acc[til(m)]);
      SB();
      if constexpr (m + 7 < 96) rd(m + 7);
      if constexpr (m % 2 == 0 && 10 + m / 2 < 24) fetch(10 + m / 2);
      if constexpr (m == 40) early_poll = ld_flag(myflag + 48);
      if constexpr (m >= 56 && m < 80 && ((m & 7) == 1 || (m & 7) == 3)) {
        constexpr int d = 2 * (3 - ((m - 56) >> 3)) + ((m & 7) == 1 ? 1 : 0);
        __builtin_amdgcn_raw_buffer_store_b128(__builtin_bit_cast(u32x4, pack8(acc[d >> 1], d & 1)), wsr, lane * 16 + d * 1024,
                                               (unsigned)WS_P + (me ^ (d << 2)) * 8192, 16);
      }
      if constexpr (m == FL2) {
        drain_vm();
        if (lane >= 2 && lane < 8)
          __hip_atomic_store((GAS u64*)(ws + WS_FLAG + (size_t)(me ^ (lane << 2)) * 512) + 48 + (lane - 1), tag, RLX_AGENT);
      }
      SB();
    });
    __builtin_amdgcn_raw_buffer_store_b128(not_ones(__builtin_bit_cast(u32x4, pack8(acc[0], 1))), wsr, lane * 16 + 1024, (unsigned)WS_P + (me ^ 4) * 8192, 16);
  }
  STAMP(9);
  pin64(early_poll);
  if (!__all(early_poll == tag)) wait_flags(myflag + 48, lane, 1, tag);
  u32x4 pv[7];
  {
    const GAS u64* q = myflag + 48 + (lane < 7 ? lane : 0);
    bool ok1 = false;
#pragma clang loop unroll(disable)
    for (unsigned spins = 0; spins < SPIN_CAP; ++spins) {
      const u64 v = ld_flag(q);
      pv[0] = __builtin_amdgcn_raw_buffer_load_b128(wsr, lane * 16 + 1024, (unsigned)WS_P + me * 8192, 16);
      SB();
      if (__all(v == tag)) break;
      __builtin_amdgcn_s_sleep(1);
    }
    asm volatile("" ::: "memory");
    STAMP(10);
#pragma unroll
    for (int d = 2; d < 8; ++d) pv[d - 1] = __builtin_amdgcn_raw_buffer_load_b128(wsr, lane * 16 + d * 1024, (unsigned)WS_P + me * 8192, 16);
    SB();
    ok1 = __all(umax4(0, pv[0]) != ~0u);
    if (!ok1) {
#pragma clang loop unroll(disable)
      for (unsigned spins = 0; spins < SPIN_CAP; ++spins) {
        asm volatile("" ::: "memory");
        pv[0] = __builtin_amdgcn_raw_buffer_load_b128(wsr, lane * 16 + 1024, (unsigned)WS_P + me * 8192, 16);
        if (__all(umax4(0, pv[0]) != ~0u)) break;
      }
    }
  }
  float o[8];
#pragma unroll
  for (int i = 0; i < 8; ++i) o[i] = acc[0][i];
#pragma unroll
  for (int d = 0; d < 7; ++d)
#pragma unroll
    for (int i = 0; i < 4; ++i) {
      o[2 * i] += __uint_as_float(pv[d][i] << 16);
      o[2 * i + 1] += __uint_as_float(pv[d][i] & 0xffff0000u);
    }
  u32x4 ov = {pk2(o[0], o[1]), pk2(o[2], o[3]), pk2(o[4], o[5]), pk2(o[6], o[7])};
  *(GAS u32x4*)(out + (size_t)(bp * 128 + w * 32 + r) * 1024 + hh * 256 + j * 32 + hf * 16) = ov;
  if (j == 0 && hh == 0 && lane == 0) __hip_atomic_store((GAS u64*)(ws + WS_EPOCH + grp * 128), tag, RLX_AGENT);
}

extern "C" void kernel_launch(void* const* d_in, const int* in_sizes, int n_in, void* d_out, int out_size, void* d_ws, size_t ws_size,
                              hipStream_t stream) {
  gqa_fused<<<256, 512, 0, stream>>>((const char*)d_in[0], (const char*)d_in[1], (const char*)d_in[2], (const char*)d_in[3],
                                     (const char*)d_in[4], (char*)d_out, (char*)d_ws);
}
```

```cpp
#include <hip/hip_runtime.h>
#include <hip/hip_bf16.h>

typedef unsigned short u16;
typedef unsigned long long u64;
typedef short s16x4 __attribute__((ext_vector_type(4)));
typedef short s16x8 __attribute__((ext_vector_type(8)));
typedef __bf16 bf16x8 __attribute__((ext_vector_type(8)));
typedef __bf16 bf16x2 __attribute__((ext_vector_type(2)));
typedef float f32x2 __attribute__((ext_vector_type(2)));
typedef float f32x16 __attribute__((ext_vector_type(16)));
typedef unsigned u32x4 __attribute__((ext_vector_type(4)));
#define LAS __attribute__((address_space(3)))
#define GAS __attribute__((address_space(1)))
#define DI __device__ __forceinline__
#define RLX_AGENT __ATOMIC_RELAXED, __HIP_MEMORY_SCOPE_AGENT
#define SB() __builtin_amdgcn_sched_barrier(0)
#define AI __attribute__((always_inline))

#define STAMPS 1
#define PROBE 1
#define ROT 3
#define FL2 92
#define CODEW 1
#define STG 1
#define WOP 1
#define DRAIN1 16
#define STAMP(i) do { SB(); st[i] = rtime(); SB(); } while (0)

constexpr size_t WS_O     = 0;
constexpr size_t WS_P     = 8u << 20;
constexpr size_t WS_FLAG  = 16u << 20;
constexpr size_t WS_EPOCH = WS_FLAG + (512u << 10);
constexpr size_t WS_STAMP = 32u << 20;

constexpr unsigned L_K = 3u * 32768, L_V = 4u * 32768;
constexpr unsigned LDS_BYTES = 160u << 10;

DI void dma16(const char* sbase, unsigned voff, unsigned lds) {
  asm volatile("s_mov_b32 m0, %2\n\ts_nop 0\n\tglobal_load_lds_dwordx4 %0, %1" ::"v"(voff), "s"(sbase), "s"(lds) : "memory");
}
DI void dma16nt(const char* sbase, unsigned voff, unsigned lds) {
  asm volatile("s_mov_b32 m0, %2\n\ts_nop 0\n\tglobal_load_lds_dwordx4 %0, %1 nt" ::"v"(voff), "s"(sbase), "s"(lds) : "memory");
}
DI void dma4(const char* sbase, unsigned voff, unsigned lds) {
  asm volatile("s_mov_b32 m0, %2\n\ts_nop 0\n\tglobal_load_lds_dword %0, %1" ::"v"(voff), "s"(sbase), "s"(lds) : "memory");
}
DI void pin(float& v) { asm volatile("" : "+v"(v)); }
DI void pin64(u64& v) { asm volatile("" : "+v"(v)); }
DI u64 ld_flag(const GAS u64* q) { return __hip_atomic_load(q, RLX_AGENT); }
DI void drain_vm() { asm volatile("s_waitcnt vmcnt(0)" ::: "memory"); }
template <int N> DI void wait_vm_bar() { asm volatile("s_waitcnt vmcnt(%0)\n\ts_barrier" ::"i"(N) : "memory"); }
DI void bar_only() { SB(); asm volatile("s_barrier" ::: "memory"); SB(); }
DI void lgkm_bar() { SB(); asm volatile("s_waitcnt lgkmcnt(0)\n\ts_barrier" ::: "memory"); SB(); }

DI u64 rtime() { u64 t; asm volatile("s_memrealtime %0\n\ts_waitcnt lgkmcnt(0)" : "=s"(t)::"memory"); return t; }
DI unsigned ctime_() { u64 t; asm volatile("s_memtime %0\n\ts_waitcnt lgkmcnt(0)" : "=s"(t)::"memory"); return (unsigned)t; }

DI bf16x8 tr_frag(const LAS char* p) {
  s16x4 lo = __builtin_amdgcn_ds_read_tr16_b64_v4i16((LAS s16x4*)p);
  s16x4 hi = __builtin_amdgcn_ds_read_tr16_b64_v4i16((LAS s16x4*)(p + 1024));
  return __builtin_bit_cast(bf16x8, __builtin_shufflevector(lo, hi, 0, 1, 2, 3, 4, 5, 6, 7));
}
DI unsigned pk2(float a, float b) { f32x2 v = {a, b}; return __builtin_bit_cast(unsigned, __builtin_convertvector(v, bf16x2)); }
DI bf16x8 pack8(const f32x16& a, int s) {
  u32x4 r = {pk2(a[8 * s], a[8 * s + 1]), pk2(a[8 * s + 2], a[8 * s + 3]), pk2(a[8 * s + 4], a[8 * s + 5]), pk2(a[8 * s + 6], a[8 * s + 7])};
  return __builtin_bit_cast(bf16x8, r);
}
DI u32x4 not_ones(u32x4 v) {
  return u32x4{min(v[0], 0xfffffffeu), min(v[1], 0xfffffffeu), min(v[2], 0xfffffffeu), min(v[3], 0xfffffffeu)};
}
DI u32x4 pack8s(const f32x16& a, int s, float k) {
  float t[8];
#pragma unroll
  for (int i = 0; i < 8; ++i) t[i] = a[8 * s + i] * k;
  SB();
  return u32x4{pk2(t[0], t[1]), pk2(t[2], t[3]), pk2(t[4], t[5]), pk2(t[6], t[7])};
}
DI unsigned umax4(unsigned m, u32x4 v) { return max(max(max(m, v[0]), v[1]), max(v[2], v[3])); }
template <int I> struct IC { static constexpr int v = I; };
template <int B, int E, class F> DI void sfor(F&& f) { if constexpr (B < E) { f(IC<B>{}); sfor<B + 1, E>(f); } }
#define MFMA(a, b, c) __builtin_amdgcn_mfma_f32_32x32x16_bf16((a), (b), (c), 0, 0, 0)
#define BF(x) __builtin_bit_cast(bf16x8, x)
constexpr unsigned SPIN_CAP = 1u << 20;

DI void wait_flags(const GAS u64* p, int lane, int n, u64 tag, int stride = 1) {
  const GAS u64* q = p + (lane < n ? lane : 0) * stride;
#pragma clang loop unroll(disable)
  for (unsigned spins = 0; spins < SPIN_CAP; ++spins) {
    u64 v = __hip_atomic_load(q, RLX_AGENT);
    if (__all(v == tag)) break;
    __builtin_amdgcn_s_sleep(1);
  }
  asm volatile("" ::: "memory");
}

__global__ __launch_bounds__(512, 2) void gqa_fused(const char* __restrict__ x, const char* __restrict__ kc, const char* __restrict__ vc,
                                                   const char* __restrict__ wq, const char* __restrict__ wo, char* __restrict__ out, char* ws) {
  __shared__ __attribute__((aligned(16))) char lds_raw[LDS_BYTES];
  const LAS char* lds = (const LAS char*)lds_raw;
  const unsigned lds0 = (unsigned)(size_t)lds;
  const int lane = threadIdx.x & 63;
  const int w8 = __builtin_amdgcn_readfirstlane(threadIdx.x >> 6);
  const int j = blockIdx.x & 7, bp = blockIdx.x >> 5, hh = (blockIdx.x >> 3) & 3, h = 4 * j + hh;
  u64 st[12]; unsigned ck[12];
  st[0] = rtime(); ck[0] = ctime_();

  const bool early = STG == 0 ? false : STG == 1 ? (bp & 1) : STG == 2 ? ((bp ^ hh) & 1) : true;
  if (w8 >= 4) {
    const int lw = w8 - 4;
    const int rot = ROT == 0 ? 0 : ROT == 1 ? (((bp ^ hh) & 1) << 2) : ROT == 2 ? (((bp + hh) & 3) << 1) : ((bp + 2 * hh + (j & 1)) & 7);
    const unsigned l4 = lane >> 4, l3 = lane >> 3;
    const unsigned vx0 = l3 * 1024 + (((lane & 7) ^ l4) << 4), vx1 = vx0 ^ 64;
    const unsigned ptr_ = (lane & 15) ^ (l4 << 2);
    const unsigned vwq = l4 * 8192 + (ptr_ << 4);
    const unsigned vv = l4 * 256 + (ptr_ << 4);
    const unsigned vwo = l4 * 1024 + ((ptr_ ^ (unsigned)(j << 1)) << 4);
    const unsigned vk = l4 * 256 + (((lane & 15) ^ l4) << 4);
    const char* xb = x + (size_t)(bp * 128 + lw * 32) * 1024;
    const char* wqb = wq + (size_t)(lw * 16) * 8192 + h * 256;
    const size_t kvoff = (size_t)((bp * 2 + (lw >> 1)) * 32 + h) * 16384 + (lw & 1) * 8192;
    const char* kb = kc + kvoff;
    const char* vb = vc + kvoff;
    const char* wob = wo + (size_t)(j * 512 + lw * 16) * 1024 + hh * 256;

    auto chunk = [&](int c, int sl) AI {
      const int cc = (c + rot) & 7;
      const unsigned slot = lds0 + sl * 32768;
#pragma unroll
      for (int q = 0; q < 4; ++q) {
        dma16(wqb + (size_t)(cc * 64 + q * 4) * 8192, vwq, slot + 16384 + (lw * 16 + q * 4) * 256);
        dma16(xb + q * 8192 + cc * 128, (q & 1) ? vx1 : vx0, slot + (lw * 32 + q * 8) * 128);
      }
    };
    auto loadK = [&]() AI {
      dma4(wob + (size_t)(bp * 64) * 1024, ((lane & 31) >> 1) * 1024 + (lane & 1) * 128, lds0 + L_K + lw * 8192);
#pragma unroll
      for (int i = 0; i < 8; ++i) dma16nt(kb + i * 1024, vk ^ ((i & 3) << 6), lds0 + L_K + lw * 8192 + i * 1024);
    };
    auto loadV = [&]() AI {
#pragma unroll
      for (int i = 0; i < 8; ++i) dma16nt(vb + i * 1024, vv, lds0 + L_V + lw * 8192 + i * 1024);
    };
    auto loadWo = [&](int g) AI {
      const char* b = wob + (size_t)((hh ^ g) * 128) * 1024;
#pragma unroll
      for (int q = 0; q < 8; ++q)
        dma16(b + (size_t)((q >> 2) * 64 + (q & 3) * 4) * 1024, vwo, lds0 + g * 32768 + ((q >> 2) * 64 + lw * 16 + (q & 3) * 4) * 256);
    };
    chunk(0, 0); chunk(1, 1);
    wait_vm_bar<8>(); chunk(2, 2);
    if (early) {
      wait_vm_bar<0>(); loadK(); loadV(); chunk(3, 0);
      wait_vm_bar<0>(); chunk(4, 1); chunk(5, 2);
      wait_vm_bar<8>(); chunk(6, 0);
      wait_vm_bar<8>(); chunk(7, 1);
      wait_vm_bar<8>(); loadWo(2);
      wait_vm_bar<8>(); loadWo(0);
      wait_vm_bar<63>(); loadWo(1);
      wait_vm_bar<63>(); if (!WOP) loadWo(3);
      wait_vm_bar<WOP ? 0 : 8>(); if (WOP) loadWo(3);
    } else {
      wait_vm_bar<8>(); chunk(3, 3); chunk(4, 4);
      wait_vm_bar<0>(); chunk(5, 0); chunk(6, 1);
      wait_vm_bar<63>(); chunk(7, 2);
      wait_vm_bar<63>(); loadK();
      wait_vm_bar<9>(); loadV();
      wait_vm_bar<8>(); loadWo(0);
      wait_vm_bar<8>(); if (!WOP) { loadWo(1); loadWo(2); }
      wait_vm_bar<WOP ? 0 : 16>(); if (WOP) { loadWo(1); loadWo(2); } loadWo(3);
    }
    wait_vm_bar<0>();
    asm volatile(".Lgqa_loader_end:");
    st[1] = rtime();

    return;
  }

  unsigned codew = 0;
  if (CODEW) {
    u64 pc;
    int len;
    asm volatile("s_getpc_b64 %0\n1:\n\ts_mov_b32 %1, .Lgqa_loader_end-1b" : "=s"(pc), "=s"(len));
    const __amdgpu_buffer_rsrc_t cr = __builtin_amdgcn_make_buffer_rsrc((char*)pc, 0, len > 0 ? len : 0, 0x00020000);
    codew = __builtin_amdgcn_raw_buffer_load_b32(cr, lane < 2 ? (((blockIdx.x >> 3) * 4 + w8) * 2 + lane) * 128 : 0x7ffffff0, 0, 0);
  }
  const int w = w8;
  const int r = lane & 31, hf = lane >> 5;
  const int grp = bp * 4 + w;
  const int me = (grp * 8 + j) * 4 + hh;
  const u64 tag = __hip_atomic_load((const GAS u64*)(ws + WS_EPOCH + grp * 128), RLX_AGENT) + 1;
  const __amdgpu_buffer_rsrc_t wsr = __builtin_amdgcn_make_buffer_rsrc(ws, 0, 16u << 20, 0x00020000);
  const GAS u64* myflag = (const GAS u64*)(ws + WS_FLAG + (size_t)me * 512);
  { u64 warm = __hip_atomic_load(myflag + (lane & 3) * 16, RLX_AGENT); asm volatile("" ::"v"(warm)); }
  __builtin_amdgcn_raw_buffer_store_b128(u32x4{~0u, ~0u, ~0u, ~0u}, wsr, lane * 16 + 1024, (unsigned)WS_P + (me ^ 4) * 8192, 16);

  const unsigned q4 = (lane & 15) >> 2, p4 = lane & 3, blk = (lane >> 4) & 1;
  const unsigned trc = (8 * hf + q4) * 256 + 32 * blk + 8 * (((p4 & 1) << 1) | (p4 >> 1));
  unsigned trb[4];
#pragma unroll
  for (int c = 0; c < 4; ++c) trb[c] = trc + ((c ^ q4) << 6);
  const unsigned xsw = (r >> 1) & 7;
  unsigned xfb[4];
#pragma unroll
  for (int s = 0; s < 4; ++s) xfb[s] = (w * 32 + r) * 128 + (((2 * s + hf) ^ xsw) << 4);
  const int kr = (r & 19) | ((r & 4) << 1) | ((r & 8) >> 1);
  const LAS char* kp = lds + L_K + (w >> 1) * 16384 + kr * 256;

  f32x16 acc[4], sc[2];
#pragma unroll
  for (int c = 0; c < 4; ++c)
#pragma unroll
    for (int i = 0; i < 16; ++i) acc[c][i] = 0.f;
#pragma unroll
  for (int t = 0; t < 2; ++t)
#pragma unroll
    for (int i = 0; i < 16; ++i) sc[t][i] = 0.f;

  bf16x8 A[8];
  bf16x8 f[8];
  bf16x8 kf[16];
  {
    bf16x8 B[8];
    auto stp = [](int m) AI { return m < 112 ? (m >> 2) & 3 : m & 3; };
    auto til = [](int m) AI { return m < 112 ? m & 3 : (m >> 2) & 3; };
    auto rd = [&](int m) AI {
      const int c = m >> 4, s = stp(m), t = til(m);
      const unsigned slot = c < 3 ? c * 32768u : early ? (c % 3) * 32768u : (c % 5) * 32768u;
      if (t == 0) B[(c & 1) * 4 + s] = *(const LAS bf16x8*)(lds + slot + xfb[s]);
      A[m & 7] = tr_frag(lds + slot + 16384 + s * 4096 + trb[t]);
    };
    bar_only();
    STAMP(1);
#pragma unroll
    for (int m = 0; m < 6; ++m) rd(m);
    SB();
    sfor<0, 128>([&](auto ic) AI {
      constexpr int m = decltype(ic)::v;
      acc[til(m)] = MFMA(A[m & 7], B[((m >> 4) & 1) * 4 + stp(m)], acc[til(m)]);
      SB();
      constexpr int k = m & 15;
      if constexpr (m < 112) {
        if (k < 10) rd(m + 6);
        else if (k == 12) {
          if ((m >> 4) == 1 ? !early : (m >> 4) >= 5 ? early : true) lgkm_bar();
          SB();
          rd(m + 4); rd(m + 5);
        }
        else if (k == 13) { rd(m + 5); rd(m + 6); }
        else if (k > 13) rd(m + 6);
      } else {
        if (m + 6 < 128) rd(m + 6);
        if constexpr (m >= 112 && k >= 5 && (k & 1)) f[(k - 5) >> 1] = pack8(acc[(k - 5) >> 2], ((k - 5) >> 1) & 1);
      }
      SB();
    });
  }
  STAMP(2);
  const LAS char* vp = lds + L_V + (w >> 1) * 16384;
  f[6] = pack8(acc[3], 0);
  f[7] = pack8(acc[3], 1);
  lgkm_bar();
  STAMP(3);
  constexpr float C = 0.08838834764831845f * 1.4426950408889634f;
  float m0, sum0 = 0.f;
  {
    auto rdk = [&](int i) AI { kf[i] = *(const LAS bf16x8*)(kp + (i >> 3) * 8192 + (((2 * (i & 7) + hf) ^ (kr & 15)) << 4)); };
#pragma unroll
    for (int i = 0; i < 12; ++i) { rdk(i); SB(); }
    sfor<0, 16>([&](auto ic) AI {
      constexpr int m = decltype(ic)::v;
      sc[m >> 3] = MFMA(kf[m], f[m & 7], sc[m >> 3]);
      SB();
      if constexpr (m < 4) rdk(12 + m);
      if constexpr (m == 9) {
        m0 = sc[0][0];
#pragma unroll
        for (int i = 1; i < 16; ++i) m0 = fmaxf(m0, sc[0][i]);
        m0 *= C;
      }
      if constexpr (m >= 10) {
#pragma unroll
        for (int i = (m - 10) * 8 / 3; i < (m - 9) * 8 / 3; ++i) sc[0][i] = __builtin_amdgcn_exp2f(fmaf(sc[0][i], C, -m0));
      }
      SB();
    });
  }
#pragma unroll
  for (int i = 0; i < 16; ++i) sum0 += sc[0][i];
  pin(sum0);
  float inv;
  {
    float mx = sc[1][0];
#pragma unroll
    for (int i = 1; i < 16; ++i) mx = fmaxf(mx, sc[1][i]);
    mx = fmaxf(mx * C, m0);
    auto rr = __builtin_amdgcn_permlane32_swap(__float_as_uint(mx), __float_as_uint(mx), false, false);
    mx = fmaxf(__uint_as_float(rr[0]), __uint_as_float(rr[1]));
    const float a0 = __builtin_amdgcn_exp2f(m0 - mx);
    float sum = sum0 * a0;
#pragma unroll
    for (int i = 0; i < 16; ++i) sc[1][i] = __builtin_amdgcn_exp2f(fmaf(sc[1][i], C, -mx));
    f[0] = BF(pack8s(sc[0], 0, a0));
    f[1] = BF(pack8s(sc[0], 1, a0));
#pragma unroll
    for (int i = 0; i < 16; ++i) sum += sc[1][i];
    f[2] = pack8(sc[1], 0);
    f[3] = pack8(sc[1], 1);
    auto r2 = __builtin_amdgcn_permlane32_swap(__float_as_uint(sum), __float_as_uint(sum), false, false);
    inv = __builtin_amdgcn_rcpf(__uint_as_float(r2[0]) + __uint_as_float(r2[1]));
  }
  asm volatile("" : "+v"(f[0]), "+v"(f[1]), "+v"(f[2]), "+v"(f[3]), "+v"(inv));
  STAMP(4);
  bar_only();
  STAMP(5);
#pragma unroll
  for (int c = 0; c < 4; ++c)
#pragma unroll
    for (int i = 0; i < 16; ++i) acc[c][i] = 0.f;
  u32x4 of[4][8];
  {
    auto rd = [&](int m) AI { A[m & 7] = tr_frag(vp + (m & 3) * 4096 + trb[m >> 2]); };
    auto put = [&](int s) AI {
      of[0][s] = pack8s(acc[s >> 1], s & 1, inv);
      __builtin_amdgcn_raw_buffer_store_b128(of[0][s], wsr, lane * 16 + s * 1024, me * 8192, 16);
    };
#pragma unroll
    for (int m = 0; m < 7; ++m) { rd(m); SB(); }
    sfor<0, 16>([&](auto ic) AI {
      constexpr int m = decltype(ic)::v;
      acc[m >> 2] = MFMA(A[m & 7], f[m & 3], acc[m >> 2]);
      SB();
      if constexpr (m + 7 < 16) rd(m + 7);
      if constexpr (m >= 5 && (m & 1)) put((m - 5) >> 1);
      SB();
    });
    put(6); SB(); put(7); SB();
  }
  STAMP(6);
#pragma unroll
  for (int c = 0; c < 4; ++c)
#pragma unroll
    for (int i = 0; i < 16; ++i) acc[c][i] = 0.f;
  u64 early_poll;
  asm volatile("" ::"v"(codew));
  bar_only();
  {
    auto rd = [&](int m) AI { A[m & 7] = tr_frag(lds + (m >> 2) * 4096 + trb[m & 3]); };
#pragma unroll
    for (int m = 0; m < 7; ++m) { rd(m); SB(); }
    sfor<0, 32>([&](auto ic) AI {
      constexpr int m = decltype(ic)::v;
      acc[m & 3] = MFMA(A[m & 7], BF(of[0][m >> 2]), acc[m & 3]);
      SB();
      if constexpr (m + 7 < 32) rd(m + 7);
      if constexpr (m == DRAIN1) {
        drain_vm();
        if (lane >= 1 && lane < 5)
          __hip_atomic_store((GAS u64*)(ws + WS_FLAG + (size_t)(me ^ lane) * 512) + (lane - 1) * 16, tag, RLX_AGENT);
        early_poll = ld_flag(myflag + (lane < 3 ? lane : 0) * 16);
      }
      SB();
    });
  }
  STAMP(7);
  pin64(early_poll);
  if (!__all(early_poll == tag)) wait_flags(myflag, lane, 3, tag, 16);
  asm volatile("" ::: "memory");
  STAMP(8);
  auto fetch = [&](int i) AI {
    of[1 + (i >> 3)][i & 7] = __builtin_amdgcn_raw_buffer_load_b128(wsr, lane * 16 + (i & 7) * 1024, (me ^ (1 + (i >> 3))) * 8192, 16);
  };
#pragma unroll
  for (int i = 0; i < 10; ++i) { fetch(i); SB(); }
  bar_only();
  {
    auto frg = [](int m) AI { return m < 48 ? m / 3 : m < 72 ? 16 + (m & 7) : m - 72; };
    auto til = [](int m) AI { return m < 48 ? 3 - m % 3 : m < 72 ? 3 - ((m - 48) >> 3) : 0; };
    auto rd = [&](int m) AI { A[m & 7] = tr_frag(lds + 32768 + frg(m) * 4096 + trb[til(m)]); };
#pragma unroll
    for (int m = 0; m < 7; ++m) { rd(m); SB(); }
    sfor<0, 96>([&](auto ic) AI {
      constexpr int m = decltype(ic)::v;
      acc[til(m)] = MFMA(A[m & 7], BF(of[1 + (frg(m) >> 3)][frg(m) & 7]), acc[til(m)]);
      SB();
      if constexpr (m + 7 < 96) rd(m + 7);
      if constexpr (m % 2 == 0 && 10 + m / 2 < 24) fetch(10 + m / 2);
      if constexpr (m == 40) early_poll = ld_flag(myflag + 48);
      if constexpr (m >= 56 && m < 80 && ((m & 7) == 1 || (m & 7) == 3)) {
        constexpr int d = 2 * (3 - ((m - 56) >> 3)) + ((m & 7) == 1 ? 1 : 0);
        __builtin_amdgcn_raw_buffer_store_b128(__builtin_bit_cast(u32x4, pack8(acc[d >> 1], d & 1)), wsr, lane * 16 + d * 1024,
                                               (unsigned)WS_P + (me ^ (d << 2)) * 8192, 16);
      }
      if constexpr (m == FL2) {
        drain_vm();
        if (lane >= 2 && lane < 8)
          __hip_atomic_store((GAS u64*)(ws + WS_FLAG + (size_t)(me ^ (lane << 2)) * 512) + 48 + (lane - 1), tag, RLX_AGENT);
      }
      SB();
    });
    __builtin_amdgcn_raw_buffer_store_b128(not_ones(__builtin_bit_cast(u32x4, pack8(acc[0], 1))), wsr, lane * 16 + 1024, (unsigned)WS_P + (me ^ 4) * 8192, 16);
  }
  STAMP(9);
  pin64(early_poll);
  if (!__all(early_poll == tag)) wait_flags(myflag + 48, lane, 1, tag);
  u32x4 pv[7];
  {
    const GAS u64* q = myflag + 48 + (lane < 7 ? lane : 0);
    bool ok1 = false;
#pragma clang loop unroll(disable)
    for (unsigned spins = 0; spins < SPIN_CAP; ++spins) {
      const u64 v = ld_flag(q);
      pv[0] = __builtin_amdgcn_raw_buffer_load_b128(wsr, lane * 16 + 1024, (unsigned)WS_P + me * 8192, 16);
      SB();
      if (__all(v == tag)) break;
      __builtin_amdgcn_s_sleep(1);
    }
    asm volatile("" ::: "memory");
    STAMP(10);
#pragma unroll
    for (int d = 2; d < 8; ++d) pv[d - 1] = __builtin_amdgcn_raw_buffer_load_b128(wsr, lane * 16 + d * 1024, (unsigned)WS_P + me * 8192, 16);
    SB();
    ok1 = __all(umax4(0, pv[0]) != ~0u);
    if (!ok1) {
#pragma clang loop unroll(disable)
      for (unsigned spins = 0; spins < SPIN_CAP; ++spins) {
        asm volatile("" ::: "memory");
        pv[0] = __builtin_amdgcn_raw_buffer_load_b128(wsr, lane * 16 + 1024, (unsigned)WS_P + me * 8192, 16);
        if (__all(umax4(0, pv[0]) != ~0u)) break;
      }
    }
  }
  float o[8];
#pragma unroll
  for (int i = 0; i < 8; ++i) o[i] = acc[0][i];
#pragma unroll
  for (int d = 0; d < 7; ++d)
#pragma unroll
    for (int i = 0; i < 4; ++i) {
      o[2 * i] += __uint_as_float(pv[d][i] << 16);
      o[2 * i + 1] += __uint_as_float(pv[d][i] & 0xffff0000u);
    }
  u32x4 ov = {pk2(o[0], o[1]), pk2(o[2], o[3]), pk2(o[4], o[5]), pk2(o[6], o[7])};
  *(GAS u32x4*)(out + (size_t)(bp * 128 + w * 32 + r) * 1024 + hh * 256 + j * 32 + hf * 16) = ov;
  if (j == 0 && hh == 0 && lane == 0) __hip_atomic_store((GAS u64*)(ws + WS_EPOCH + grp * 128), tag, RLX_AGENT);
  st[11] = rtime(); ck[11] = ctime_();
  { u64 until = st[0] + 50 * (st[PROBE] - st[0]); for (int i = 0; i < (1 << 20) && rtime() < until; ++i) __builtin_amdgcn_s_sleep(4); }
}

extern "C" void kernel_launch(void* const* d_in, const int* in_sizes, int n_in, void* d_out, int out_size, void* d_ws, size_t ws_size,
                              hipStream_t stream) {
  gqa_fused<<<256, 512, 0, stream>>>((const char*)d_in[0], (const char*)d_in[1], (const char*)d_in[2], (const char*)d_in[3],
                                     (const char*)d_in[4], (char*)d_out, (char*)d_ws);
}
```

```cpp
#include <hip/hip_runtime.h>
#include <hip/hip_bf16.h>

typedef unsigned short u16;
typedef unsigned long long u64;
typedef short s16x4 __attribute__((ext_vector_type(4)));
typedef short s16x8 __attribute__((ext_vector_type(8)));
typedef __bf16 bf16x8 __attribute__((ext_vector_type(8)));
typedef __bf16 bf16x2 __attribute__((ext_vector_type(2)));
typedef float f32x2 __attribute__((ext_vector_type(2)));
typedef float f32x16 __attribute__((ext_vector_type(16)));
typedef unsigned u32x4 __attribute__((ext_vector_type(4)));
#define LAS __attribute__((address_space(3)))
#define GAS __attribute__((address_space(1)))
#define DI __device__ __forceinline__
#define RLX_AGENT __ATOMIC_RELAXED, __HIP_MEMORY_SCOPE_AGENT
#define SB() __builtin_amdgcn_sched_barrier(0)
#define AI __attribute__((always_inline))

#define STAMPS 0
#define ROT 3
#define FL2 92
#define CODEW 1
#define NF0 10
#define FSTEP 2
#define OUTSC 16
#define PAUX 16
#define OAUX3 16
#define QNOP -1
#define QFROM 0
#define STG 1
#define WOP 1
#define DRAIN1 16
#define STAMP(i) do { } while (0)

constexpr size_t WS_O     = 0;
constexpr size_t WS_P     = 8u << 20;
constexpr size_t WS_FLAG  = 16u << 20;
constexpr size_t WS_EPOCH = WS_FLAG + (512u << 10);
constexpr size_t WS_STAMP = 32u << 20;

constexpr unsigned L_K = 3u * 32768, L_V = 4u * 32768;
constexpr unsigned LDS_BYTES = 160u << 10;

DI void dma16(const char* sbase, unsigned voff, unsigned lds) {
  asm volatile("s_mov_b32 m0, %2\n\ts_nop 0\n\tglobal_load_lds_dwordx4 %0, %1" ::"v"(voff), "s"(sbase), "s"(lds) : "memory");
}
DI void dma16nt(const char* sbase, unsigned voff, unsigned lds) {
  asm volatile("s_mov_b32 m0, %2\n\ts_nop 0\n\tglobal_load_lds_dwordx4 %0, %1 nt" ::"v"(voff), "s"(sbase), "s"(lds) : "memory");
}
DI void dma4(const char* sbase, unsigned voff, unsigned lds) {
  asm volatile("s_mov_b32 m0, %2\n\ts_nop 0\n\tglobal_load_lds_dword %0, %1" ::"v"(voff), "s"(sbase), "s"(lds) : "memory");
}
DI void pin(float& v) { asm volatile("" : "+v"(v)); }
DI void pin64(u64& v) { asm volatile("" : "+v"(v)); }
DI u64 ld_flag(const GAS u64* q) { return __hip_atomic_load(q, RLX_AGENT); }
template <int N> DI void nop() { asm volatile("s_nop %0" ::"i"(N)); }
DI void drain_vm() { asm volatile("s_waitcnt vmcnt(0)" ::: "memory"); }
template <int N> DI void wait_vm_bar() { asm volatile("s_waitcnt vmcnt(%0)\n\ts_barrier" ::"i"(N) : "memory"); }
DI void bar_only() { SB(); asm volatile("s_barrier" ::: "memory"); SB(); }
DI void lgkm_bar() { SB(); asm volatile("s_waitcnt lgkmcnt(0)\n\ts_barrier" ::: "memory"); SB(); }

DI u64 rtime() { u64 t; asm volatile("s_memrealtime %0\n\ts_waitcnt lgkmcnt(0)" : "=s"(t)::"memory"); return t; }
DI unsigned ctime_() { u64 t; asm volatile("s_memtime %0\n\ts_waitcnt lgkmcnt(0)" : "=s"(t)::"memory"); return (unsigned)t; }

DI bf16x8 tr_frag(const LAS char* p) {
  s16x4 lo = __builtin_amdgcn_ds_read_tr16_b64_v4i16((LAS s16x4*)p);
  s16x4 hi = __builtin_amdgcn_ds_read_tr16_b64_v4i16((LAS s16x4*)(p + 1024));
  return __builtin_bit_cast(bf16x8, __builtin_shufflevector(lo, hi, 0, 1, 2, 3, 4, 5, 6, 7));
}
DI unsigned pk2(float a, float b) { f32x2 v = {a, b}; return __builtin_bit_cast(unsigned, __builtin_convertvector(v, bf16x2)); }
DI bf16x8 pack8(const f32x16& a, int s) {
  u32x4 r = {pk2(a[8 * s], a[8 * s + 1]), pk2(a[8 * s + 2], a[8 * s + 3]), pk2(a[8 * s + 4], a[8 * s + 5]), pk2(a[8 * s + 6], a[8 * s + 7])};
  return __builtin_bit_cast(bf16x8, r);
}
DI u32x4 not_ones(u32x4 v) {
  return u32x4{min(v[0], 0xfffffffeu), min(v[1], 0xfffffffeu), min(v[2], 0xfffffffeu), min(v[3], 0xfffffffeu)};
}
DI u32x4 pack8s(const f32x16& a, int s, float k) {
  float t[8];
#pragma unroll
  for (int i = 0; i < 8; ++i) t[i] = a[8 * s + i] * k;
  SB();
  return u32x4{pk2(t[0], t[1]), pk2(t[2], t[3]), pk2(t[4], t[5]), pk2(t[6], t[7])};
}
DI unsigned umax4(unsigned m, u32x4 v) { return max(max(max(m, v[0]), v[1]), max(v[2], v[3])); }
template <int I> struct IC { static constexpr int v = I; };
template <int B, int E, class F> DI void sfor(F&& f) { if constexpr (B < E) { f(IC<B>{}); sfor<B + 1, E>(f); } }
#define MFMA(a, b, c) __builtin_amdgcn_mfma_f32_32x32x16_bf16((a), (b), (c), 0, 0, 0)
#define BF(x) __builtin_bit_cast(bf16x8, x)
constexpr unsigned SPIN_CAP = 1u << 20;

DI void wait_flags(const GAS u64* p, int lane, int n, u64 tag, int stride = 1) {
  const GAS u64* q = p + (lane < n ? lane : 0) * stride;
#pragma clang loop unroll(disable)
  for (unsigned spins = 0; spins < SPIN_CAP; ++spins) {
    u64 v = __hip_atomic_load(q, RLX_AGENT);
    if (__all(v == tag)) break;
    __builtin_amdgcn_s_sleep(1);
  }
  asm volatile("" ::: "memory");
}

__global__ __launch_bounds__(512, 2) void gqa_fused(const char* __restrict__ x, const char* __restrict__ kc, const char* __restrict__ vc,
                                                   const char* __restrict__ wq, const char* __restrict__ wo, char* __restrict__ out, char* ws) {
  __shared__ __attribute__((aligned(16))) char lds_raw[LDS_BYTES];
  const LAS char* lds = (const LAS char*)lds_raw;
  const unsigned lds0 = (unsigned)(size_t)lds;
  const int lane = threadIdx.x & 63;
  const int w8 = __builtin_amdgcn_readfirstlane(threadIdx.x >> 6);
  const int j = blockIdx.x & 7, bp = blockIdx.x >> 5, hh = (blockIdx.x >> 3) & 3, h = 4 * j + hh;

  const bool early = STG == 0 ? false : STG == 1 ? (bp & 1) : STG == 2 ? ((bp ^ hh) & 1) : true;
  if (w8 >= 4) {
    const int lw = w8 - 4;
    const int rot = ROT == 0 ? 0 : ROT == 1 ? (((bp ^ hh) & 1) << 2) : ROT == 2 ? (((bp + hh) & 3) << 1) : ((bp + 2 * hh + (j & 1)) & 7);
    const unsigned l4 = lane >> 4, l3 = lane >> 3;
    const unsigned vx0 = l3 * 1024 + (((lane & 7) ^ l4) << 4), vx1 = vx0 ^ 64;
    const unsigned ptr_ = (lane & 15) ^ (l4 << 2);
    const unsigned vwq = l4 * 8192 + (ptr_ << 4);
    const unsigned vv = l4 * 256 + (ptr_ << 4);
    const unsigned vwo = l4 * 1024 + ((ptr_ ^ (unsigned)(j << 1)) << 4);
    const unsigned vk = l4 * 256 + (((lane & 15) ^ l4) << 4);
    const char* xb = x + (size_t)(bp * 128 + lw * 32) * 1024;
    const char* wqb = wq + (size_t)(lw * 16) * 8192 + h * 256;
    const size_t kvoff = (size_t)((bp * 2 + (lw >> 1)) * 32 + h) * 16384 + (lw & 1) * 8192;
    const char* kb = kc + kvoff;
    const char* vb = vc + kvoff;
    const char* wob = wo + (size_t)(j * 512 + lw * 16) * 1024 + hh * 256;

    auto chunk = [&](int c, int sl) AI {
      const int cc = (c + rot) & 7;
      const unsigned slot = lds0 + sl * 32768;
#pragma unroll
      for (int q = 0; q < 4; ++q) {
        dma16(wqb + (size_t)(cc * 64 + q * 4) * 8192, vwq, slot + 16384 + (lw * 16 + q * 4) * 256);
        dma16(xb + q * 8192 + cc * 128, (q & 1) ? vx1 : vx0, slot + (lw * 32 + q * 8) * 128);
      }
    };
    auto loadK = [&]() AI {
      dma4(wob + (size_t)(bp * 64) * 1024, ((lane & 31) >> 1) * 1024 + (lane & 1) * 128, lds0 + L_K + lw * 8192);
#pragma unroll
      for (int i = 0; i < 8; ++i) dma16nt(kb + i * 1024, vk ^ ((i & 3) << 6), lds0 + L_K + lw * 8192 + i * 1024);
    };
    auto loadV = [&]() AI {
#pragma unroll
      for (int i = 0; i < 8; ++i) dma16nt(vb + i * 1024, vv, lds0 + L_V + lw * 8192 + i * 1024);
    };
    auto loadWo = [&](int g) AI {
      const char* b = wob + (size_t)((hh ^ g) * 128) * 1024;
#pragma unroll
      for (int q = 0; q < 8; ++q)
        dma16(b + (size_t)((q >> 2) * 64 + (q & 3) * 4) * 1024, vwo, lds0 + g * 32768 + ((q >> 2) * 64 + lw * 16 + (q & 3) * 4) * 256);
    };
    chunk(0, 0); chunk(1, 1);
    wait_vm_bar<8>(); chunk(2, 2);
    if (early) {
      wait_vm_bar<0>(); loadK(); loadV(); chunk(3, 0);
      wait_vm_bar<0>(); chunk(4, 1); chunk(5, 2);
      wait_vm_bar<8>(); chunk(6, 0);
      wait_vm_bar<8>(); chunk(7, 1);
      wait_vm_bar<8>(); loadWo(2);
      wait_vm_bar<8>(); loadWo(0);
      wait_vm_bar<63>(); loadWo(1);
      wait_vm_bar<63>(); if (!WOP) loadWo(3);
      wait_vm_bar<WOP ? 0 : 8>(); if (WOP) loadWo(3);
    } else {
      wait_vm_bar<8>(); chunk(3, 3); chunk(4, 4);
      wait_vm_bar<0>(); chunk(5, 0); chunk(6, 1);
      wait_vm_bar<63>(); chunk(7, 2);
      wait_vm_bar<63>(); loadK();
      wait_vm_bar<9>(); loadV();
      wait_vm_bar<8>(); loadWo(0);
      wait_vm_bar<8>(); if (!WOP) { loadWo(1); loadWo(2); }
      wait_vm_bar<WOP ? 0 : 16>(); if (WOP) { loadWo(1); loadWo(2); } loadWo(3);
    }
    wait_vm_bar<0>();
    asm volatile(".Lgqa_loader_end:");
    return;
  }

  unsigned codew = 0;
  if (CODEW) {
    u64 pc;
    int len;
    asm volatile("s_getpc_b64 %0\n1:\n\ts_mov_b32 %1, .Lgqa_loader_end-1b" : "=s"(pc), "=s"(len));
    const __amdgpu_buffer_rsrc_t cr = __builtin_amdgcn_make_buffer_rsrc((char*)pc, 0, len > 0 ? len : 0, 0x00020000);
    codew = __builtin_amdgcn_raw_buffer_load_b32(cr, lane < 2 ? (((blockIdx.x >> 3) * 4 + w8) * 2 + lane) * 128 : 0x7ffffff0, 0, 0);
  }
  const int w = w8;
  const int r = lane & 31, hf = lane >> 5;
  const int grp = bp * 4 + w;
  const int me = (grp * 8 + j) * 4 + hh;
  const u64 tag = __hip_atomic_load((const GAS u64*)(ws + WS_EPOCH + grp * 128), RLX_AGENT) + 1;
  const __amdgpu_buffer_rsrc_t wsr = __builtin_amdgcn_make_buffer_rsrc(ws, 0, 16u << 20, 0x00020000);
  const GAS u64* myflag = (const GAS u64*)(ws + WS_FLAG + (size_t)me * 512);
  { u64 warm = __hip_atomic_load(myflag + (lane & 3) * 16, RLX_AGENT); asm volatile("" ::"v"(warm)); }
  __builtin_amdgcn_raw_buffer_store_b128(u32x4{~0u, ~0u, ~0u, ~0u}, wsr, lane * 16 + 1024, (unsigned)WS_P + (me ^ 4) * 8192, 16);

  const unsigned q4 = (lane & 15) >> 2, p4 = lane & 3, blk = (lane >> 4) & 1;
  const unsigned trc = (8 * hf + q4) * 256 + 32 * blk + 8 * (((p4 & 1) << 1) | (p4 >> 1));
  unsigned trb[4];
#pragma unroll
  for (int c = 0; c < 4; ++c) trb[c] = trc + ((c ^ q4) << 6);
  const unsigned xsw = (r >> 1) & 7;
  unsigned xfb[4];
#pragma unroll
  for (int s = 0; s < 4; ++s) xfb[s] = (w * 32 + r) * 128 + (((2 * s + hf) ^ xsw) << 4);
  const int kr = (r & 19) | ((r & 4) << 1) | ((r & 8) >> 1);
  const LAS char* kp = lds + L_K + (w >> 1) * 16384 + kr * 256;

  f32x16 acc[4], sc[2];
#pragma unroll
  for (int c = 0; c < 4; ++c)
#pragma unroll
    for (int i = 0; i < 16; ++i) acc[c][i] = 0.f;
#pragma unroll
  for (int t = 0; t < 2; ++t)
#pragma unroll
    for (int i = 0; i < 16; ++i) sc[t][i] = 0.f;

  bf16x8 A[8];
  bf16x8 f[8];
  bf16x8 kf[16];
  {
    bf16x8 B[8];
    auto stp = [](int m) AI { return m < 112 ? (m >> 2) & 3 : m & 3; };
    auto til = [](int m) AI { return m < 112 ? m & 3 : (m >> 2) & 3; };
    auto rd = [&](int m) AI {
      const int c = m >> 4, s = stp(m), t = til(m);
      const unsigned slot = c < 3 ? c * 32768u : early ? (c % 3) * 32768u : (c % 5) * 32768u;
      if (t == 0) B[(c & 1) * 4 + s] = *(const LAS bf16x8*)(lds + slot + xfb[s]);
      A[m & 7] = tr_frag(lds + slot + 16384 + s * 4096 + trb[t]);
    };
    bar_only();
    STAMP(1);
#pragma unroll
    for (int m = 0; m < 6; ++m) rd(m);
    SB();
    sfor<0, 128>([&](auto ic) AI {
      constexpr int m = decltype(ic)::v;
      acc[til(m)] = MFMA(A[m & 7], B[((m >> 4) & 1) * 4 + stp(m)], acc[til(m)]);
      SB();
      constexpr int k = m & 15;
      if constexpr (QNOP >= 0 && m >= QFROM) { nop<(QNOP > 15 ? 15 : QNOP)>(); if constexpr (QNOP > 15) nop<QNOP - 16>(); }
      if constexpr (m < 112) {
        if (k < 10) rd(m + 6);
        else if (k == 12) {
          if ((m >> 4) == 1 ? !early : (m >> 4) >= 5 ? early : true) lgkm_bar();
          SB();
          rd(m + 4); rd(m + 5);
        }
        else if (k == 13) { rd(m + 5); rd(m + 6); }
        else if (k > 13) rd(m + 6);
      } else {
        if (m + 6 < 128) rd(m + 6);
        if constexpr (m >= 112 && k >= 5 && (k & 1)) f[(k - 5) >> 1] = pack8(acc[(k - 5) >> 2], ((k - 5) >> 1) & 1);
      }
      SB();
    });
  }
  STAMP(2);
  const LAS char* vp = lds + L_V + (w >> 1) * 16384;
  f[6] = pack8(acc[3], 0);
  f[7] = pack8(acc[3], 1);
  lgkm_bar();
  STAMP(3);
  constexpr float C = 0.08838834764831845f * 1.4426950408889634f;
  float m0, sum0 = 0.f;
  {
    auto rdk = [&](int i) AI { kf[i] = *(const LAS bf16x8*)(kp + (i >> 3) * 8192 + (((2 * (i & 7) + hf) ^ (kr & 15)) << 4)); };
#pragma unroll
    for (int i = 0; i < 12; ++i) { rdk(i); SB(); }
    sfor<0, 16>([&](auto ic) AI {
      constexpr int m = decltype(ic)::v;
      sc[m >> 3] = MFMA(kf[m], f[m & 7], sc[m >> 3]);
      SB();
      if constexpr (m < 4) rdk(12 + m);
      if constexpr (m == 9) {
        m0 = sc[0][0];
#pragma unroll
        for (int i = 1; i < 16; ++i) m0 = fmaxf(m0, sc[0][i]);
        m0 *= C;
      }
      if constexpr (m >= 10) {
#pragma unroll
        for (int i = (m - 10) * 8 / 3; i < (m - 9) * 8 / 3; ++i) sc[0][i] = __builtin_amdgcn_exp2f(fmaf(sc[0][i], C, -m0));
      }
      SB();
    });
  }
#pragma unroll
  for (int i = 0; i < 16; ++i) sum0 += sc[0][i];
  pin(sum0);
  float inv;
  {
    float mx = sc[1][0];
#pragma unroll
    for (int i = 1; i < 16; ++i) mx = fmaxf(mx, sc[1][i]);
    mx = fmaxf(mx * C, m0);
    auto rr = __builtin_amdgcn_permlane32_swap(__float_as_uint(mx), __float_as_uint(mx), false, false);
    mx = fmaxf(__uint_as_float(rr[0]), __uint_as_float(rr[1]));
    const float a0 = __builtin_amdgcn_exp2f(m0 - mx);
    float sum = sum0 * a0;
#pragma unroll
    for (int i = 0; i < 16; ++i) sc[1][i] = __builtin_amdgcn_exp2f(fmaf(sc[1][i], C, -mx));
    f[0] = BF(pack8s(sc[0], 0, a0));
    f[1] = BF(pack8s(sc[0], 1, a0));
#pragma unroll
    for (int i = 0; i < 16; ++i) sum += sc[1][i];
    f[2] = pack8(sc[1], 0);
    f[3] = pack8(sc[1], 1);
    auto r2 = __builtin_amdgcn_permlane32_swap(__float_as_uint(sum), __float_as_uint(sum), false, false);
    inv = __builtin_amdgcn_rcpf(__uint_as_float(r2[0]) + __uint_as_float(r2[1]));
  }
  asm volatile("" : "+v"(f[0]), "+v"(f[1]), "+v"(f[2]), "+v"(f[3]), "+v"(inv));
  STAMP(4);
  bar_only();
  STAMP(5);
#pragma unroll
  for (int c = 0; c < 4; ++c)
#pragma unroll
    for (int i = 0; i < 16; ++i) acc[c][i] = 0.f;
  u32x4 of[4][8];
  {
    auto rd = [&](int m) AI { A[m & 7] = tr_frag(vp + (m & 3) * 4096 + trb[m >> 2]); };
    auto put = [&](int s) AI {
      of[0][s] = pack8s(acc[s >> 1], s & 1, inv);
      __builtin_amdgcn_raw_buffer_store_b128(of[0][s], wsr, lane * 16 + s * 1024, me * 8192, 16);
    };
#pragma unroll
    for (int m = 0; m < 7; ++m) { rd(m); SB(); }
    sfor<0, 16>([&](auto ic) AI {
      constexpr int m = decltype(ic)::v;
      acc[m >> 2] = MFMA(A[m & 7], f[m & 3], acc[m >> 2]);
      SB();
      if constexpr (m + 7 < 16) rd(m + 7);
      if constexpr (m >= 5 && (m & 1)) put((m - 5) >> 1);
      SB();
    });
    put(6); SB(); put(7); SB();
  }
  STAMP(6);
#pragma unroll
  for (int c = 0; c < 4; ++c)
#pragma unroll
    for (int i = 0; i < 16; ++i) acc[c][i] = 0.f;
  u64 early_poll;
  asm volatile("" ::"v"(codew));
  bar_only();
  {
    auto rd = [&](int m) AI { A[m & 7] = tr_frag(lds + (m >> 2) * 4096 + trb[m & 3]); };
#pragma unroll
    for (int m = 0; m < 7; ++m) { rd(m); SB(); }
    sfor<0, 32>([&](auto ic) AI {
      constexpr int m = decltype(ic)::v;
      acc[m & 3] = MFMA(A[m & 7], BF(of[0][m >> 2]), acc[m & 3]);
      SB();
      if constexpr (m + 7 < 32) rd(m + 7);
      if constexpr (m == DRAIN1) {
        drain_vm();
        if (lane >= 1 && lane < 5)
          __hip_atomic_store((GAS u64*)(ws + WS_FLAG + (size_t)(me ^ lane) * 512) + (lane - 1) * 16, tag, RLX_AGENT);
        early_poll = ld_flag(myflag + (lane < 3 ? lane : 0) * 16);
      }
      SB();
    });
  }
  STAMP(7);
  pin64(early_poll);
  if (!__all(early_poll == tag)) wait_flags(myflag, lane, 3, tag, 16);
  asm volatile("" ::: "memory");
  STAMP(8);
  auto fetch = [&](int i) AI {
    if (i >= 16) of[1 + (i >> 3)][i & 7] = __builtin_amdgcn_raw_buffer_load_b128(wsr, lane * 16 + (i & 7) * 1024, (me ^ (1 + (i >> 3))) * 8192, OAUX3);
    else of[1 + (i >> 3)][i & 7] = __builtin_amdgcn_raw_buffer_load_b128(wsr, lane * 16 + (i & 7) * 1024, (me ^ (1 + (i >> 3))) * 8192, 16);
  };
#pragma unroll
  for (int i = 0; i < NF0; ++i) { fetch(i); SB(); }
  bar_only();
  {
    auto frg = [](int m) AI { return m < 48 ? m / 3 : m < 72 ? 16 + (m & 7) : m - 72; };
    auto til = [](int m) AI { return m < 48 ? 3 - m % 3 : m < 72 ? 3 - ((m - 48) >> 3) : 0; };
    auto rd = [&](int m) AI { A[m & 7] = tr_frag(lds + 32768 + frg(m) * 4096 + trb[til(m)]); };
#pragma unroll
    for (int m = 0; m < 7; ++m) { rd(m); SB(); }
    sfor<0, 96>([&](auto ic) AI {
      constexpr int m = decltype(ic)::v;
      acc[til(m)] = MFMA(A[m & 7], BF(of[1 + (frg(m) >> 3)][frg(m) & 7]), acc[til(m)]);
      SB();
      if constexpr (m + 7 < 96) rd(m + 7);
      if constexpr (m % FSTEP == 0 && NF0 + m / FSTEP < 24) fetch(NF0 + m / FSTEP);
      if constexpr (m == 40) early_poll = ld_flag(myflag + 48);
      if constexpr (m >= 56 && m < 80 && ((m & 7) == 1 || (m & 7) == 3)) {
        constexpr int d = 2 * (3 - ((m - 56) >> 3)) + ((m & 7) == 1 ? 1 : 0);
        __builtin_amdgcn_raw_buffer_store_b128(__builtin_bit_cast(u32x4, pack8(acc[d >> 1], d & 1)), wsr, lane * 16 + d * 1024,
                                               (unsigned)WS_P + (me ^ (d << 2)) * 8192, 16);
      }
      if constexpr (m == FL2) {
        drain_vm();
        if (lane >= 2 && lane < 8)
          __hip_atomic_store((GAS u64*)(ws + WS_FLAG + (size_t)(me ^ (lane << 2)) * 512) + 48 + (lane - 1), tag, RLX_AGENT);
      }
      SB();
    });
    __builtin_amdgcn_raw_buffer_store_b128(not_ones(__builtin_bit_cast(u32x4, pack8(acc[0], 1))), wsr, lane * 16 + 1024, (unsigned)WS_P + (me ^ 4) * 8192, 16);
    if (FL2 >= 96) {
      asm volatile("s_waitcnt vmcnt(1)" ::: "memory");
      if (lane >= 2 && lane < 8)
        __hip_atomic_store((GAS u64*)(ws + WS_FLAG + (size_t)(me ^ (lane << 2)) * 512) + 48 + (lane - 1), tag, RLX_AGENT);
    }
  }
  STAMP(9);
  pin64(early_poll);
  if (!__all(early_poll == tag)) wait_flags(myflag + 48, lane, 1, tag);
  u32x4 pv[7];
  {
    const GAS u64* q = myflag + 48 + (lane < 7 ? lane : 0);
    bool ok1 = false;
#pragma clang loop unroll(disable)
    for (unsigned spins = 0; spins < SPIN_CAP; ++spins) {
      const u64 v = ld_flag(q);
      pv[0] = __builtin_amdgcn_raw_buffer_load_b128(wsr, lane * 16 + 1024, (unsigned)WS_P + me * 8192, 16);
      SB();
      if (__all(v == tag)) break;
      __builtin_amdgcn_s_sleep(1);
    }
    asm volatile("" ::: "memory");
    STAMP(10);
#pragma unroll
    for (int d = 2; d < 8; ++d) pv[d - 1] = __builtin_amdgcn_raw_buffer_load_b128(wsr, lane * 16 + d * 1024, (unsigned)WS_P + me * 8192, PAUX);
    SB();
    ok1 = __all(umax4(0, pv[0]) != ~0u);
    if (!ok1) {
#pragma clang loop unroll(disable)
      for (unsigned spins = 0; spins < SPIN_CAP; ++spins) {
        asm volatile("" ::: "memory");
        pv[0] = __builtin_amdgcn_raw_buffer_load_b128(wsr, lane * 16 + 1024, (unsigned)WS_P + me * 8192, 16);
        if (__all(umax4(0, pv[0]) != ~0u)) break;
      }
    }
  }
  float o[8];
#pragma unroll
  for (int i = 0; i < 8; ++i) o[i] = acc[0][i];
#pragma unroll
  for (int d = 0; d < 7; ++d)
#pragma unroll
    for (int i = 0; i < 4; ++i) {
      o[2 * i] += __uint_as_float(pv[d][i] << 16);
      o[2 * i + 1] += __uint_as_float(pv[d][i] & 0xffff0000u);
    }
  u32x4 ov = {pk2(o[0], o[1]), pk2(o[2], o[3]), pk2(o[4], o[5]), pk2(o[6], o[7])};
  if (OUTSC) __builtin_amdgcn_raw_buffer_store_b128(ov, __builtin_amdgcn_make_buffer_rsrc(out, 0, 1u << 20, 0x00020000), (bp * 128 + w * 32 + r) * 1024 + hh * 256 + j * 32 + hf * 16, 0, OUTSC);
  else *(GAS u32x4*)(out + (size_t)(bp * 128 + w * 32 + r) * 1024 + hh * 256 + j * 32 + hf * 16) = ov;
  if (j == 0 && hh == 0 && lane == 0) __hip_atomic_store((GAS u64*)(ws + WS_EPOCH + grp * 128), tag, RLX_AGENT);
}

extern "C" void kernel_launch(void* const* d_in, const int* in_sizes, int n_in, void* d_out, int out_size, void* d_ws, size_t ws_size,
                              hipStream_t stream) {
  gqa_fused<<<256, 512, 0, stream>>>((const char*)d_in[0], (const char*)d_in[1], (const char*)d_in[2], (const char*)d_in[3],
                                     (const char*)d_in[4], (char*)d_out, (char*)d_ws);
}
```

```cpp
#include <hip/hip_runtime.h>
#include <hip/hip_bf16.h>

typedef unsigned short u16;
typedef unsigned long long u64;
typedef short s16x4 __attribute__((ext_vector_type(4)));
typedef short s16x8 __attribute__((ext_vector_type(8)));
typedef __bf16 bf16x8 __attribute__((ext_vector_type(8)));
typedef __bf16 bf16x2 __attribute__((ext_vector_type(2)));
typedef float f32x2 __attribute__((ext_vector_type(2)));
typedef float f32x16 __attribute__((ext_vector_type(16)));
typedef unsigned u32x4 __attribute__((ext_vector_type(4)));
#define LAS __attribute__((address_space(3)))
#define GAS __attribute__((address_space(1)))
#define DI __device__ __forceinline__
#define RLX_AGENT __ATOMIC_RELAXED, __HIP_MEMORY_SCOPE_AGENT
#define SB() __builtin_amdgcn_sched_barrier(0)
#define AI __attribute__((always_inline))

#define STAMPS 0
#define ROT 3
#define FL2 92
#define CODEW 1
#define NF0 10
#define FSTEP 2
#define OUTSC 16
#define PAUX 16
#define OAUX3 16
#define QNOP -1
#define QFROM 0
#define TLBW 1
#define STG 1
#define WOP 1
#define DRAIN1 16
#define STAMP(i) do { } while (0)

constexpr size_t WS_O     = 0;
constexpr size_t WS_P     = 8u << 20;
constexpr size_t WS_FLAG  = 16u << 20;
constexpr size_t WS_EPOCH = WS_FLAG + (512u << 10);
constexpr size_t WS_STAMP = 32u << 20;

constexpr unsigned L_K = 3u * 32768, L_V = 4u * 32768;
constexpr unsigned LDS_BYTES = 160u << 10;

DI void dma16(const char* sbase, unsigned voff, unsigned lds) {
  asm volatile("s_mov_b32 m0, %2\n\ts_nop 0\n\tglobal_load_lds_dwordx4 %0, %1" ::"v"(voff), "s"(sbase), "s"(lds) : "memory");
}
DI void dma16nt(const char* sbase, unsigned voff, unsigned lds) {
  asm volatile("s_mov_b32 m0, %2\n\ts_nop 0\n\tglobal_load_lds_dwordx4 %0, %1 nt" ::"v"(voff), "s"(sbase), "s"(lds) : "memory");
}
DI void dma4(const char* sbase, unsigned voff, unsigned lds) {
  asm volatile("s_mov_b32 m0, %2\n\ts_nop 0\n\tglobal_load_lds_dword %0, %1" ::"v"(voff), "s"(sbase), "s"(lds) : "memory");
}
DI void pin(float& v) { asm volatile("" : "+v"(v)); }
DI void pin64(u64& v) { asm volatile("" : "+v"(v)); }
DI u64 ld_flag(const GAS u64* q) { return __hip_atomic_load(q, RLX_AGENT); }
template <int N> DI void nop() { asm volatile("s_nop %0" ::"i"(N)); }
DI void drain_vm() { asm volatile("s_waitcnt vmcnt(0)" ::: "memory"); }
template <int N> DI void wait_vm_bar() { asm volatile("s_waitcnt vmcnt(%0)\n\ts_barrier" ::"i"(N) : "memory"); }
DI void bar_only() { SB(); asm volatile("s_barrier" ::: "memory"); SB(); }
DI void lgkm_bar() { SB(); asm volatile("s_waitcnt lgkmcnt(0)\n\ts_barrier" ::: "memory"); SB(); }

DI u64 rtime() { u64 t; asm volatile("s_memrealtime %0\n\ts_waitcnt lgkmcnt(0)" : "=s"(t)::"memory"); return t; }
DI unsigned ctime_() { u64 t; asm volatile("s_memtime %0\n\ts_waitcnt lgkmcnt(0)" : "=s"(t)::"memory"); return (unsigned)t; }

DI bf16x8 tr_frag(const LAS char* p) {
  s16x4 lo = __builtin_amdgcn_ds_read_tr16_b64_v4i16((LAS s16x4*)p);
  s16x4 hi = __builtin_amdgcn_ds_read_tr16_b64_v4i16((LAS s16x4*)(p + 1024));
  return __builtin_bit_cast(bf16x8, __builtin_shufflevector(lo, hi, 0, 1, 2, 3, 4, 5, 6, 7));
}
DI unsigned pk2(float a, float b) { f32x2 v = {a, b}; return __builtin_bit_cast(unsigned, __builtin_convertvector(v, bf16x2)); }
DI bf16x8 pack8(const f32x16& a, int s) {
  u32x4 r = {pk2(a[8 * s], a[8 * s + 1]), pk2(a[8 * s + 2], a[8 * s + 3]), pk2(a[8 * s + 4], a[8 * s + 5]), pk2(a[8 * s + 6], a[8 * s + 7])};
  return __builtin_bit_cast(bf16x8, r);
}
DI u32x4 not_ones(u32x4 v) {
  return u32x4{min(v[0], 0xfffffffeu), min(v[1], 0xfffffffeu), min(v[2], 0xfffffffeu), min(v[3], 0xfffffffeu)};
}
DI u32x4 pack8s(const f32x16& a, int s, float k) {
  float t[8];
#pragma unroll
  for (int i = 0; i < 8; ++i) t[i] = a[8 * s + i] * k;
  SB();
  return u32x4{pk2(t[0], t[1]), pk2(t[2], t[3]), pk2(t[4], t[5]), pk2(t[6], t[7])};
}
DI unsigned umax4(unsigned m, u32x4 v) { return max(max(max(m, v[0]), v[1]), max(v[2], v[3])); }
template <int I> struct IC { static constexpr int v = I; };
template <int B, int E, class F> DI void sfor(F&& f) { if constexpr (B < E) { f(IC<B>{}); sfor<B + 1, E>(f); } }
#define MFMA(a, b, c) __builtin_amdgcn_mfma_f32_32x32x16_bf16((a), (b), (c), 0, 0, 0)
#define BF(x) __builtin_bit_cast(bf16x8, x)
constexpr unsigned SPIN_CAP = 1u << 20;

DI void wait_flags(const GAS u64* p, int lane, int n, u64 tag, int stride = 1) {
  const GAS u64* q = p + (lane < n ? lane : 0) * stride;
#pragma clang loop unroll(disable)
  for (unsigned spins = 0; spins < SPIN_CAP; ++spins) {
    u64 v = __hip_atomic_load(q, RLX_AGENT);
    if (__all(v == tag)) break;
    __builtin_amdgcn_s_sleep(1);
  }
  asm volatile("" ::: "memory");
}

__global__ __launch_bounds__(512, 2) void gqa_fused(const char* __restrict__ x, const char* __restrict__ kc, const char* __restrict__ vc,
                                                   const char* __restrict__ wq, const char* __restrict__ wo, char* __restrict__ out, char* ws) {
  __shared__ __attribute__((aligned(16))) char lds_raw[LDS_BYTES];
  const LAS char* lds = (const LAS char*)lds_raw;
  const unsigned lds0 = (unsigned)(size_t)lds;
  const int lane = threadIdx.x & 63;
  const int w8 = __builtin_amdgcn_readfirstlane(threadIdx.x >> 6);
  const int j = blockIdx.x & 7, bp = blockIdx.x >> 5, hh = (blockIdx.x >> 3) & 3, h = 4 * j + hh;

  const bool early = STG == 0 ? false : STG == 1 ? (bp & 1) : STG == 2 ? ((bp ^ hh) & 1) : true;
  if (w8 >= 4) {
    const int lw = w8 - 4;
    const int rot = ROT == 0 ? 0 : ROT == 1 ? (((bp ^ hh) & 1) << 2) : ROT == 2 ? (((bp + hh) & 3) << 1) : ((bp + 2 * hh + (j & 1)) & 7);
    const unsigned l4 = lane >> 4, l3 = lane >> 3;
    const unsigned vx0 = l3 * 1024 + (((lane & 7) ^ l4) << 4), vx1 = vx0 ^ 64;
    const unsigned ptr_ = (lane & 15) ^ (l4 << 2);
    const unsigned vwq = l4 * 8192 + (ptr_ << 4);
    const unsigned vv = l4 * 256 + (ptr_ << 4);
    const unsigned vwo = l4 * 1024 + ((ptr_ ^ (unsigned)(j << 1)) << 4);
    const unsigned vk = l4 * 256 + (((lane & 15) ^ l4) << 4);
    const char* xb = x + (size_t)(bp * 128 + lw * 32) * 1024;
    const char* wqb = wq + (size_t)(lw * 16) * 8192 + h * 256;
    const size_t kvoff = (size_t)((bp * 2 + (lw >> 1)) * 32 + h) * 16384 + (lw & 1) * 8192;
    const char* kb = kc + kvoff;
    const char* vb = vc + kvoff;
    const char* wob = wo + (size_t)(j * 512 + lw * 16) * 1024 + hh * 256;

    auto chunk = [&](int c, int sl) AI {
      const int cc = (c + rot) & 7;
      const unsigned slot = lds0 + sl * 32768;
#pragma unroll
      for (int q = 0; q < 4; ++q) {
        dma16(wqb + (size_t)(cc * 64 + q * 4) * 8192, vwq, slot + 16384 + (lw * 16 + q * 4) * 256);
        dma16(xb + q * 8192 + cc * 128, (q & 1) ? vx1 : vx0, slot + (lw * 32 + q * 8) * 128);
      }
    };
    auto loadK = [&]() AI {
      dma4(wob + (size_t)(bp * 64) * 1024, ((lane & 31) >> 1) * 1024 + (lane & 1) * 128, lds0 + L_K + lw * 8192);
#pragma unroll
      for (int i = 0; i < 8; ++i) dma16nt(kb + i * 1024, vk ^ ((i & 3) << 6), lds0 + L_K + lw * 8192 + i * 1024);
    };
    auto loadV = [&]() AI {
#pragma unroll
      for (int i = 0; i < 8; ++i) dma16nt(vb + i * 1024, vv, lds0 + L_V + lw * 8192 + i * 1024);
    };
    auto loadWo = [&](int g) AI {
      const char* b = wob + (size_t)((hh ^ g) * 128) * 1024;
#pragma unroll
      for (int q = 0; q < 8; ++q)
        dma16(b + (size_t)((q >> 2) * 64 + (q & 3) * 4) * 1024, vwo, lds0 + g * 32768 + ((q >> 2) * 64 + lw * 16 + (q & 3) * 4) * 256);
    };
    chunk(0, 0); chunk(1, 1);
    wait_vm_bar<8>(); chunk(2, 2);
    if (early) {
      wait_vm_bar<0>(); loadK(); loadV(); chunk(3, 0);
      wait_vm_bar<0>(); chunk(4, 1); chunk(5, 2);
      wait_vm_bar<8>(); chunk(6, 0);
      wait_vm_bar<8>(); chunk(7, 1);
      wait_vm_bar<8>(); loadWo(2);
      wait_vm_bar<8>(); loadWo(0);
      wait_vm_bar<63>(); loadWo(1);
      wait_vm_bar<63>(); if (!WOP) loadWo(3);
      wait_vm_bar<WOP ? 0 : 8>(); if (WOP) loadWo(3);
    } else {
      wait_vm_bar<8>(); chunk(3, 3); chunk(4, 4);
      wait_vm_bar<0>(); chunk(5, 0); chunk(6, 1);
      wait_vm_bar<63>(); chunk(7, 2);
      wait_vm_bar<63>(); loadK();
      wait_vm_bar<9>(); loadV();
      wait_vm_bar<8>(); loadWo(0);
      wait_vm_bar<8>(); if (!WOP) { loadWo(1); loadWo(2); }
      wait_vm_bar<WOP ? 0 : 16>(); if (WOP) { loadWo(1); loadWo(2); } loadWo(3);
    }
    wait_vm_bar<0>();
    asm volatile(".Lgqa_loader_end:");
    return;
  }

  unsigned codew = 0;
  if (CODEW) {
    u64 pc;
    int len;
    asm volatile("s_getpc_b64 %0\n1:\n\ts_mov_b32 %1, .Lgqa_loader_end-1b" : "=s"(pc), "=s"(len));
    const __amdgpu_buffer_rsrc_t cr = __builtin_amdgcn_make_buffer_rsrc((char*)pc, 0, len > 0 ? len : 0, 0x00020000);
    codew = __builtin_amdgcn_raw_buffer_load_b32(cr, lane < 2 ? (((blockIdx.x >> 3) * 4 + w8) * 2 + lane) * 128 : 0x7ffffff0, 0, 0);
  }
  const int w = w8;
  const int r = lane & 31, hf = lane >> 5;
  const int grp = bp * 4 + w;
  const int me = (grp * 8 + j) * 4 + hh;
  const u64 tag = __hip_atomic_load((const GAS u64*)(ws + WS_EPOCH + grp * 128), RLX_AGENT) + 1;
  const __amdgpu_buffer_rsrc_t wsr = __builtin_amdgcn_make_buffer_rsrc(ws, 0, 16u << 20, 0x00020000);
  const GAS u64* myflag = (const GAS u64*)(ws + WS_FLAG + (size_t)me * 512);
  { u64 warm = __hip_atomic_load(myflag + (lane & 3) * 16, RLX_AGENT); asm volatile("" ::"v"(warm)); }
  unsigned tl = 0;
  if (TLBW) {
    const size_t kvo = (size_t)((bp * 2 + (w >> 1)) * 32 + h) * 16384;
    const char* p = lane == 0 ? kc + kvo : lane == 1 ? vc + kvo : lane == 2 ? wo + (size_t)(j * 512) * 1024 : lane == 3 ? ws + (size_t)me * 8192
                                                                                                                       : out + (size_t)(bp * 128 + w * 32) * 1024;
    tl = __hip_atomic_load((const GAS unsigned*)p, RLX_AGENT);
  }
  __builtin_amdgcn_raw_buffer_store_b128(u32x4{~0u, ~0u, ~0u, ~0u}, wsr, lane * 16 + 1024, (unsigned)WS_P + (me ^ 4) * 8192, 16);

  const unsigned q4 = (lane & 15) >> 2, p4 = lane & 3, blk = (lane >> 4) & 1;
  const unsigned trc = (8 * hf + q4) * 256 + 32 * blk + 8 * (((p4 & 1) << 1) | (p4 >> 1));
  unsigned trb[4];
#pragma unroll
  for (int c = 0; c < 4; ++c) trb[c] = trc + ((c ^ q4) << 6);
  const unsigned xsw = (r >> 1) & 7;
  unsigned xfb[4];
#pragma unroll
  for (int s = 0; s < 4; ++s) xfb[s] = (w * 32 + r) * 128 + (((2 * s + hf) ^ xsw) << 4);
  const int kr = (r & 19) | ((r & 4) << 1) | ((r & 8) >> 1);
  const LAS char* kp = lds + L_K + (w >> 1) * 16384 + kr * 256;

  f32x16 acc[4], sc[2];
#pragma unroll
  for (int c = 0; c < 4; ++c)
#pragma unroll
    for (int i = 0; i < 16; ++i) acc[c][i] = 0.f;
#pragma unroll
  for (int t = 0; t < 2; ++t)
#pragma unroll
    for (int i = 0; i < 16; ++i) sc[t][i] = 0.f;

  bf16x8 A[8];
  bf16x8 f[8];
  bf16x8 kf[16];
  {
    bf16x8 B[8];
    auto stp = [](int m) AI { return m < 112 ? (m >> 2) & 3 : m & 3; };
    auto til = [](int m) AI { return m < 112 ? m & 3 : (m >> 2) & 3; };
    auto rd = [&](int m) AI {
      const int c = m >> 4, s = stp(m), t = til(m);
      const unsigned slot = c < 3 ? c * 32768u : early ? (c % 3) * 32768u : (c % 5) * 32768u;
      if (t == 0) B[(c & 1) * 4 + s] = *(const LAS bf16x8*)(lds + slot + xfb[s]);
      A[m & 7] = tr_frag(lds + slot + 16384 + s * 4096 + trb[t]);
    };
    bar_only();
    STAMP(1);
#pragma unroll
    for (int m = 0; m < 6; ++m) rd(m);
    SB();
    sfor<0, 128>([&](auto ic) AI {
      constexpr int m = decltype(ic)::v;
      acc[til(m)] = MFMA(A[m & 7], B[((m >> 4) & 1) * 4 + stp(m)], acc[til(m)]);
      SB();
      constexpr int k = m & 15;
      if constexpr (QNOP >= 0 && m >= QFROM) { nop<(QNOP > 15 ? 15 : QNOP)>(); if constexpr (QNOP > 15) nop<QNOP - 16>(); }
      if constexpr (m < 112) {
        if (k < 10) rd(m + 6);
        else if (k == 12) {
          if ((m >> 4) == 1 ? !early : (m >> 4) >= 5 ? early : true) lgkm_bar();
          SB();
          rd(m + 4); rd(m + 5);
        }
        else if (k == 13) { rd(m + 5); rd(m + 6); }
        else if (k > 13) rd(m + 6);
      } else {
        if (m + 6 < 128) rd(m + 6);
        if constexpr (m >= 112 && k >= 5 && (k & 1)) f[(k - 5) >> 1] = pack8(acc[(k - 5) >> 2], ((k - 5) >> 1) & 1);
      }
      SB();
    });
  }
  STAMP(2);
  const LAS char* vp = lds + L_V + (w >> 1) * 16384;
  f[6] = pack8(acc[3], 0);
  f[7] = pack8(acc[3], 1);
  lgkm_bar();
  STAMP(3);
  constexpr float C = 0.08838834764831845f * 1.4426950408889634f;
  float m0, sum0 = 0.f;
  {
    auto rdk = [&](int i) AI { kf[i] = *(const LAS bf16x8*)(kp + (i >> 3) * 8192 + (((2 * (i & 7) + hf) ^ (kr & 15)) << 4)); };
#pragma unroll
    for (int i = 0; i < 12; ++i) { rdk(i); SB(); }
    sfor<0, 16>([&](auto ic) AI {
      constexpr int m = decltype(ic)::v;
      sc[m >> 3] = MFMA(kf[m], f[m & 7], sc[m >> 3]);
      SB();
      if constexpr (m < 4) rdk(12 + m);
      if constexpr (m == 9) {
        m0 = sc[0][0];
#pragma unroll
        for (int i = 1; i < 16; ++i) m0 = fmaxf(m0, sc[0][i]);
        m0 *= C;
      }
      if constexpr (m >= 10) {
#pragma unroll
        for (int i = (m - 10) * 8 / 3; i < (m - 9) * 8 / 3; ++i) sc[0][i] = __builtin_amdgcn_exp2f(fmaf(sc[0][i], C, -m0));
      }
      SB();
    });
  }
#pragma unroll
  for (int i = 0; i < 16; ++i) sum0 += sc[0][i];
  pin(sum0);
  float inv;
  {
    float mx = sc[1][0];
#pragma unroll
    for (int i = 1; i < 16; ++i) mx = fmaxf(mx, sc[1][i]);
    mx = fmaxf(mx * C, m0);
    auto rr = __builtin_amdgcn_permlane32_swap(__float_as_uint(mx), __float_as_uint(mx), false, false);
    mx = fmaxf(__uint_as_float(rr[0]), __uint_as_float(rr[1]));
    const float a0 = __builtin_amdgcn_exp2f(m0 - mx);
    float sum = sum0 * a0;
#pragma unroll
    for (int i = 0; i < 16; ++i) sc[1][i] = __builtin_amdgcn_exp2f(fmaf(sc[1][i], C, -mx));
    f[0] = BF(pack8s(sc[0], 0, a0));
    f[1] = BF(pack8s(sc[0], 1, a0));
#pragma unroll
    for (int i = 0; i < 16; ++i) sum += sc[1][i];
    f[2] = pack8(sc[1], 0);
    f[3] = pack8(sc[1], 1);
    auto r2 = __builtin_amdgcn_permlane32_swap(__float_as_uint(sum), __float_as_uint(sum), false, false);
    inv = __builtin_amdgcn_rcpf(__uint_as_float(r2[0]) + __uint_as_float(r2[1]));
  }
  asm volatile("" : "+v"(f[0]), "+v"(f[1]), "+v"(f[2]), "+v"(f[3]), "+v"(inv));
  STAMP(4);
  bar_only();
  STAMP(5);
#pragma unroll
  for (int c = 0; c < 4; ++c)
#pragma unroll
    for (int i = 0; i < 16; ++i) acc[c][i] = 0.f;
  u32x4 of[4][8];
  {
    auto rd = [&](int m) AI { A[m & 7] = tr_frag(vp + (m & 3) * 4096 + trb[m >> 2]); };
    auto put = [&](int s) AI {
      of[0][s] = pack8s(acc[s >> 1], s & 1, inv);
      __builtin_amdgcn_raw_buffer_store_b128(of[0][s], wsr, lane * 16 + s * 1024, me * 8192, 16);
    };
#pragma unroll
    for (int m = 0; m < 7; ++m) { rd(m); SB(); }
    sfor<0, 16>([&](auto ic) AI {
      constexpr int m = decltype(ic)::v;
      acc[m >> 2] = MFMA(A[m & 7], f[m & 3], acc[m >> 2]);
      SB();
      if constexpr (m + 7 < 16) rd(m + 7);
      if constexpr (m >= 5 && (m & 1)) put((m - 5) >> 1);
      SB();
    });
    put(6); SB(); put(7); SB();
  }
  STAMP(6);
#pragma unroll
  for (int c = 0; c < 4; ++c)
#pragma unroll
    for (int i = 0; i < 16; ++i) acc[c][i] = 0.f;
  u64 early_poll;
  asm volatile("" ::"v"(tl));
  asm volatile("" ::"v"(codew));
  bar_only();
  {
    auto rd = [&](int m) AI { A[m & 7] = tr_frag(lds + (m >> 2) * 4096 + trb[m & 3]); };
#pragma unroll
    for (int m = 0; m < 7; ++m) { rd(m); SB(); }
    sfor<0, 32>([&](auto ic) AI {
      constexpr int m = decltype(ic)::v;
      acc[m & 3] = MFMA(A[m & 7], BF(of[0][m >> 2]), acc[m & 3]);
      SB();
      if constexpr (m + 7 < 32) rd(m + 7);
      if constexpr (m == DRAIN1) {
        drain_vm();
        if (lane >= 1 && lane < 5)
          __hip_atomic_store((GAS u64*)(ws + WS_FLAG + (size_t)(me ^ lane) * 512) + (lane - 1) * 16, tag, RLX_AGENT);
        early_poll = ld_flag(myflag + (lane < 3 ? lane : 0) * 16);
      }
      SB();
    });
  }
  STAMP(7);
  pin64(early_poll);
  if (!__all(early_poll == tag)) wait_flags(myflag, lane, 3, tag, 16);
  asm volatile("" ::: "memory");
  STAMP(8);
  auto fetch = [&](int i) AI {
    if (i >= 16) of[1 + (i >> 3)][i & 7] = __builtin_amdgcn_raw_buffer_load_b128(wsr, lane * 16 + (i & 7) * 1024, (me ^ (1 + (i >> 3))) * 8192, OAUX3);
    else of[1 + (i >> 3)][i & 7] = __builtin_amdgcn_raw_buffer_load_b128(wsr, lane * 16 + (i & 7) * 1024, (me ^ (1 + (i >> 3))) * 8192, 16);
  };
#pragma unroll
  for (int i = 0; i < NF0; ++i) { fetch(i); SB(); }
  bar_only();
  {
    auto frg = [](int m) AI { return m < 48 ? m / 3 : m < 72 ? 16 + (m & 7) : m - 72; };
    auto til = [](int m) AI { return m < 48 ? 3 - m % 3 : m < 72 ? 3 - ((m - 48) >> 3) : 0; };
    auto rd = [&](int m) AI { A[m & 7] = tr_frag(lds + 32768 + frg(m) * 4096 + trb[til(m)]); };
#pragma unroll
    for (int m = 0; m < 7; ++m) { rd(m); SB(); }
    sfor<0, 96>([&](auto ic) AI {
      constexpr int m = decltype(ic)::v;
      acc[til(m)] = MFMA(A[m & 7], BF(of[1 + (frg(m) >> 3)][frg(m) & 7]), acc[til(m)]);
      SB();
      if constexpr (m + 7 < 96) rd(m + 7);
      if constexpr (m % FSTEP == 0 && NF0 + m / FSTEP < 24) fetch(NF0 + m / FSTEP);
      if constexpr (m == 40) early_poll = ld_flag(myflag + 48);
      if constexpr (m >= 56 && m < 80 && ((m & 7) == 1 || (m & 7) == 3)) {
        constexpr int d = 2 * (3 - ((m - 56) >> 3)) + ((m & 7) == 1 ? 1 : 0);
        __builtin_amdgcn_raw_buffer_store_b128(__builtin_bit_cast(u32x4, pack8(acc[d >> 1], d & 1)), wsr, lane * 16 + d * 1024,
                                               (unsigned)WS_P + (me ^ (d << 2)) * 8192, 16);
      }
      if constexpr (m == FL2) {
        drain_vm();
        if (lane >= 2 && lane < 8)
          __hip_atomic_store((GAS u64*)(ws + WS_FLAG + (size_t)(me ^ (lane << 2)) * 512) + 48 + (lane - 1), tag, RLX_AGENT);
      }
      SB();
    });
    __builtin_amdgcn_raw_buffer_store_b128(not_ones(__builtin_bit_cast(u32x4, pack8(acc[0], 1))), wsr, lane * 16 + 1024, (unsigned)WS_P + (me ^ 4) * 8192, 16);
    if (FL2 >= 96) {
      asm volatile("s_waitcnt vmcnt(1)" ::: "memory");
      if (lane >= 2 && lane < 8)
        __hip_atomic_store((GAS u64*)(ws + WS_FLAG + (size_t)(me ^ (lane << 2)) * 512) + 48 + (lane - 1), tag, RLX_AGENT);
    }
  }
  STAMP(9);
  pin64(early_poll);
  if (!__all(early_poll == tag)) wait_flags(myflag + 48, lane, 1, tag);
  u32x4 pv[7];
  {
    const GAS u64* q = myflag + 48 + (lane < 7 ? lane : 0);
    bool ok1 = false;
#pragma clang loop unroll(disable)
    for (unsigned spins = 0; spins < SPIN_CAP; ++spins) {
      const u64 v = ld_flag(q);
      pv[0] = __builtin_amdgcn_raw_buffer_load_b128(wsr, lane * 16 + 1024, (unsigned)WS_P + me * 8192, 16);
      SB();
      if (__all(v == tag)) break;
      __builtin_amdgcn_s_sleep(1);
    }
    asm volatile("" ::: "memory");
    STAMP(10);
#pragma unroll
    for (int d = 2; d < 8; ++d) pv[d - 1] = __builtin_amdgcn_raw_buffer_load_b128(wsr, lane * 16 + d * 1024, (unsigned)WS_P + me * 8192, PAUX);
    SB();
    ok1 = __all(umax4(0, pv[0]) != ~0u);
    if (!ok1) {
#pragma clang loop unroll(disable)
      for (unsigned spins = 0; spins < SPIN_CAP; ++spins) {
        asm volatile("" ::: "memory");
        pv[0] = __builtin_amdgcn_raw_buffer_load_b128(wsr, lane * 16 + 1024, (unsigned)WS_P + me * 8192, 16);
        if (__all(umax4(0, pv[0]) != ~0u)) break;
      }
    }
  }
  float o[8];
#pragma unroll
  for (int i = 0; i < 8; ++i) o[i] = acc[0][i];
#pragma unroll
  for (int d = 0; d < 7; ++d)
#pragma unroll
    for (int i = 0; i < 4; ++i) {
      o[2 * i] += __uint_as_float(pv[d][i] << 16);
      o[2 * i + 1] += __uint_as_float(pv[d][i] & 0xffff0000u);
    }
  u32x4 ov = {pk2(o[0], o[1]), pk2(o[2], o[3]), pk2(o[4], o[5]), pk2(o[6], o[7])};
  if (OUTSC) __builtin_amdgcn_raw_buffer_store_b128(ov, __builtin_amdgcn_make_buffer_rsrc(out, 0, 1u << 20, 0x00020000), (bp * 128 + w * 32 + r) * 1024 + hh * 256 + j * 32 + hf * 16, 0, OUTSC);
  else *(GAS u32x4*)(out + (size_t)(bp * 128 + w * 32 + r) * 1024 + hh * 256 + j * 32 + hf * 16) = ov;
  if (j == 0 && hh == 0 && lane == 0) __hip_atomic_store((GAS u64*)(ws + WS_EPOCH + grp * 128), tag, RLX_AGENT);
}

extern "C" void kernel_launch(void* const* d_in, const int* in_sizes, int n_in, void* d_out, int out_size, void* d_ws, size_t ws_size,
                              hipStream_t stream) {
  gqa_fused<<<256, 512, 0, stream>>>((const char*)d_in[0], (const char*)d_in[1], (const char*)d_in[2], (const char*)d_in[3],
                                     (const char*)d_in[4], (char*)d_out, (char*)d_ws);
}
```

```cpp
#include <hip/hip_runtime.h>
#include <hip/hip_bf16.h>

typedef unsigned short u16;
typedef unsigned long long u64;
typedef short s16x4 __attribute__((ext_vector_type(4)));
typedef short s16x8 __attribute__((ext_vector_type(8)));
typedef __bf16 bf16x8 __attribute__((ext_vector_type(8)));
typedef __bf16 bf16x2 __attribute__((ext_vector_type(2)));
typedef float f32x2 __attribute__((ext_vector_type(2)));
typedef float f32x16 __attribute__((ext_vector_type(16)));
typedef unsigned u32x4 __attribute__((ext_vector_type(4)));
#define LAS __attribute__((address_space(3)))
#define GAS __attribute__((address_space(1)))
#define DI __device__ __forceinline__
#define RLX_AGENT __ATOMIC_RELAXED, __HIP_MEMORY_SCOPE_AGENT
#define SB() __builtin_amdgcn_sched_barrier(0)
#define AI __attribute__((always_inline))

#define STAMPS 0
#define ROT 3
#define FL2 92
#define CODEW 1
#define NF0 10
#define FSTEP 2
#define OUTSC 16
#define PAUX 16
#define OAUX3 16
#define QNOP -1
#define QFROM 0
#define BVM 8
#define TA 1
#define STG 1
#define WOP 1
#define DRAIN1 16
#define STAMP(i) do { } while (0)

constexpr size_t WS_O     = 0;
constexpr size_t WS_P     = 8u << 20;
constexpr size_t WS_FLAG  = 16u << 20;
constexpr size_t WS_EPOCH = WS_FLAG + (512u << 10);
constexpr size_t WS_STAMP = 32u << 20;

constexpr unsigned L_K = 3u * 32768, L_V = 4u * 32768;
constexpr unsigned LDS_BYTES = 160u << 10;

DI void dma16(const char* sbase, unsigned voff, unsigned lds) {
  asm volatile("s_mov_b32 m0, %2\n\ts_nop 0\n\tglobal_load_lds_dwordx4 %0, %1" ::"v"(voff), "s"(sbase), "s"(lds) : "memory");
}
DI void dma16nt(const char* sbase, unsigned voff, unsigned lds) {
  asm volatile("s_mov_b32 m0, %2\n\ts_nop 0\n\tglobal_load_lds_dwordx4 %0, %1 nt" ::"v"(voff), "s"(sbase), "s"(lds) : "memory");
}
DI void dma4(const char* sbase, unsigned voff, unsigned lds) {
  asm volatile("s_mov_b32 m0, %2\n\ts_nop 0\n\tglobal_load_lds_dword %0, %1" ::"v"(voff), "s"(sbase), "s"(lds) : "memory");
}
DI void pin(float& v) { asm volatile("" : "+v"(v)); }
DI void pin64(u64& v) { asm volatile("" : "+v"(v)); }
DI u64 ld_flag(const GAS u64* q) { return __hip_atomic_load(q, RLX_AGENT); }
template <int N> DI void nop() { asm volatile("s_nop %0" ::"i"(N)); }
DI void drain_vm() { asm volatile("s_waitcnt vmcnt(0)" ::: "memory"); }
template <int N> DI void wait_vm_bar() { asm volatile("s_waitcnt vmcnt(%0)\n\ts_barrier" ::"i"(N) : "memory"); }
DI void bar_only() { SB(); asm volatile("s_barrier" ::: "memory"); SB(); }
DI void lgkm_bar() { SB(); asm volatile("s_waitcnt lgkmcnt(0)\n\ts_barrier" ::: "memory"); SB(); }

DI u64 rtime() { u64 t; asm volatile("s_memrealtime %0\n\ts_waitcnt lgkmcnt(0)" : "=s"(t)::"memory"); return t; }
DI unsigned ctime_() { u64 t; asm volatile("s_memtime %0\n\ts_waitcnt lgkmcnt(0)" : "=s"(t)::"memory"); return (unsigned)t; }

DI bf16x8 tr_frag(const LAS char* p) {
  s16x4 lo = __builtin_amdgcn_ds_read_tr16_b64_v4i16((LAS s16x4*)p);
  s16x4 hi = __builtin_amdgcn_ds_read_tr16_b64_v4i16((LAS s16x4*)(p + 1024));
  return __builtin_bit_cast(bf16x8, __builtin_shufflevector(lo, hi, 0, 1, 2, 3, 4, 5, 6, 7));
}
DI unsigned pk2(float a, float b) { f32x2 v = {a, b}; return __builtin_bit_cast(unsigned, __builtin_convertvector(v, bf16x2)); }
DI bf16x8 pack8(const f32x16& a, int s) {
  u32x4 r = {pk2(a[8 * s], a[8 * s + 1]), pk2(a[8 * s + 2], a[8 * s + 3]), pk2(a[8 * s + 4], a[8 * s + 5]), pk2(a[8 * s + 6], a[8 * s + 7])};
  return __builtin_bit_cast(bf16x8, r);
}
DI u32x4 not_ones(u32x4 v) {
  return u32x4{min(v[0], 0xfffffffeu), min(v[1], 0xfffffffeu), min(v[2], 0xfffffffeu), min(v[3], 0xfffffffeu)};
}
DI u32x4 pack8s(const f32x16& a, int s, float k) {
  float t[8];
#pragma unroll
  for (int i = 0; i < 8; ++i) t[i] = a[8 * s + i] * k;
  SB();
  return u32x4{pk2(t[0], t[1]), pk2(t[2], t[3]), pk2(t[4], t[5]), pk2(t[6], t[7])};
}
DI unsigned umax4(unsigned m, u32x4 v) { return max(max(max(m, v[0]), v[1]), max(v[2], v[3])); }
template <int I> struct IC { static constexpr int v = I; };
template <int B, int E, class F> DI void sfor(F&& f) { if constexpr (B < E) { f(IC<B>{}); sfor<B + 1, E>(f); } }
#define MFMA(a, b, c) __builtin_amdgcn_mfma_f32_32x32x16_bf16((a), (b), (c), 0, 0, 0)
#define BF(x) __builtin_bit_cast(bf16x8, x)
constexpr unsigned SPIN_CAP = 1u << 20;

DI void wait_flags(const GAS u64* p, int lane, int n, u64 tag, int stride = 1) {
  const GAS u64* q = p + (lane < n ? lane : 0) * stride;
#pragma clang loop unroll(disable)
  for (unsigned spins = 0; spins < SPIN_CAP; ++spins) {
    u64 v = __hip_atomic_load(q, RLX_AGENT);
    if (__all(v == tag)) break;
    __builtin_amdgcn_s_sleep(1);
  }
  asm volatile("" ::: "memory");
}

__global__ __launch_bounds__(512, 2) void gqa_fused(const char* __restrict__ x, const char* __restrict__ kc, const char* __restrict__ vc,
                                                   const char* __restrict__ wq, const char* __restrict__ wo, char* __restrict__ out, char* ws) {
  __shared__ __attribute__((aligned(16))) char lds_raw[LDS_BYTES];
  const LAS char* lds = (const LAS char*)lds_raw;
  const unsigned lds0 = (unsigned)(size_t)lds;
  const int lane = threadIdx.x & 63;
  const int w8 = __builtin_amdgcn_readfirstlane(threadIdx.x >> 6);
  const int j = blockIdx.x & 7, bp = blockIdx.x >> 5, hh = (blockIdx.x >> 3) & 3, h = 4 * j + hh;

  const bool early = STG == 0 ? false : STG == 1 ? (bp & 1) : STG == 2 ? ((bp ^ hh) & 1) : true;
  if (w8 >= 4) {
    const int lw = w8 - 4;
    const int rot = ROT == 0 ? 0 : ROT == 1 ? (((bp ^ hh) & 1) << 2) : ROT == 2 ? (((bp + hh) & 3) << 1) : ((bp + 2 * hh + (j & 1)) & 7);
    const unsigned l4 = lane >> 4, l3 = lane >> 3;
    const unsigned vx0 = l3 * 1024 + (((lane & 7) ^ l4) << 4), vx1 = vx0 ^ 64;
    const unsigned ptr_ = (lane & 15) ^ (l4 << 2);
    const unsigned vwq = l4 * 8192 + (ptr_ << 4);
    const unsigned vv = l4 * 256 + (ptr_ << 4);
    const unsigned vwo = l4 * 1024 + ((ptr_ ^ (unsigned)(j << 1)) << 4);
    const unsigned vk = l4 * 256 + (((lane & 15) ^ l4) << 4);
    const char* xb = x + (size_t)(bp * 128 + lw * 32) * 1024;
    const char* wqb = wq + (size_t)(lw * 16) * 8192 + h * 256;
    const size_t kvoff = (size_t)((bp * 2 + (lw >> 1)) * 32 + h) * 16384 + (lw & 1) * 8192;
    const char* kb = kc + kvoff;
    const char* vb = vc + kvoff;
    const char* wob = wo + (size_t)(j * 512 + lw * 16) * 1024 + hh * 256;

    auto chunk = [&](int c, int sl) AI {
      const int cc = (c + rot) & 7;
      const unsigned slot = lds0 + sl * 32768;
#pragma unroll
      for (int q = 0; q < 4; ++q) {
        dma16(wqb + (size_t)(cc * 64 + q * 4) * 8192, vwq, slot + 16384 + (lw * 16 + q * 4) * 256);
        dma16(xb + q * 8192 + cc * 128, (q & 1) ? vx1 : vx0, slot + (lw * 32 + q * 8) * 128);
      }
    };
    auto loadK = [&]() AI {
      if (TA) dma4(wob + (size_t)((bp >> 1) * 128) * 1024, early ? 0 : (lane >> 5) * 65536 + ((lane & 31) >> 1) * 1024 + (lane & 1) * 128, lds0 + L_K + lw * 8192);
      else dma4(wob + (size_t)(bp * 64) * 1024, ((lane & 31) >> 1) * 1024 + (lane & 1) * 128, lds0 + L_K + lw * 8192);
#pragma unroll
      for (int i = 0; i < 8; ++i) dma16nt(kb + i * 1024, vk ^ ((i & 3) << 6), lds0 + L_K + lw * 8192 + i * 1024);
    };
    auto loadV = [&]() AI {
#pragma unroll
      for (int i = 0; i < 8; ++i) dma16nt(vb + i * 1024, vv, lds0 + L_V + lw * 8192 + i * 1024);
    };
    auto loadWo = [&](int g) AI {
      const char* b = wob + (size_t)((hh ^ g) * 128) * 1024;
#pragma unroll
      for (int q = 0; q < 8; ++q)
        dma16(b + (size_t)((q >> 2) * 64 + (q & 3) * 4) * 1024, vwo, lds0 + g * 32768 + ((q >> 2) * 64 + lw * 16 + (q & 3) * 4) * 256);
    };
    chunk(0, 0); chunk(1, 1);
    wait_vm_bar<8>(); chunk(2, 2);
    if (early) {
      wait_vm_bar<0>(); loadK(); loadV(); chunk(3, 0);
      wait_vm_bar<0>(); chunk(4, 1); chunk(5, 2);
      wait_vm_bar<8>(); chunk(6, 0);
      wait_vm_bar<8>(); chunk(7, 1);
      wait_vm_bar<8>(); loadWo(2);
      wait_vm_bar<8>(); loadWo(0);
      wait_vm_bar<63>(); loadWo(1);
      wait_vm_bar<63>(); if (!WOP) loadWo(3);
      wait_vm_bar<WOP ? 0 : 8>(); if (WOP) loadWo(3);
    } else {
      wait_vm_bar<8>(); chunk(3, 3); chunk(4, 4);
      wait_vm_bar<0>(); chunk(5, 0); chunk(6, 1);
      wait_vm_bar<63>(); chunk(7, 2);
      wait_vm_bar<63>(); loadK();
      wait_vm_bar<9>(); loadV();
      wait_vm_bar<8>(); loadWo(0);
      wait_vm_bar<8>(); if (!WOP) { loadWo(1); loadWo(2); }
      wait_vm_bar<WOP ? 0 : 16>(); if (WOP) { loadWo(1); loadWo(2); } loadWo(3);
    }
    wait_vm_bar<0>();
    asm volatile(".Lgqa_loader_end:");
    return;
  }

  unsigned codew = 0;
  if (CODEW) {
    u64 pc;
    int len;
    asm volatile("s_getpc_b64 %0\n1:\n\ts_mov_b32 %1, .Lgqa_loader_end-1b" : "=s"(pc), "=s"(len));
    const __amdgpu_buffer_rsrc_t cr = __builtin_amdgcn_make_buffer_rsrc((char*)pc, 0, len > 0 ? len : 0, 0x00020000);
    codew = __builtin_amdgcn_raw_buffer_load_b32(cr, lane < 2 ? (((blockIdx.x >> 3) * 4 + w8) * 2 + lane) * 128 : 0x7ffffff0, 0, 0);
  }
  const int w = w8;
  const int r = lane & 31, hf = lane >> 5;
  const int grp = bp * 4 + w;
  const int me = (grp * 8 + j) * 4 + hh;
  const u64 tag = __hip_atomic_load((const GAS u64*)(ws + WS_EPOCH + grp * 128), RLX_AGENT) + 1;
  const __amdgpu_buffer_rsrc_t wsr = __builtin_amdgcn_make_buffer_rsrc(ws, 0, 16u << 20, 0x00020000);
  const GAS u64* myflag = (const GAS u64*)(ws + WS_FLAG + (size_t)me * 512);
  { u64 warm = __hip_atomic_load(myflag + (lane & 3) * 16, RLX_AGENT); asm volatile("" ::"v"(warm)); }
  __builtin_amdgcn_raw_buffer_store_b128(u32x4{~0u, ~0u, ~0u, ~0u}, wsr, lane * 16 + 1024, (unsigned)WS_P + (me ^ 4) * 8192, 16);

  const unsigned q4 = (lane & 15) >> 2, p4 = lane & 3, blk = (lane >> 4) & 1;
  const unsigned trc = (8 * hf + q4) * 256 + 32 * blk + 8 * (((p4 & 1) << 1) | (p4 >> 1));
  unsigned trb[4];
#pragma unroll
  for (int c = 0; c < 4; ++c) trb[c] = trc + ((c ^ q4) << 6);
  const unsigned xsw = (r >> 1) & 7;
  unsigned xfb[4];
#pragma unroll
  for (int s = 0; s < 4; ++s) xfb[s] = (w * 32 + r) * 128 + (((2 * s + hf) ^ xsw) << 4);
  const int kr = (r & 19) | ((r & 4) << 1) | ((r & 8) >> 1);
  const LAS char* kp = lds + L_K + (w >> 1) * 16384 + kr * 256;

  f32x16 acc[4], sc[2];
#pragma unroll
  for (int c = 0; c < 4; ++c)
#pragma unroll
    for (int i = 0; i < 16; ++i) acc[c][i] = 0.f;
#pragma unroll
  for (int t = 0; t < 2; ++t)
#pragma unroll
    for (int i = 0; i < 16; ++i) sc[t][i] = 0.f;

  bf16x8 A[8];
  bf16x8 f[8];
  bf16x8 kf[16];
  auto s_t = [](int m) AI { return (m < 6 || m == 8 || m == 9) ? 0 : 1; };
  auto s_s = [](int m) AI { return m < 6 ? m : m < 8 ? m - 6 : m < 10 ? m - 2 : m - 8; };
  auto rdk = [&](int i) AI { kf[i] = *(const LAS bf16x8*)(kp + s_t(i) * 8192 + (((2 * s_s(i) + hf) ^ (kr & 15)) << 4)); };
  {
    bf16x8 B[8];
    auto stp = [](int m) AI { return m < 112 ? (m >> 2) & 3 : m & 3; };
    auto til = [](int m) AI { return m < 112 ? m & 3 : (m >> 2) & 3; };
    auto rd = [&](int m) AI {
      const int c = m >> 4, s = stp(m), t = til(m);
      const unsigned slot = c < 3 ? c * 32768u : early ? (c % 3) * 32768u : (c % 5) * 32768u;
      if (t == 0) B[(c & 1) * 4 + s] = *(const LAS bf16x8*)(lds + slot + xfb[s]);
      A[m & 7] = tr_frag(lds + slot + 16384 + s * 4096 + trb[t]);
    };
    bar_only();
    STAMP(1);
#pragma unroll
    for (int m = 0; m < 6; ++m) rd(m);
    SB();
    sfor<0, 128>([&](auto ic) AI {
      constexpr int m = decltype(ic)::v;
      acc[til(m)] = MFMA(A[m & 7], B[((m >> 4) & 1) * 4 + stp(m)], acc[til(m)]);
      SB();
      constexpr int k = m & 15;
      if constexpr (QNOP >= 0 && m >= QFROM) { nop<(QNOP > 15 ? 15 : QNOP)>(); if constexpr (QNOP > 15) nop<QNOP - 16>(); }
      if constexpr (m < 112) {
        if (k < 10) rd(m + 6);
        else if (k == 12) {
          if ((m >> 4) == 1 ? !early : (m >> 4) >= 5 ? early : true) lgkm_bar();
          SB();
          rd(m + 4); rd(m + 5);
        }
        else if (k == 13) { rd(m + 5); rd(m + 6); }
        else if (k > 13) rd(m + 6);
      } else {
        if (m + 6 < 128) rd(m + 6);
        if constexpr (k == 12) lgkm_bar();
        if constexpr (k >= 12) { rdk(2 * k - 24); rdk(2 * k - 23); }
        if constexpr (k >= 5 && (k & 1)) f[(k - 5) >> 1] = pack8(acc[(k - 5) >> 2], ((k - 5) >> 1) & 1);
      }
      SB();
    });
  }
  STAMP(2);
  const LAS char* vp = lds + L_V + (w >> 1) * 16384;
  constexpr float C = 0.08838834764831845f * 1.4426950408889634f;
  auto o_s = [](int m) AI { return m < 8 ? m >> 2 : 2 + (m & 1); };
  auto o_t = [](int m) AI { return m < 8 ? m & 3 : (m - 8) >> 1; };
  auto rdv = [&](int m) AI { A[m & 7] = tr_frag(vp + o_s(m) * 4096 + trb[o_t(m)]); };
  float m0;
  sfor<0, 16>([&](auto ic) AI {
    constexpr int m = decltype(ic)::v;
    sc[s_t(m)] = MFMA(kf[m], f[s_s(m)], sc[s_t(m)]);
    SB();
    if constexpr (m < 4) { rdk(8 + 2 * m); rdk(9 + 2 * m); }
    if constexpr (m == 1) f[6] = pack8(acc[3], 0);
    if constexpr (m == 2) f[7] = pack8(acc[3], 1);
    if constexpr (m == BVM) lgkm_bar();
    if constexpr (m >= BVM && m < BVM + 7 && BVM < 10) rdv(m - BVM);
    if constexpr (m == 11) {
      m0 = sc[0][0];
#pragma unroll
      for (int i = 1; i < 16; ++i) m0 = fmaxf(m0, sc[0][i]);
      m0 *= C;
    }
    if constexpr (m >= 12) {
#pragma unroll
      for (int i = 4 * (m - 12); i < 4 * (m - 11); ++i) sc[0][i] = __builtin_amdgcn_exp2f(fmaf(sc[0][i], C, -m0));
    }
    SB();
  });
  if constexpr (BVM >= 10) {
#pragma unroll
    for (int m = 0; m < 7; ++m) { rdv(m); SB(); }
  }
  STAMP(3);
  float sum = 0.f, mx, inv;
#pragma unroll
  for (int i = 0; i < 16; ++i) sum += sc[0][i];
  pin(sum);
  {
    mx = sc[1][0];
#pragma unroll
    for (int i = 1; i < 16; ++i) mx = fmaxf(mx, sc[1][i]);
    mx = fmaxf(mx * C, m0);
    auto rr = __builtin_amdgcn_permlane32_swap(__float_as_uint(mx), __float_as_uint(mx), false, false);
    mx = fmaxf(__uint_as_float(rr[0]), __uint_as_float(rr[1]));
  }
  const float a0 = __builtin_amdgcn_exp2f(m0 - mx);
  sum *= a0;
  f[0] = BF(pack8s(sc[0], 0, a0));
  STAMP(4);
  STAMP(5);
#pragma unroll
  for (int c = 0; c < 4; ++c)
#pragma unroll
    for (int i = 0; i < 16; ++i) acc[c][i] = 0.f;
  u32x4 of[4][8];
  {
    auto put = [&](int s) AI {
      of[0][s] = pack8s(acc[s >> 1], s & 1, inv);
      __builtin_amdgcn_raw_buffer_store_b128(of[0][s], wsr, lane * 16 + s * 1024, me * 8192, 16);
    };
    auto ex = [&](int i0, int i1) AI {
#pragma unroll
      for (int i = i0; i < i1; ++i) sc[1][i] = __builtin_amdgcn_exp2f(fmaf(sc[1][i], C, -mx));
    };
    SB();
    sfor<0, 16>([&](auto ic) AI {
      constexpr int m = decltype(ic)::v;
      acc[o_t(m)] = MFMA(A[m & 7], f[o_s(m)], acc[o_t(m)]);
      SB();
      if constexpr (m + 7 < 16) rdv(m + 7);
      if constexpr (m == 0) f[1] = BF(pack8s(sc[0], 1, a0));
      if constexpr (m >= 1 && m < 7) ex((m - 1) * 8 / 3, m * 8 / 3);
      if constexpr (m == 4) f[2] = pack8(sc[1], 0);
      if constexpr (m == 7) f[3] = pack8(sc[1], 1);
      if constexpr (m == 7 || m == 8) {
#pragma unroll
        for (int i = 8 * (m - 7); i < 8 * (m - 6); ++i) sum += sc[1][i];
      }
      if constexpr (m == 9) {
        auto r2 = __builtin_amdgcn_permlane32_swap(__float_as_uint(sum), __float_as_uint(sum), false, false);
        inv = __builtin_amdgcn_rcpf(__uint_as_float(r2[0]) + __uint_as_float(r2[1]));
      }
      if constexpr (m >= 11) put(m - 11);
      SB();
    });
    put(5); SB(); put(6); SB(); put(7); SB();
  }
  STAMP(6);
#pragma unroll
  for (int c = 0; c < 4; ++c)
#pragma unroll
    for (int i = 0; i < 16; ++i) acc[c][i] = 0.f;
  u64 early_poll;
  asm volatile("" ::"v"(codew));
  bar_only();
  {
    auto rd = [&](int m) AI { A[m & 7] = tr_frag(lds + (m >> 2) * 4096 + trb[m & 3]); };
#pragma unroll
    for (int m = 0; m < 7; ++m) { rd(m); SB(); }
    sfor<0, 32>([&](auto ic) AI {
      constexpr int m = decltype(ic)::v;
      acc[m & 3] = MFMA(A[m & 7], BF(of[0][m >> 2]), acc[m & 3]);
      SB();
      if constexpr (m + 7 < 32) rd(m + 7);
      if constexpr (m == DRAIN1) {
        drain_vm();
        if (lane >= 1 && lane < 5)
          __hip_atomic_store((GAS u64*)(ws + WS_FLAG + (size_t)(me ^ lane) * 512) + (lane - 1) * 16, tag, RLX_AGENT);
        early_poll = ld_flag(myflag + (lane < 3 ? lane : 0) * 16);
      }
      SB();
    });
  }
  STAMP(7);
  pin64(early_poll);
  if (!__all(early_poll == tag)) wait_flags(myflag, lane, 3, tag, 16);
  asm volatile("" ::: "memory");
  STAMP(8);
  auto fetch = [&](int i) AI {
    if (i >= 16) of[1 + (i >> 3)][i & 7] = __builtin_amdgcn_raw_buffer_load_b128(wsr, lane * 16 + (i & 7) * 1024, (me ^ (1 + (i >> 3))) * 8192, OAUX3);
    else of[1 + (i >> 3)][i & 7] = __builtin_amdgcn_raw_buffer_load_b128(wsr, lane * 16 + (i & 7) * 1024, (me ^ (1 + (i >> 3))) * 8192, 16);
  };
#pragma unroll
  for (int i = 0; i < NF0; ++i) { fetch(i); SB(); }
  bar_only();
  {
    auto frg = [](int m) AI { return m < 48 ? m / 3 : m < 72 ? 16 + (m & 7) : m - 72; };
    auto til = [](int m) AI { return m < 48 ? 3 - m % 3 : m < 72 ? 3 - ((m - 48) >> 3) : 0; };
    auto rd = [&](int m) AI { A[m & 7] = tr_frag(lds + 32768 + frg(m) * 4096 + trb[til(m)]); };
#pragma unroll
    for (int m = 0; m < 7; ++m) { rd(m); SB(); }
    sfor<0, 96>([&](auto ic) AI {
      constexpr int m = decltype(ic)::v;
      acc[til(m)] = MFMA(A[m & 7], BF(of[1 + (frg(m) >> 3)][frg(m) & 7]), acc[til(m)]);
      SB();
      if constexpr (m + 7 < 96) rd(m + 7);
      if constexpr (m % FSTEP == 0 && NF0 + m / FSTEP < 24) fetch(NF0 + m / FSTEP);
      if constexpr (m == 40) early_poll = ld_flag(myflag + 48);
      if constexpr (m >= 56 && m < 80 && ((m & 7) == 1 || (m & 7) == 3)) {
        constexpr int d = 2 * (3 - ((m - 56) >> 3)) + ((m & 7) == 1 ? 1 : 0);
        __builtin_amdgcn_raw_buffer_store_b128(__builtin_bit_cast(u32x4, pack8(acc[d >> 1], d & 1)), wsr, lane * 16 + d * 1024,
                                               (unsigned)WS_P + (me ^ (d << 2)) * 8192, 16);
      }
      if constexpr (m == FL2) {
        drain_vm();
        if (lane >= 2 && lane < 8)
          __hip_atomic_store((GAS u64*)(ws + WS_FLAG + (size_t)(me ^ (lane << 2)) * 512) + 48 + (lane - 1), tag, RLX_AGENT);
      }
      SB();
    });
    __builtin_amdgcn_raw_buffer_store_b128(not_ones(__builtin_bit_cast(u32x4, pack8(acc[0], 1))), wsr, lane * 16 + 1024, (unsigned)WS_P + (me ^ 4) * 8192, 16);
    if (FL2 >= 96) {
      asm volatile("s_waitcnt vmcnt(1)" ::: "memory");
      if (lane >= 2 && lane < 8)
        __hip_atomic_store((GAS u64*)(ws + WS_FLAG + (size_t)(me ^ (lane << 2)) * 512) + 48 + (lane - 1), tag, RLX_AGENT);
    }
  }
  STAMP(9);
  pin64(early_poll);
  if (!__all(early_poll == tag)) wait_flags(myflag + 48, lane, 1, tag);
  u32x4 pv[7];
  {
    const GAS u64* q = myflag + 48 + (lane < 7 ? lane : 0);
    bool ok1 = false;
#pragma clang loop unroll(disable)
    for (unsigned spins = 0; spins < SPIN_CAP; ++spins) {
      const u64 v = ld_flag(q);
      pv[0] = __builtin_amdgcn_raw_buffer_load_b128(wsr, lane * 16 + 1024, (unsigned)WS_P + me * 8192, 16);
      SB();
      if (__all(v == tag)) break;
      __builtin_amdgcn_s_sleep(1);
    }
    asm volatile("" ::: "memory");
    STAMP(10);
#pragma unroll
    for (int d = 2; d < 8; ++d) pv[d - 1] = __builtin_amdgcn_raw_buffer_load_b128(wsr, lane * 16 + d * 1024, (unsigned)WS_P + me * 8192, PAUX);
    SB();
    ok1 = __all(umax4(0, pv[0]) != ~0u);
    if (!ok1) {
#pragma clang loop unroll(disable)
      for (unsigned spins = 0; spins < SPIN_CAP; ++spins) {
        asm volatile("" ::: "memory");
        pv[0] = __builtin_amdgcn_raw_buffer_load_b128(wsr, lane * 16 + 1024, (unsigned)WS_P + me * 8192, 16);
        if (__all(umax4(0, pv[0]) != ~0u)) break;
      }
    }
  }
  float o[8];
#pragma unroll
  for (int i = 0; i < 8; ++i) o[i] = acc[0][i];
#pragma unroll
  for (int d = 0; d < 7; ++d)
#pragma unroll
    for (int i = 0; i < 4; ++i) {
      o[2 * i] += __uint_as_float(pv[d][i] << 16);
      o[2 * i + 1] += __uint_as_float(pv[d][i] & 0xffff0000u);
    }
  u32x4 ov = {pk2(o[0], o[1]), pk2(o[2], o[3]), pk2(o[4], o[5]), pk2(o[6], o[7])};
  if (OUTSC) __builtin_amdgcn_raw_buffer_store_b128(ov, __builtin_amdgcn_make_buffer_rsrc(out, 0, 1u << 20, 0x00020000), (bp * 128 + w * 32 + r) * 1024 + hh * 256 + j * 32 + hf * 16, 0, OUTSC);
  else *(GAS u32x4*)(out + (size_t)(bp * 128 + w * 32 + r) * 1024 + hh * 256 + j * 32 + hf * 16) = ov;
  if (j == 0 && hh == 0 && lane == 0) __hip_atomic_store((GAS u64*)(ws + WS_EPOCH + grp * 128), tag, RLX_AGENT);
}

extern "C" void kernel_launch(void* const* d_in, const int* in_sizes, int n_in, void* d_out, int out_size, void* d_ws, size_t ws_size,
                              hipStream_t stream) {
  gqa_fused<<<256, 512, 0, stream>>>((const char*)d_in[0], (const char*)d_in[1], (const char*)d_in[2], (const char*)d_in[3],
                                     (const char*)d_in[4], (char*)d_out, (char*)d_ws);
}
```

```cpp
#include <hip/hip_runtime.h>
#include <hip/hip_bf16.h>

typedef unsigned short u16;
typedef unsigned long long u64;
typedef short s16x4 __attribute__((ext_vector_type(4)));
typedef short s16x8 __attribute__((ext_vector_type(8)));
typedef __bf16 bf16x8 __attribute__((ext_vector_type(8)));
typedef __bf16 bf16x2 __attribute__((ext_vector_type(2)));
typedef float f32x2 __attribute__((ext_vector_type(2)));
typedef float f32x16 __attribute__((ext_vector_type(16)));
typedef unsigned u32x4 __attribute__((ext_vector_type(4)));
#define LAS __attribute__((address_space(3)))
#define GAS __attribute__((address_space(1)))
#define DI __device__ __forceinline__
#define RLX_AGENT __ATOMIC_RELAXED, __HIP_MEMORY_SCOPE_AGENT
#define SB() __builtin_amdgcn_sched_barrier(0)
#define AI __attribute__((always_inline))

#define STAMPS 0
#define ROT 3
#define FL2 92
#define CODEW 1
#define NF0 10
#define FSTEP 2
#define OUTSC 16
#define PAUX 16
#define OAUX3 16
#define QNOP 5
#define QFROM 0
#define BVM 8
#define TA 1
#define PWQ 1
#define PX 0
#define SAUX1 16
#define SAUX2 16
#define PTN 0
#define WQN 2
#define STG 1
#define WOP 1
#define DRAIN1 16
#define STAMP(i) do { } while (0)

constexpr size_t WS_O     = 0;
constexpr size_t WS_P     = 8u << 20;
constexpr size_t WS_FLAG  = 16u << 20;
constexpr size_t WS_EPOCH = WS_FLAG + (512u << 10);
constexpr size_t WS_STAMP = 32u << 20;

constexpr unsigned L_K = 3u * 32768, L_V = 4u * 32768;
constexpr unsigned LDS_BYTES = 160u << 10;

DI void dma16(const char* sbase, unsigned voff, unsigned lds) {
  asm volatile("s_mov_b32 m0, %2\n\ts_nop 0\n\tglobal_load_lds_dwordx4 %0, %1" ::"v"(voff), "s"(sbase), "s"(lds) : "memory");
}
DI void dma16nt(const char* sbase, unsigned voff, unsigned lds) {
  asm volatile("s_mov_b32 m0, %2\n\ts_nop 0\n\tglobal_load_lds_dwordx4 %0, %1 nt" ::"v"(voff), "s"(sbase), "s"(lds) : "memory");
}
template <int P> DI void dma16p(const char* sbase, unsigned voff, unsigned lds) {
  if (P == 0) dma16(sbase, voff, lds);
  else if (P == 1) dma16nt(sbase, voff, lds);
  else if (P == 2) asm volatile("s_mov_b32 m0, %2\n\ts_nop 0\n\tglobal_load_lds_dwordx4 %0, %1 sc1" ::"v"(voff), "s"(sbase), "s"(lds) : "memory");
  else if (P == 3) asm volatile("s_mov_b32 m0, %2\n\ts_nop 0\n\tglobal_load_lds_dwordx4 %0, %1 sc0 sc1" ::"v"(voff), "s"(sbase), "s"(lds) : "memory");
  else asm volatile("s_mov_b32 m0, %2\n\ts_nop 0\n\tglobal_load_lds_dwordx4 %0, %1 sc0" ::"v"(voff), "s"(sbase), "s"(lds) : "memory");
}
DI void dma4(const char* sbase, unsigned voff, unsigned lds) {
  if (PTN) asm volatile("s_mov_b32 m0, %2\n\ts_nop 0\n\tglobal_load_lds_dword %0, %1 nt" ::"v"(voff), "s"(sbase), "s"(lds) : "memory");
  else asm volatile("s_mov_b32 m0, %2\n\ts_nop 0\n\tglobal_load_lds_dword %0, %1" ::"v"(voff), "s"(sbase), "s"(lds) : "memory");
}
DI void pin(float& v) { asm volatile("" : "+v"(v)); }
DI void pin64(u64& v) { asm volatile("" : "+v"(v)); }
DI u64 ld_flag(const GAS u64* q) { return __hip_atomic_load(q, RLX_AGENT); }
template <int N> DI void nop() { asm volatile("s_nop %0" ::"i"(N)); }
DI void drain_vm() { asm volatile("s_waitcnt vmcnt(0)" ::: "memory"); }
template <int N> DI void wait_vm_bar() { asm volatile("s_waitcnt vmcnt(%0)\n\ts_barrier" ::"i"(N) : "memory"); }
DI void bar_only() { SB(); asm volatile("s_barrier" ::: "memory"); SB(); }
DI void lgkm_bar() { SB(); asm volatile("s_waitcnt lgkmcnt(0)\n\ts_barrier" ::: "memory"); SB(); }

DI u64 rtime() { u64 t; asm volatile("s_memrealtime %0\n\ts_waitcnt lgkmcnt(0)" : "=s"(t)::"memory"); return t; }
DI unsigned ctime_() { u64 t; asm volatile("s_memtime %0\n\ts_waitcnt lgkmcnt(0)" : "=s"(t)::"memory"); return (unsigned)t; }

DI bf16x8 tr_frag(const LAS char* p) {
  s16x4 lo = __builtin_amdgcn_ds_read_tr16_b64_v4i16((LAS s16x4*)p);
  s16x4 hi = __builtin_amdgcn_ds_read_tr16_b64_v4i16((LAS s16x4*)(p + 1024));
  return __builtin_bit_cast(bf16x8, __builtin_shufflevector(lo, hi, 0, 1, 2, 3, 4, 5, 6, 7));
}
DI unsigned pk2(float a, float b) { f32x2 v = {a, b}; return __builtin_bit_cast(unsigned, __builtin_convertvector(v, bf16x2)); }
DI bf16x8 pack8(const f32x16& a, int s) {
  u32x4 r = {pk2(a[8 * s], a[8 * s + 1]), pk2(a[8 * s + 2], a[8 * s + 3]), pk2(a[8 * s + 4], a[8 * s + 5]), pk2(a[8 * s + 6], a[8 * s + 7])};
  return __builtin_bit_cast(bf16x8, r);
}
DI u32x4 not_ones(u32x4 v) {
  return u32x4{min(v[0], 0xfffffffeu), min(v[1], 0xfffffffeu), min(v[2], 0xfffffffeu), min(v[3], 0xfffffffeu)};
}
DI u32x4 pack8s(const f32x16& a, int s, float k) {
  float t[8];
#pragma unroll
  for (int i = 0; i < 8; ++i) t[i] = a[8 * s + i] * k;
  SB();
  return u32x4{pk2(t[0], t[1]), pk2(t[2], t[3]), pk2(t[4], t[5]), pk2(t[6], t[7])};
}
DI unsigned umax4(unsigned m, u32x4 v) { return max(max(max(m, v[0]), v[1]), max(v[2], v[3])); }
template <int I> struct IC { static constexpr int v = I; };
template <int B, int E, class F> DI void sfor(F&& f) { if constexpr (B < E) { f(IC<B>{}); sfor<B + 1, E>(f); } }
#define MFMA(a, b, c) __builtin_amdgcn_mfma_f32_32x32x16_bf16((a), (b), (c), 0, 0, 0)
#define BF(x) __builtin_bit_cast(bf16x8, x)
constexpr unsigned SPIN_CAP = 1u << 20;

DI void wait_flags(const GAS u64* p, int lane, int n, u64 tag, int stride = 1) {
  const GAS u64* q = p + (lane < n ? lane : 0) * stride;
#pragma clang loop unroll(disable)
  for (unsigned spins = 0; spins < SPIN_CAP; ++spins) {
    u64 v = __hip_atomic_load(q, RLX_AGENT);
    if (__all(v == tag)) break;
    __builtin_amdgcn_s_sleep(1);
  }
  asm volatile("" ::: "memory");
}

__global__ __launch_bounds__(512, 2) void gqa_fused(const char* __restrict__ x, const char* __restrict__ kc, const char* __restrict__ vc,
                                                   const char* __restrict__ wq, const char* __restrict__ wo, char* __restrict__ out, char* ws) {
  __shared__ __attribute__((aligned(16))) char lds_raw[LDS_BYTES];
  const LAS char* lds = (const LAS char*)lds_raw;
  const unsigned lds0 = (unsigned)(size_t)lds;
  const int lane = threadIdx.x & 63;
  const int w8 = __builtin_amdgcn_readfirstlane(threadIdx.x >> 6);
  const int j = blockIdx.x & 7, bp = blockIdx.x >> 5, hh = (blockIdx.x >> 3) & 3, h = 4 * j + hh;

  const bool early = STG == 0 ? false : STG == 1 ? (bp & 1) : STG == 2 ? ((bp ^ hh) & 1) : true;
  if (w8 >= 4) {
    const int lw = w8 - 4;
    const int rot = ROT == 0 ? 0 : ROT == 1 ? (((bp ^ hh) & 1) << 2) : ROT == 2 ? (((bp + hh) & 3) << 1) : ((bp + 2 * hh + (j & 1)) & 7);
    const unsigned l4 = lane >> 4, l3 = lane >> 3;
    const unsigned vx0 = l3 * 1024 + (((lane & 7) ^ l4) << 4), vx1 = vx0 ^ 64;
    const unsigned ptr_ = (lane & 15) ^ (l4 << 2);
    const unsigned vwq = l4 * 8192 + (ptr_ << 4);
    const unsigned vv = l4 * 256 + (ptr_ << 4);
    const unsigned vwo = l4 * 1024 + ((ptr_ ^ (unsigned)(j << 1)) << 4);
    const unsigned vk = l4 * 256 + (((lane & 15) ^ l4) << 4);
    const char* xb = x + (size_t)(bp * 128 + lw * 32) * 1024;
    const char* wqb = wq + (size_t)(lw * 16) * 8192 + h * 256;
    const size_t kvoff = (size_t)((bp * 2 + (lw >> 1)) * 32 + h) * 16384 + (lw & 1) * 8192;
    const char* kb = kc + kvoff;
    const char* vb = vc + kvoff;
    const char* wob = wo + (size_t)(j * 512 + lw * 16) * 1024 + hh * 256;

    auto chunk = [&](int c, int sl) AI {
      const int cc = (c + rot) & 7;
      const unsigned slot = lds0 + sl * 32768;
#pragma unroll
      for (int q = 0; q < 4; ++q) {
        if (c < WQN) dma16p<PWQ>(wqb + (size_t)(cc * 64 + q * 4) * 8192, vwq, slot + 16384 + (lw * 16 + q * 4) * 256);
        else dma16(wqb + (size_t)(cc * 64 + q * 4) * 8192, vwq, slot + 16384 + (lw * 16 + q * 4) * 256);
        if (c < 2) dma16p<PX>(xb + q * 8192 + cc * 128, (q & 1) ? vx1 : vx0, slot + (lw * 32 + q * 8) * 128);
        else dma16(xb + q * 8192 + cc * 128, (q & 1) ? vx1 : vx0, slot + (lw * 32 + q * 8) * 128);
      }
    };
    auto loadK = [&]() AI {
      if (TA) dma4(wob + (size_t)((bp >> 1) * 128) * 1024, early ? 0 : (lane >> 5) * 65536 + ((lane & 31) >> 1) * 1024 + (lane & 1) * 128, lds0 + L_K + lw * 8192);
      else dma4(wob + (size_t)(bp * 64) * 1024, ((lane & 31) >> 1) * 1024 + (lane & 1) * 128, lds0 + L_K + lw * 8192);
#pragma unroll
      for (int i = 0; i < 8; ++i) dma16nt(kb + i * 1024, vk ^ ((i & 3) << 6), lds0 + L_K + lw * 8192 + i * 1024);
    };
    auto loadV = [&]() AI {
#pragma unroll
      for (int i = 0; i < 8; ++i) dma16nt(vb + i * 1024, vv, lds0 + L_V + lw * 8192 + i * 1024);
    };
    auto loadWo = [&](int g) AI {
      const char* b = wob + (size_t)((hh ^ g) * 128) * 1024;
#pragma unroll
      for (int q = 0; q < 8; ++q)
        dma16(b + (size_t)((q >> 2) * 64 + (q & 3) * 4) * 1024, vwo, lds0 + g * 32768 + ((q >> 2) * 64 + lw * 16 + (q & 3) * 4) * 256);
    };
    chunk(0, 0); chunk(1, 1);
    wait_vm_bar<8>(); chunk(2, 2);
    if (early) {
      wait_vm_bar<0>(); loadK(); loadV(); chunk(3, 0);
      wait_vm_bar<0>(); chunk(4, 1); chunk(5, 2);
      wait_vm_bar<8>(); chunk(6, 0);
      wait_vm_bar<8>(); chunk(7, 1);
      wait_vm_bar<8>(); loadWo(2);
      wait_vm_bar<8>(); loadWo(0);
      wait_vm_bar<63>(); loadWo(1);
      wait_vm_bar<63>(); if (!WOP) loadWo(3);
      wait_vm_bar<WOP ? 0 : 8>(); if (WOP) loadWo(3);
    } else {
      wait_vm_bar<8>(); chunk(3, 3); chunk(4, 4);
      wait_vm_bar<0>(); chunk(5, 0); chunk(6, 1);
      wait_vm_bar<63>(); chunk(7, 2);
      wait_vm_bar<63>(); loadK();
      wait_vm_bar<9>(); loadV();
      wait_vm_bar<8>(); loadWo(0);
      wait_vm_bar<8>(); if (!WOP) { loadWo(1); loadWo(2); }
      wait_vm_bar<WOP ? 0 : 16>(); if (WOP) { loadWo(1); loadWo(2); } loadWo(3);
    }
    wait_vm_bar<0>();
    asm volatile(".Lgqa_loader_end:");
    return;
  }

  unsigned codew = 0;
  if (CODEW) {
    u64 pc;
    int len;
    asm volatile("s_getpc_b64 %0\n1:\n\ts_mov_b32 %1, .Lgqa_loader_end-1b" : "=s"(pc), "=s"(len));
    const __amdgpu_buffer_rsrc_t cr = __builtin_amdgcn_make_buffer_rsrc((char*)pc, 0, len > 0 ? len : 0, 0x00020000);
    codew = __builtin_amdgcn_raw_buffer_load_b32(cr, lane < 2 ? (((blockIdx.x >> 3) * 4 + w8) * 2 + lane) * 128 : 0x7ffffff0, 0, 0);
  }
  const int w = w8;
  const int r = lane & 31, hf = lane >> 5;
  const int grp = bp * 4 + w;
  const int me = (grp * 8 + j) * 4 + hh;
  const u64 tag = __hip_atomic_load((const GAS u64*)(ws + WS_EPOCH + grp * 128), RLX_AGENT) + 1;
  const __amdgpu_buffer_rsrc_t wsr = __builtin_amdgcn_make_buffer_rsrc(ws, 0, 16u << 20, 0x00020000);
  const GAS u64* myflag = (const GAS u64*)(ws + WS_FLAG + (size_t)me * 512);
  { u64 warm = __hip_atomic_load(myflag + (lane & 3) * 16, RLX_AGENT); asm volatile("" ::"v"(warm)); }
  __builtin_amdgcn_raw_buffer_store_b128(u32x4{~0u, ~0u, ~0u, ~0u}, wsr, lane * 16 + 1024, (unsigned)WS_P + (me ^ 4) * 8192, 16);

  const unsigned q4 = (lane & 15) >> 2, p4 = lane & 3, blk = (lane >> 4) & 1;
  const unsigned trc = (8 * hf + q4) * 256 + 32 * blk + 8 * (((p4 & 1) << 1) | (p4 >> 1));
  unsigned trb[4];
#pragma unroll
  for (int c = 0; c < 4; ++c) trb[c] = trc + ((c ^ q4) << 6);
  const unsigned xsw = (r >> 1) & 7;
  unsigned xfb[4];
#pragma unroll
  for (int s = 0; s < 4; ++s) xfb[s] = (w * 32 + r) * 128 + (((2 * s + hf) ^ xsw) << 4);
  const int kr = (r & 19) | ((r & 4) << 1) | ((r & 8) >> 1);
  const LAS char* kp = lds + L_K + (w >> 1) * 16384 + kr * 256;

  f32x16 acc[4], sc[2];
#pragma unroll
  for (int c = 0; c < 4; ++c)
#pragma unroll
    for (int i = 0; i < 16; ++i) acc[c][i] = 0.f;
#pragma unroll
  for (int t = 0; t < 2; ++t)
#pragma unroll
    for (int i = 0; i < 16; ++i) sc[t][i] = 0.f;

  bf16x8 A[8];
  bf16x8 f[8];
  bf16x8 kf[16];
  auto s_t = [](int m) AI { return (m < 6 || m == 8 || m == 9) ? 0 : 1; };
  auto s_s = [](int m) AI { return m < 6 ? m : m < 8 ? m - 6 : m < 10 ? m - 2 : m - 8; };
  auto rdk = [&](int i) AI { kf[i] = *(const LAS bf16x8*)(kp + s_t(i) * 8192 + (((2 * s_s(i) + hf) ^ (kr & 15)) << 4)); };
  {
    bf16x8 B[8];
    auto stp = [](int m) AI { return m < 112 ? (m >> 2) & 3 : m & 3; };
    auto til = [](int m) AI { return m < 112 ? m & 3 : (m >> 2) & 3; };
    auto rd = [&](int m) AI {
      const int c = m >> 4, s = stp(m), t = til(m);
      const unsigned slot = c < 3 ? c * 32768u : early ? (c % 3) * 32768u : (c % 5) * 32768u;
      if (t == 0) B[(c & 1) * 4 + s] = *(const LAS bf16x8*)(lds + slot + xfb[s]);
      A[m & 7] = tr_frag(lds + slot + 16384 + s * 4096 + trb[t]);
    };
    bar_only();
    STAMP(1);
#pragma unroll
    for (int m = 0; m < 6; ++m) rd(m);
    SB();
    sfor<0, 128>([&](auto ic) AI {
      constexpr int m = decltype(ic)::v;
      acc[til(m)] = MFMA(A[m & 7], B[((m >> 4) & 1) * 4 + stp(m)], acc[til(m)]);
      SB();
      constexpr int k = m & 15;
      if constexpr (QNOP >= 0 && m >= QFROM) { nop<(QNOP > 15 ? 15 : QNOP)>(); if constexpr (QNOP > 15) nop<QNOP - 16>(); }
      if constexpr (m < 112) {
        if (k < 10) rd(m + 6);
        else if (k == 12) {
          if ((m >> 4) == 1 ? !early : (m >> 4) >= 5 ? early : true) lgkm_bar();
          SB();
          rd(m + 4); rd(m + 5);
        }
        else if (k == 13) { rd(m + 5); rd(m + 6); }
        else if (k > 13) rd(m + 6);
      } else {
        if (m + 6 < 128) rd(m + 6);
        if constexpr (k == 12) lgkm_bar();
        if constexpr (k >= 12) { rdk(2 * k - 24); rdk(2 * k - 23); }
        if constexpr (k >= 5 && (k & 1)) f[(k - 5) >> 1] = pack8(acc[(k - 5) >> 2], ((k - 5) >> 1) & 1);
      }
      SB();
    });
  }
  STAMP(2);
  const LAS char* vp = lds + L_V + (w >> 1) * 16384;
  constexpr float C = 0.08838834764831845f * 1.4426950408889634f;
  auto o_s = [](int m) AI { return m < 8 ? m >> 2 : 2 + (m & 1); };
  auto o_t = [](int m) AI { return m < 8 ? m & 3 : (m - 8) >> 1; };
  auto rdv = [&](int m) AI { A[m & 7] = tr_frag(vp + o_s(m) * 4096 + trb[o_t(m)]); };
  float m0;
  sfor<0, 16>([&](auto ic) AI {
    constexpr int m = decltype(ic)::v;
    sc[s_t(m)] = MFMA(kf[m], f[s_s(m)], sc[s_t(m)]);
    SB();
    if constexpr (m < 4) { rdk(8 + 2 * m); rdk(9 + 2 * m); }
    if constexpr (m == 1) f[6] = pack8(acc[3], 0);
    if constexpr (m == 2) f[7] = pack8(acc[3], 1);
    if constexpr (m == BVM) lgkm_bar();
    if constexpr (m >= BVM && m < BVM + 7 && BVM < 10) rdv(m - BVM);
    if constexpr (m == 11) {
      m0 = sc[0][0];
#pragma unroll
      for (int i = 1; i < 16; ++i) m0 = fmaxf(m0, sc[0][i]);
      m0 *= C;
    }
    if constexpr (m >= 12) {
#pragma unroll
      for (int i = 4 * (m - 12); i < 4 * (m - 11); ++i) sc[0][i] = __builtin_amdgcn_exp2f(fmaf(sc[0][i], C, -m0));
    }
    SB();
  });
  if constexpr (BVM >= 10) {
#pragma unroll
    for (int m = 0; m < 7; ++m) { rdv(m); SB(); }
  }
  STAMP(3);
  float sum = 0.f, mx, inv;
#pragma unroll
  for (int i = 0; i < 16; ++i) sum += sc[0][i];
  pin(sum);
  {
    mx = sc[1][0];
#pragma unroll
    for (int i = 1; i < 16; ++i) mx = fmaxf(mx, sc[1][i]);
    mx = fmaxf(mx * C, m0);
    auto rr = __builtin_amdgcn_permlane32_swap(__float_as_uint(mx), __float_as_uint(mx), false, false);
    mx = fmaxf(__uint_as_float(rr[0]), __uint_as_float(rr[1]));
  }
  const float a0 = __builtin_amdgcn_exp2f(m0 - mx);
  sum *= a0;
  f[0] = BF(pack8s(sc[0], 0, a0));
  STAMP(4);
  STAMP(5);
#pragma unroll
  for (int c = 0; c < 4; ++c)
#pragma unroll
    for (int i = 0; i < 16; ++i) acc[c][i] = 0.f;
  u32x4 of[4][8];
  {
    auto put = [&](int s) AI {
      of[0][s] = pack8s(acc[s >> 1], s & 1, inv);
      __builtin_amdgcn_raw_buffer_store_b128(of[0][s], wsr, lane * 16 + s * 1024, me * 8192, SAUX1);
    };
    auto ex = [&](int i0, int i1) AI {
#pragma unroll
      for (int i = i0; i < i1; ++i) sc[1][i] = __builtin_amdgcn_exp2f(fmaf(sc[1][i], C, -mx));
    };
    SB();
    sfor<0, 16>([&](auto ic) AI {
      constexpr int m = decltype(ic)::v;
      acc[o_t(m)] = MFMA(A[m & 7], f[o_s(m)], acc[o_t(m)]);
      SB();
      if constexpr (m + 7 < 16) rdv(m + 7);
      if constexpr (m == 0) f[1] = BF(pack8s(sc[0], 1, a0));
      if constexpr (m >= 1 && m < 7) ex((m - 1) * 8 / 3, m * 8 / 3);
      if constexpr (m == 4) f[2] = pack8(sc[1], 0);
      if constexpr (m == 7) f[3] = pack8(sc[1], 1);
      if constexpr (m == 7 || m == 8) {
#pragma unroll
        for (int i = 8 * (m - 7); i < 8 * (m - 6); ++i) sum += sc[1][i];
      }
      if constexpr (m == 9) {
        auto r2 = __builtin_amdgcn_permlane32_swap(__float_as_uint(sum), __float_as_uint(sum), false, false);
        inv = __builtin_amdgcn_rcpf(__uint_as_float(r2[0]) + __uint_as_float(r2[1]));
      }
      if constexpr (m >= 11) put(m - 11);
      SB();
    });
    put(5); SB(); put(6); SB(); put(7); SB();
  }
  STAMP(6);
#pragma unroll
  for (int c = 0; c < 4; ++c)
#pragma unroll
    for (int i = 0; i < 16; ++i) acc[c][i] = 0.f;
  u64 early_poll;
  asm volatile("" ::"v"(codew));
  bar_only();
  {
    auto rd = [&](int m) AI { A[m & 7] = tr_frag(lds + (m >> 2) * 4096 + trb[m & 3]); };
#pragma unroll
    for (int m = 0; m < 7; ++m) { rd(m); SB(); }
    sfor<0, 32>([&](auto ic) AI {
      constexpr int m = decltype(ic)::v;
      acc[m & 3] = MFMA(A[m & 7], BF(of[0][m >> 2]), acc[m & 3]);
      SB();
      if constexpr (m + 7 < 32) rd(m + 7);
      if constexpr (m == DRAIN1) {
        drain_vm();
        if (lane >= 1 && lane < 5)
          __hip_atomic_store((GAS u64*)(ws + WS_FLAG + (size_t)(me ^ lane) * 512) + (lane - 1) * 16, tag, RLX_AGENT);
        early_poll = ld_flag(myflag + (lane < 3 ? lane : 0) * 16);
      }
      SB();
    });
  }
  STAMP(7);
  pin64(early_poll);
  if (!__all(early_poll == tag)) wait_flags(myflag, lane, 3, tag, 16);
  asm volatile("" ::: "memory");
  STAMP(8);
  auto fetch = [&](int i) AI {
    if (i >= 16) of[1 + (i >> 3)][i & 7] = __builtin_amdgcn_raw_buffer_load_b128(wsr, lane * 16 + (i & 7) * 1024, (me ^ (1 + (i >> 3))) * 8192, OAUX3);
    else of[1 + (i >> 3)][i & 7] = __builtin_amdgcn_raw_buffer_load_b128(wsr, lane * 16 + (i & 7) * 1024, (me ^ (1 + (i >> 3))) * 8192, 16);
  };
#pragma unroll
  for (int i = 0; i < NF0; ++i) { fetch(i); SB(); }
  bar_only();
  {
    auto frg = [](int m) AI { return m < 48 ? m / 3 : m < 72 ? 16 + (m & 7) : m - 72; };
    auto til = [](int m) AI { return m < 48 ? 3 - m % 3 : m < 72 ? 3 - ((m - 48) >> 3) : 0; };
    auto rd = [&](int m) AI { A[m & 7] = tr_frag(lds + 32768 + frg(m) * 4096 + trb[til(m)]); };
#pragma unroll
    for (int m = 0; m < 7; ++m) { rd(m); SB(); }
    sfor<0, 96>([&](auto ic) AI {
      constexpr int m = decltype(ic)::v;
      acc[til(m)] = MFMA(A[m & 7], BF(of[1 + (frg(m) >> 3)][frg(m) & 7]), acc[til(m)]);
      SB();
      if constexpr (m + 7 < 96) rd(m + 7);
      if constexpr (m % FSTEP == 0 && NF0 + m / FSTEP < 24) fetch(NF0 + m / FSTEP);
      if constexpr (m == 40) early_poll = ld_flag(myflag + 48);
      if constexpr (m >= 56 && m < 80 && ((m & 7) == 1 || (m & 7) == 3)) {
        constexpr int d = 2 * (3 - ((m - 56) >> 3)) + ((m & 7) == 1 ? 1 : 0);
        __builtin_amdgcn_raw_buffer_store_b128(__builtin_bit_cast(u32x4, pack8(acc[d >> 1], d & 1)), wsr, lane * 16 + d * 1024,
                                               (unsigned)WS_P + (me ^ (d << 2)) * 8192, SAUX2);
      }
      if constexpr (m == FL2) {
        drain_vm();
        if (lane >= 2 && lane < 8)
          __hip_atomic_store((GAS u64*)(ws + WS_FLAG + (size_t)(me ^ (lane << 2)) * 512) + 48 + (lane - 1), tag, RLX_AGENT);
      }
      SB();
    });
    __builtin_amdgcn_raw_buffer_store_b128(not_ones(__builtin_bit_cast(u32x4, pack8(acc[0], 1))), wsr, lane * 16 + 1024, (unsigned)WS_P + (me ^ 4) * 8192, 16);
    if (FL2 >= 96) {
      asm volatile("s_waitcnt vmcnt(1)" ::: "memory");
      if (lane >= 2 && lane < 8)
        __hip_atomic_store((GAS u64*)(ws + WS_FLAG + (size_t)(me ^ (lane << 2)) * 512) + 48 + (lane - 1), tag, RLX_AGENT);
    }
  }
  STAMP(9);
  pin64(early_poll);
  if (!__all(early_poll == tag)) wait_flags(myflag + 48, lane, 1, tag);
  u32x4 pv[7];
  {
    const GAS u64* q = myflag + 48 + (lane < 7 ? lane : 0);
    bool ok1 = false;
#pragma clang loop unroll(disable)
    for (unsigned spins = 0; spins < SPIN_CAP; ++spins) {
      const u64 v = ld_flag(q);
      pv[0] = __builtin_amdgcn_raw_buffer_load_b128(wsr, lane * 16 + 1024, (unsigned)WS_P + me * 8192, 16);
      SB();
      if (__all(v == tag)) break;
      __builtin_amdgcn_s_sleep(1);
    }
    asm volatile("" ::: "memory");
    STAMP(10);
#pragma unroll
    for (int d = 2; d < 8; ++d) pv[d - 1] = __builtin_amdgcn_raw_buffer_load_b128(wsr, lane * 16 + d * 1024, (unsigned)WS_P + me * 8192, PAUX);
    SB();
    ok1 = __all(umax4(0, pv[0]) != ~0u);
    if (!ok1) {
#pragma clang loop unroll(disable)
      for (unsigned spins = 0; spins < SPIN_CAP; ++spins) {
        asm volatile("" ::: "memory");
        pv[0] = __builtin_amdgcn_raw_buffer_load_b128(wsr, lane * 16 + 1024, (unsigned)WS_P + me * 8192, 16);
        if (__all(umax4(0, pv[0]) != ~0u)) break;
      }
    }
  }
  float o[8];
#pragma unroll
  for (int i = 0; i < 8; ++i) o[i] = acc[0][i];
#pragma unroll
  for (int d = 0; d < 7; ++d)
#pragma unroll
    for (int i = 0; i < 4; ++i) {
      o[2 * i] += __uint_as_float(pv[d][i] << 16);
      o[2 * i + 1] += __uint_as_float(pv[d][i] & 0xffff0000u);
    }
  u32x4 ov = {pk2(o[0], o[1]), pk2(o[2], o[3]), pk2(o[4], o[5]), pk2(o[6], o[7])};
  if (OUTSC) __builtin_amdgcn_raw_buffer_store_b128(ov, __builtin_amdgcn_make_buffer_rsrc(out, 0, 1u << 20, 0x00020000), (bp * 128 + w * 32 + r) * 1024 + hh * 256 + j * 32 + hf * 16, 0, OUTSC);
  else *(GAS u32x4*)(out + (size_t)(bp * 128 + w * 32 + r) * 1024 + hh * 256 + j * 32 + hf * 16) = ov;
  if (j == 0 && hh == 0 && lane == 0) __hip_atomic_store((GAS u64*)(ws + WS_EPOCH + grp * 128), tag, RLX_AGENT);
}

extern "C" void kernel_launch(void* const* d_in, const int* in_sizes, int n_in, void* d_out, int out_size, void* d_ws, size_t ws_size,
                              hipStream_t stream) {
  gqa_fused<<<256, 512, 0, stream>>>((const char*)d_in[0], (const char*)d_in[1], (const char*)d_in[2], (const char*)d_in[3],
                                     (const char*)d_in[4], (char*)d_out, (char*)d_ws);
}
```

```cpp
#include <hip/hip_runtime.h>
#include <hip/hip_bf16.h>

typedef unsigned long long u64;
typedef short s16x4 __attribute__((ext_vector_type(4)));
typedef __bf16 bf16x8 __attribute__((ext_vector_type(8)));
typedef __bf16 bf16x2 __attribute__((ext_vector_type(2)));
typedef float f32x2 __attribute__((ext_vector_type(2)));
typedef float f32x16 __attribute__((ext_vector_type(16)));
typedef unsigned u32x4 __attribute__((ext_vector_type(4)));
#define LAS __attribute__((address_space(3)))
#define GAS __attribute__((address_space(1)))
#define DI __device__ __forceinline__
#define AI __attribute__((always_inline))
#define RLX_AGENT __ATOMIC_RELAXED, __HIP_MEMORY_SCOPE_AGENT
#define SB() __builtin_amdgcn_sched_barrier(0)
#define MFMA(a, b, c) __builtin_amdgcn_mfma_f32_32x32x16_bf16((a), (b), (c), 0, 0, 0)
#define BF(x) __builtin_bit_cast(bf16x8, x)
constexpr int SC1 = 16;

constexpr unsigned WS_P     = 8u << 20;
constexpr unsigned WS_FLAG  = 16u << 20;
constexpr unsigned WS_EPOCH = WS_FLAG + (512u << 10);
constexpr unsigned L_K = 3u * 32768, L_V = 4u * 32768, LDS_BYTES = 160u << 10;
constexpr unsigned SPIN_CAP = 1u << 20;

DI void dma16(const char* sbase, unsigned voff, unsigned lds) {
  asm volatile("s_mov_b32 m0, %2\n\ts_nop 0\n\tglobal_load_lds_dwordx4 %0, %1" ::"v"(voff), "s"(sbase), "s"(lds) : "memory");
}
DI void dma16nt(const char* sbase, unsigned voff, unsigned lds) {
  asm volatile("s_mov_b32 m0, %2\n\ts_nop 0\n\tglobal_load_lds_dwordx4 %0, %1 nt" ::"v"(voff), "s"(sbase), "s"(lds) : "memory");
}
DI void touch(unsigned& sink, const char* sbase, unsigned voff) {
  asm volatile("global_load_dword %0, %1, %2" : "=v"(sink) : "v"(voff), "s"(sbase) : "memory");
}
DI void pin(float& v) { asm volatile("" : "+v"(v)); }
DI void pin(u64& v) { asm volatile("" : "+v"(v)); }
DI void drain_vm() { asm volatile("s_waitcnt vmcnt(0)" ::: "memory"); }
template <int N> DI void wait_vm_bar() { asm volatile("s_waitcnt vmcnt(%0)\n\ts_barrier" ::"i"(N) : "memory"); }
DI void bar_only() { SB(); asm volatile("s_barrier" ::: "memory"); SB(); }
DI void lgkm_bar() { SB(); asm volatile("s_waitcnt lgkmcnt(0)\n\ts_barrier" ::: "memory"); SB(); }
DI u64 ld_flag(const GAS u64* q) { return __hip_atomic_load(q, RLX_AGENT); }

DI bf16x8 tr_frag(const LAS char* p) {
  s16x4 lo = __builtin_amdgcn_ds_read_tr16_b64_v4i16((LAS s16x4*)p);
  s16x4 hi = __builtin_amdgcn_ds_read_tr16_b64_v4i16((LAS s16x4*)(p + 1024));
  return __builtin_bit_cast(bf16x8, __builtin_shufflevector(lo, hi, 0, 1, 2, 3, 4, 5, 6, 7));
}
DI unsigned pk2(float a, float b) { f32x2 v = {a, b}; return __builtin_bit_cast(unsigned, __builtin_convertvector(v, bf16x2)); }
DI u32x4 pack8(const f32x16& a, int s) {
  return u32x4{pk2(a[8 * s], a[8 * s + 1]), pk2(a[8 * s + 2], a[8 * s + 3]), pk2(a[8 * s + 4], a[8 * s + 5]), pk2(a[8 * s + 6], a[8 * s + 7])};
}
DI u32x4 pack8s(const f32x16& a, int s, float k) {
  float t[8];
#pragma unroll
  for (int i = 0; i < 8; ++i) t[i] = a[8 * s + i] * k;
  SB();
  return u32x4{pk2(t[0], t[1]), pk2(t[2], t[3]), pk2(t[4], t[5]), pk2(t[6], t[7])};
}
DI u32x4 not_ones(u32x4 v) {
  return u32x4{min(v[0], 0xfffffffeu), min(v[1], 0xfffffffeu), min(v[2], 0xfffffffeu), min(v[3], 0xfffffffeu)};
}
DI bool no_ones(u32x4 v) { return __all(max(max(v[0], v[1]), max(v[2], v[3])) != ~0u); }
template <int I> struct IC { static constexpr int v = I; };
template <int B, int E, class F> DI void sfor(F&& f) { if constexpr (B < E) { f(IC<B>{}); sfor<B + 1, E>(f); } }

DI void wait_flags(const GAS u64* q, u64 tag) {
#pragma clang loop unroll(disable)
  for (unsigned spins = 0; spins < SPIN_CAP; ++spins) {
    if (__all(ld_flag(q) == tag)) break;
    __builtin_amdgcn_s_sleep(1);
  }
  asm volatile("" ::: "memory");
}

__global__ __launch_bounds__(512, 2) void gqa_fused(const char* __restrict__ x, const char* __restrict__ kc, const char* __restrict__ vc,
                                                   const char* __restrict__ wq, const char* __restrict__ wo, char* __restrict__ out, char* ws) {
  __shared__ __attribute__((aligned(16))) char lds_raw[LDS_BYTES];
  const LAS char* lds = (const LAS char*)lds_raw;
  const unsigned lds0 = (unsigned)(size_t)lds;
  const int lane = threadIdx.x & 63;
  const int w8 = __builtin_amdgcn_readfirstlane(threadIdx.x >> 6);
  const int j = blockIdx.x & 7, bp = blockIdx.x >> 5, hh = (blockIdx.x >> 3) & 3, h = 4 * j + hh;
  const bool early = bp & 1;

  if (w8 >= 4) {
    const int lw = w8 - 4;
    const int rot = (bp + 2 * hh + (j & 1)) & 7;
    const unsigned l4 = lane >> 4, l3 = lane >> 3;
    const unsigned vx0 = l3 * 1024 + (((lane & 7) ^ l4) << 4), vx1 = vx0 ^ 64;
    const unsigned pc = (lane & 15) ^ (l4 << 2);
    const unsigned vwq = l4 * 8192 + (pc << 4);
    const unsigned vv = l4 * 256 + (pc << 4);
    const unsigned vwo = l4 * 1024 + ((pc ^ (unsigned)(j << 1)) << 4);
    const unsigned vk = l4 * 256 + (((lane & 15) ^ l4) << 4);
    const char* xb = x + (size_t)(bp * 128 + lw * 32) * 1024;
    const char* wqb = wq + (size_t)(lw * 16) * 8192 + h * 256;
    const size_t kvoff = (size_t)((bp * 2 + (lw >> 1)) * 32 + h) * 16384 + (lw & 1) * 8192;
    const char* kb = kc + kvoff;
    const char* vb = vc + kvoff;
    const char* wob = wo + (size_t)(j * 512 + lw * 16) * 1024 + hh * 256;

    auto chunk = [&](int c, int sl) AI {
      const int cc = (c + rot) & 7;
      const unsigned slot = lds0 + sl * 32768;
#pragma unroll
      for (int q = 0; q < 4; ++q) {
        if (c < 2) dma16nt(wqb + (size_t)(cc * 64 + q * 4) * 8192, vwq, slot + 16384 + (lw * 16 + q * 4) * 256);
        else dma16(wqb + (size_t)(cc * 64 + q * 4) * 8192, vwq, slot + 16384 + (lw * 16 + q * 4) * 256);
        dma16(xb + q * 8192 + cc * 128, (q & 1) ? vx1 : vx0, slot + (lw * 32 + q * 8) * 128);
      }
    };
    auto loadK = [&]() AI {
#pragma unroll
      for (int i = 0; i < 8; ++i) dma16nt(kb + i * 1024, vk ^ ((i & 3) << 6), lds0 + L_K + lw * 8192 + i * 1024);
    };
    auto loadV = [&]() AI {
#pragma unroll
      for (int i = 0; i < 8; ++i) dma16nt(vb + i * 1024, vv, lds0 + L_V + lw * 8192 + i * 1024);
    };
    auto loadWo = [&](int g) AI {
      const char* b = wob + (size_t)((hh ^ g) * 128) * 1024;
#pragma unroll
      for (int q = 0; q < 8; ++q)
        dma16(b + (size_t)((q >> 2) * 64 + (q & 3) * 4) * 1024, vwo, lds0 + g * 32768 + ((q >> 2) * 64 + lw * 16 + (q & 3) * 4) * 256);
    };
    chunk(0, 0); chunk(1, 1);
    wait_vm_bar<8>(); chunk(2, 2);
    if (early) {
      wait_vm_bar<0>(); loadK(); loadV(); chunk(3, 0);
      wait_vm_bar<0>(); chunk(4, 1); chunk(5, 2);
      wait_vm_bar<8>(); chunk(6, 0);
      wait_vm_bar<8>(); chunk(7, 1);
      wait_vm_bar<8>(); loadWo(2);
      wait_vm_bar<8>(); loadWo(0);
      wait_vm_bar<63>(); loadWo(1);
      wait_vm_bar<63>(); loadWo(3);
      wait_vm_bar<0>();
      wait_vm_bar<0>();
    } else {
      unsigned sink;
      wait_vm_bar<8>(); chunk(3, 3); chunk(4, 4);
      wait_vm_bar<0>(); chunk(5, 0); chunk(6, 1);
      wait_vm_bar<63>(); chunk(7, 2);
      wait_vm_bar<63>();
      touch(sink, wob + (size_t)((bp >> 1) * 128) * 1024, (lane >> 5) * 65536 + ((lane & 31) >> 1) * 1024 + (lane & 1) * 128);
      loadK();
      wait_vm_bar<9>(); loadV();
      wait_vm_bar<8>(); loadWo(0);
      wait_vm_bar<8>();
      wait_vm_bar<0>(); loadWo(1); loadWo(2); loadWo(3);
      wait_vm_bar<0>();
      asm volatile("" ::"v"(sink));
    }
    asm volatile(".Lgqa_loader_end:");
    return;
  }

  unsigned codew;
  {
    u64 ip;
    int len;
    asm volatile("s_getpc_b64 %0\n1:\n\ts_mov_b32 %1, .Lgqa_loader_end-1b" : "=s"(ip), "=s"(len));
    const __amdgpu_buffer_rsrc_t cr = __builtin_amdgcn_make_buffer_rsrc((char*)ip, 0, len > 0 ? len : 0, 0x00020000);
    codew = __builtin_amdgcn_raw_buffer_load_b32(cr, lane < 2 ? (((blockIdx.x >> 3) * 4 + w8) * 2 + lane) * 128 : 0x7ffffff0, 0, 0);
  }
  const int w = w8;
  const int r = lane & 31, hf = lane >> 5;
  const int grp = bp * 4 + w;
  const int me = (grp * 8 + j) * 4 + hh;
  const u64 tag = ld_flag((const GAS u64*)(ws + WS_EPOCH + grp * 128)) + 1;
  const __amdgpu_buffer_rsrc_t wsr = __builtin_amdgcn_make_buffer_rsrc(ws, 0, 16u << 20, 0x00020000);
  const GAS u64* myflag = (const GAS u64*)(ws + WS_FLAG + (size_t)me * 512);
  { u64 warm = ld_flag(myflag + (lane & 3) * 16); asm volatile("" ::"v"(warm)); }
  __builtin_amdgcn_raw_buffer_store_b128(u32x4{~0u, ~0u, ~0u, ~0u}, wsr, lane * 16 + 1024, WS_P + (me ^ 4) * 8192, SC1);

  const unsigned q4 = (lane & 15) >> 2, p4 = lane & 3, blk = (lane >> 4) & 1;
  const unsigned trc = (8 * hf + q4) * 256 + 32 * blk + 8 * (((p4 & 1) << 1) | (p4 >> 1));
  unsigned trb[4];
#pragma unroll
  for (int c = 0; c < 4; ++c) trb[c] = trc + ((c ^ q4) << 6);
  unsigned xfb[4];
#pragma unroll
  for (int s = 0; s < 4; ++s) xfb[s] = (w * 32 + r) * 128 + (((2 * s + hf) ^ ((r >> 1) & 7)) << 4);
  const int kr = (r & 19) | ((r & 4) << 1) | ((r & 8) >> 1);
  const LAS char* kp = lds + L_K + (w >> 1) * 16384 + kr * 256;
  const LAS char* vp = lds + L_V + (w >> 1) * 16384;

  f32x16 acc[4], sc[2];
#pragma unroll
  for (int c = 0; c < 4; ++c)
#pragma unroll
    for (int i = 0; i < 16; ++i) acc[c][i] = 0.f;
#pragma unroll
  for (int t = 0; t < 2; ++t)
#pragma unroll
    for (int i = 0; i < 16; ++i) sc[t][i] = 0.f;

  bf16x8 A[8];
  u32x4 f[8];
  bf16x8 kf[16];
  auto s_t = [](int m) AI { return (m < 6 || m == 8 || m == 9) ? 0 : 1; };
  auto s_s = [](int m) AI { return m < 6 ? m : m < 8 ? m - 6 : m < 10 ? m - 2 : m - 8; };
  auto rdk = [&](int i) AI { kf[i] = *(const LAS bf16x8*)(kp + s_t(i) * 8192 + (((2 * s_s(i) + hf) ^ (kr & 15)) << 4)); };

  {
    bf16x8 B[8];
    auto stp = [](int m) AI { return m < 112 ? (m >> 2) & 3 : m & 3; };
    auto til = [](int m) AI { return m < 112 ? m & 3 : (m >> 2) & 3; };
    auto rd = [&](int m) AI {
      const int c = m >> 4, s = stp(m), t = til(m);
      const unsigned slot = c < 3 ? c * 32768u : early ? (c % 3) * 32768u : (c % 5) * 32768u;
      if (t == 0) B[(c & 1) * 4 + s] = *(const LAS bf16x8*)(lds + slot + xfb[s]);
      A[m & 7] = tr_frag(lds + slot + 16384 + s * 4096 + trb[t]);
    };
    bar_only();
#pragma unroll
    for (int m = 0; m < 6; ++m) rd(m);
    SB();
    sfor<0, 128>([&](auto ic) AI {
      constexpr int m = decltype(ic)::v, k = m & 15, c = m >> 4;
      acc[til(m)] = MFMA(A[m & 7], B[(c & 1) * 4 + stp(m)], acc[til(m)]);
      SB();
      if constexpr (c < 7) {
        if (k < 10) rd(m + 6);
        else if (k == 12) {
          if (c == 1 ? !early : c >= 5 ? early : true) lgkm_bar();
          SB();
          if (c >= 4 && !early) __builtin_amdgcn_s_sleep(6);
          SB();
          rd(m + 4); rd(m + 5);
        }
        else if (k == 13) { rd(m + 5); rd(m + 6); }
        else if (k > 13) rd(m + 6);
      } else {
        if (m + 6 < 128) rd(m + 6);
        if constexpr (k == 12) lgkm_bar();
        if constexpr (k >= 12) { rdk(2 * k - 24); rdk(2 * k - 23); }
        if constexpr (k >= 5 && (k & 1)) f[(k - 5) >> 1] = pack8(acc[(k - 5) >> 2], ((k - 5) >> 1) & 1);
      }
      SB();
    });
  }
  constexpr float C = 0.08838834764831845f * 1.4426950408889634f;
  auto o_s = [](int m) AI { return m < 8 ? m >> 2 : 2 + (m & 1); };
  auto o_t = [](int m) AI { return m < 8 ? m & 3 : (m - 8) >> 1; };
  auto rdv = [&](int m) AI { A[m & 7] = tr_frag(vp + o_s(m) * 4096 + trb[o_t(m)]); };
  float m0;
  sfor<0, 16>([&](auto ic) AI {
    constexpr int m = decltype(ic)::v;
    sc[s_t(m)] = MFMA(kf[m], BF(f[s_s(m)]), sc[s_t(m)]);
    SB();
    if constexpr (m < 4) { rdk(8 + 2 * m); rdk(9 + 2 * m); }
    if constexpr (m == 1) f[6] = pack8(acc[3], 0);
    if constexpr (m == 2) f[7] = pack8(acc[3], 1);
    if constexpr (m == 8) lgkm_bar();
    if constexpr (m >= 8 && m < 15) rdv(m - 8);
    if constexpr (m == 11) {
      m0 = sc[0][0];
#pragma unroll
      for (int i = 1; i < 16; ++i) m0 = fmaxf(m0, sc[0][i]);
      m0 *= C;
    }
    if constexpr (m >= 12) {
#pragma unroll
      for (int i = 4 * (m - 12); i < 4 * (m - 11); ++i) sc[0][i] = __builtin_amdgcn_exp2f(fmaf(sc[0][i], C, -m0));
    }
    SB();
  });
  float sum = 0.f, mx, inv;
#pragma unroll
  for (int i = 0; i < 16; ++i) sum += sc[0][i];
  pin(sum);
  {
    mx = sc[1][0];
#pragma unroll
    for (int i = 1; i < 16; ++i) mx = fmaxf(mx, sc[1][i]);
    mx = fmaxf(mx * C, m0);
    auto rr = __builtin_amdgcn_permlane32_swap(__float_as_uint(mx), __float_as_uint(mx), false, false);
    mx = fmaxf(__uint_as_float(rr[0]), __uint_as_float(rr[1]));
  }
  const float a0 = __builtin_amdgcn_exp2f(m0 - mx);
  sum *= a0;
  f[0] = pack8s(sc[0], 0, a0);
#pragma unroll
  for (int c = 0; c < 4; ++c)
#pragma unroll
    for (int i = 0; i < 16; ++i) acc[c][i] = 0.f;
  u32x4 of[4][8];
  {
    auto put = [&](int s) AI {
      of[0][s] = pack8s(acc[s >> 1], s & 1, inv);
      __builtin_amdgcn_raw_buffer_store_b128(of[0][s], wsr, lane * 16 + s * 1024, me * 8192, SC1);
    };
    SB();
    sfor<0, 16>([&](auto ic) AI {
      constexpr int m = decltype(ic)::v;
      acc[o_t(m)] = MFMA(A[m & 7], BF(f[o_s(m)]), acc[o_t(m)]);
      SB();
      if constexpr (m + 7 < 16) rdv(m + 7);
      if constexpr (m == 0) f[1] = pack8s(sc[0], 1, a0);
      if constexpr (m >= 1 && m < 7) {
#pragma unroll
        for (int i = (m - 1) * 8 / 3; i < m * 8 / 3; ++i) sc[1][i] = __builtin_amdgcn_exp2f(fmaf(sc[1][i], C, -mx));
      }
      if constexpr (m == 4) f[2] = pack8(sc[1], 0);
      if constexpr (m == 7) f[3] = pack8(sc[1], 1);
      if constexpr (m == 7 || m == 8) {
#pragma unroll
        for (int i = 8 * (m - 7); i < 8 * (m - 6); ++i) sum += sc[1][i];
      }
      if constexpr (m == 9) {
        auto r2 = __builtin_amdgcn_permlane32_swap(__float_as_uint(sum), __float_as_uint(sum), false, false);
        inv = __builtin_amdgcn_rcpf(__uint_as_float(r2[0]) + __uint_as_float(r2[1]));
      }
      if constexpr (m >= 11) put(m - 11);
      SB();
    });
    put(5); SB(); put(6); SB(); put(7); SB();
  }
#pragma unroll
  for (int c = 0; c < 4; ++c)
#pragma unroll
    for (int i = 0; i < 16; ++i) acc[c][i] = 0.f;
  u64 poll;
  asm volatile("" ::"v"(codew));
  bar_only();
  const GAS u64* fq1 = myflag + (lane < 3 ? lane : 0) * 16;
  {
    auto rd = [&](int m) AI { A[m & 7] = tr_frag(lds + (m >> 2) * 4096 + trb[m & 3]); };
#pragma unroll
    for (int m = 0; m < 7; ++m) { rd(m); SB(); }
    sfor<0, 32>([&](auto ic) AI {
      constexpr int m = decltype(ic)::v;
      acc[m & 3] = MFMA(A[m & 7], BF(of[0][m >> 2]), acc[m & 3]);
      SB();
      if constexpr (m + 7 < 32) rd(m + 7);
      if constexpr (m == 16) {
        drain_vm();
        if (lane >= 1 && lane < 5) __hip_atomic_store((GAS u64*)(ws + WS_FLAG + (size_t)(me ^ lane) * 512) + (lane - 1) * 16, tag, RLX_AGENT);
        poll = ld_flag(fq1);
      }
      SB();
    });
  }
  pin(poll);
  if (!__all(poll == tag)) wait_flags(fq1, tag);
  asm volatile("" ::: "memory");
  auto fetch = [&](int i) AI {
    of[1 + (i >> 3)][i & 7] = __builtin_amdgcn_raw_buffer_load_b128(wsr, lane * 16 + (i & 7) * 1024, (me ^ (1 + (i >> 3))) * 8192, SC1);
  };
#pragma unroll
  for (int i = 0; i < 10; ++i) { fetch(i); SB(); }
  bar_only();
  {
    auto frg = [](int m) AI { return m < 48 ? m / 3 : m < 72 ? 16 + (m & 7) : m - 72; };
    auto til = [](int m) AI { return m < 48 ? 3 - m % 3 : m < 72 ? 3 - ((m - 48) >> 3) : 0; };
    auto rd = [&](int m) AI { A[m & 7] = tr_frag(lds + 32768 + frg(m) * 4096 + trb[til(m)]); };
#pragma unroll
    for (int m = 0; m < 7; ++m) { rd(m); SB(); }
    sfor<0, 96>([&](auto ic) AI {
      constexpr int m = decltype(ic)::v;
      acc[til(m)] = MFMA(A[m & 7], BF(of[1 + (frg(m) >> 3)][frg(m) & 7]), acc[til(m)]);
      SB();
      if constexpr (m + 7 < 96) rd(m + 7);
      if constexpr (m % 2 == 0 && 10 + m / 2 < 24) fetch(10 + m / 2);
      if constexpr (m == 40) poll = ld_flag(myflag + 48);
      if constexpr (m >= 56 && m < 80 && ((m & 7) == 1 || (m & 7) == 3)) {
        constexpr int d = 2 * (3 - ((m - 56) >> 3)) + ((m & 7) == 1 ? 1 : 0);
        __builtin_amdgcn_raw_buffer_store_b128(pack8(acc[d >> 1], d & 1), wsr, lane * 16 + d * 1024, WS_P + (me ^ (d << 2)) * 8192, SC1);
      }
      if constexpr (m == 92) {
        drain_vm();
        if (lane >= 2 && lane < 8) __hip_atomic_store((GAS u64*)(ws + WS_FLAG + (size_t)(me ^ (lane << 2)) * 512) + 48 + (lane - 1), tag, RLX_AGENT);
      }
      SB();
    });
    __builtin_amdgcn_raw_buffer_store_b128(not_ones(pack8(acc[0], 1)), wsr, lane * 16 + 1024, WS_P + (me ^ 4) * 8192, SC1);
  }
  pin(poll);
  if (!__all(poll == tag)) wait_flags(myflag + 48, tag);
  asm volatile("" ::: "memory");
  u32x4 pv[7];
  {
    const GAS u64* q = myflag + 48 + (lane < 7 ? lane : 0);
#pragma clang loop unroll(disable)
    for (unsigned spins = 0; spins < SPIN_CAP; ++spins) {
      const u64 v = ld_flag(q);
      pv[0] = __builtin_amdgcn_raw_buffer_load_b128(wsr, lane * 16 + 1024, WS_P + me * 8192, SC1);
      SB();
      if (__all(v == tag)) break;
      __builtin_amdgcn_s_sleep(1);
    }
    asm volatile("" ::: "memory");
#pragma unroll
    for (int d = 2; d < 8; ++d) pv[d - 1] = __builtin_amdgcn_raw_buffer_load_b128(wsr, lane * 16 + d * 1024, WS_P + me * 8192, SC1);
    SB();
    if (!no_ones(pv[0])) {
#pragma clang loop unroll(disable)
      for (unsigned spins = 0; spins < SPIN_CAP; ++spins) {
        asm volatile("" ::: "memory");
        pv[0] = __builtin_amdgcn_raw_buffer_load_b128(wsr, lane * 16 + 1024, WS_P + me * 8192, SC1);
        if (no_ones(pv[0])) break;
      }
    }
  }
  float o[8];
#pragma unroll
  for (int i = 0; i < 8; ++i) o[i] = acc[0][i];
#pragma unroll
  for (int d = 0; d < 7; ++d)
#pragma unroll
    for (int i = 0; i < 4; ++i) {
      o[2 * i] += __uint_as_float(pv[d][i] << 16);
      o[2 * i + 1] += __uint_as_float(pv[d][i] & 0xffff0000u);
    }
  __builtin_amdgcn_raw_buffer_store_b128(u32x4{pk2(o[0], o[1]), pk2(o[2], o[3]), pk2(o[4], o[5]), pk2(o[6], o[7])},
                                         __builtin_amdgcn_make_buffer_rsrc(out, 0, 1u << 20, 0x00020000),
                                         (bp * 128 + w * 32 + r) * 1024 + hh * 256 + j * 32 + hf * 16, 0, SC1);
  if (j == 0 && hh == 0 && lane == 0) __hip_atomic_store((GAS u64*)(ws + WS_EPOCH + grp * 128), tag, RLX_AGENT);
}

extern "C" void kernel_launch(void* const* d_in, const int* in_sizes, int n_in, void* d_out, int out_size, void* d_ws, size_t ws_size,
                              hipStream_t stream) {
  gqa_fused<<<256, 512, 0, stream>>>((const char*)d_in[0], (const char*)d_in[1], (const char*)d_in[2], (const char*)d_in[3],
                                     (const char*)d_in[4], (char*)d_out, (char*)d_ws);
}
```

```cpp
#include <hip/hip_runtime.h>
#include <hip/hip_bf16.h>

typedef unsigned long long u64;
typedef short s16x4 __attribute__((ext_vector_type(4)));
typedef __bf16 bf16x8 __attribute__((ext_vector_type(8)));
typedef __bf16 bf16x2 __attribute__((ext_vector_type(2)));
typedef float f32x2 __attribute__((ext_vector_type(2)));
typedef float f32x16 __attribute__((ext_vector_type(16)));
typedef unsigned u32x4 __attribute__((ext_vector_type(4)));
#define LAS __attribute__((address_space(3)))
#define GAS __attribute__((address_space(1)))
#define DI __device__ __forceinline__
#define AI __attribute__((always_inline))
#define RLX_AGENT __ATOMIC_RELAXED, __HIP_MEMORY_SCOPE_AGENT
#define SB() __builtin_amdgcn_sched_barrier(0)
#define MFMA(a, b, c) __builtin_amdgcn_mfma_f32_32x32x16_bf16((a), (b), (c), 0, 0, 0)
#define BF(x) __builtin_bit_cast(bf16x8, x)
constexpr int SC1 = 16;

constexpr unsigned WS_P     = 8u << 20;
constexpr unsigned WS_FLAG  = 16u << 20;
constexpr unsigned WS_EPOCH = WS_FLAG + (512u << 10);
constexpr unsigned L_K = 3u * 32768, L_V = 4u * 32768, LDS_BYTES = 160u << 10;
constexpr unsigned SPIN_CAP = 1u << 20;

DI void dma16(const char* sbase, unsigned voff, unsigned lds) {
  asm volatile("s_mov_b32 m0, %2\n\ts_nop 0\n\tglobal_load_lds_dwordx4 %0, %1" ::"v"(voff), "s"(sbase), "s"(lds) : "memory");
}
DI void dma16nt(const char* sbase, unsigned voff, unsigned lds) {
  asm volatile("s_mov_b32 m0, %2\n\ts_nop 0\n\tglobal_load_lds_dwordx4 %0, %1 nt" ::"v"(voff), "s"(sbase), "s"(lds) : "memory");
}
DI void touch(unsigned& sink, const char* sbase, unsigned voff) {
  asm volatile("global_load_dword %0, %1, %2" : "=v"(sink) : "v"(voff), "s"(sbase) : "memory");
}
DI void pin(float& v) { asm volatile("" : "+v"(v)); }
DI void pin(u64& v) { asm volatile("" : "+v"(v)); }
DI void drain_vm() { asm volatile("s_waitcnt vmcnt(0)" ::: "memory"); }
template <int N> DI void wait_vm_bar() { asm volatile("s_waitcnt vmcnt(%0)\n\ts_barrier" ::"i"(N) : "memory"); }
DI void bar_only() { SB(); asm volatile("s_barrier" ::: "memory"); SB(); }
DI void lgkm_bar() { SB(); asm volatile("s_waitcnt lgkmcnt(0)\n\ts_barrier" ::: "memory"); SB(); }
DI u64 ld_flag(const GAS u64* q) { return __hip_atomic_load(q, RLX_AGENT); }

DI bf16x8 tr_frag(const LAS char* p) {
  s16x4 lo = __builtin_amdgcn_ds_read_tr16_b64_v4i16((LAS s16x4*)p);
  s16x4 hi = __builtin_amdgcn_ds_read_tr16_b64_v4i16((LAS s16x4*)(p + 1024));
  return __builtin_bit_cast(bf16x8, __builtin_shufflevector(lo, hi, 0, 1, 2, 3, 4, 5, 6, 7));
}
DI unsigned pk2(float a, float b) { f32x2 v = {a, b}; return __builtin_bit_cast(unsigned, __builtin_convertvector(v, bf16x2)); }
DI u32x4 pack8(const f32x16& a, int s) {
  return u32x4{pk2(a[8 * s], a[8 * s + 1]), pk2(a[8 * s + 2], a[8 * s + 3]), pk2(a[8 * s + 4], a[8 * s + 5]), pk2(a[8 * s + 6], a[8 * s + 7])};
}
DI u32x4 pack8s(const f32x16& a, int s, float k) {
  float t[8];
#pragma unroll
  for (int i = 0; i < 8; ++i) t[i] = a[8 * s + i] * k;
  SB();
  return u32x4{pk2(t[0], t[1]), pk2(t[2], t[3]), pk2(t[4], t[5]), pk2(t[6], t[7])};
}
DI u32x4 not_ones(u32x4 v) {
  return u32x4{min(v[0], 0xfffffffeu), min(v[1], 0xfffffffeu), min(v[2], 0xfffffffeu), min(v[3], 0xfffffffeu)};
}
DI bool no_ones(u32x4 v) { return __all(max(max(v[0], v[1]), max(v[2], v[3])) != ~0u); }
template <int I> struct IC { static constexpr int v = I; };
template <int B, int E, class F> DI void sfor(F&& f) { if constexpr (B < E) { f(IC<B>{}); sfor<B + 1, E>(f); } }

DI void wait_flags(const GAS u64* q, u64 tag) {
#pragma clang loop unroll(disable)
  for (unsigned spins = 0; spins < SPIN_CAP; ++spins) {
    if (__all(ld_flag(q) == tag)) break;
    __builtin_amdgcn_s_sleep(1);
  }
  asm volatile("" ::: "memory");
}

__global__ __launch_bounds__(512, 2) void gqa_fused(const char* __restrict__ x, const char* __restrict__ kc, const char* __restrict__ vc,
                                                   const char* __restrict__ wq, const char* __restrict__ wo, char* __restrict__ out, char* ws) {
  __shared__ __attribute__((aligned(16))) char lds_raw[LDS_BYTES];
  const LAS char* lds = (const LAS char*)lds_raw;
  const unsigned lds0 = (unsigned)(size_t)lds;
  const int lane = threadIdx.x & 63;
  const int w8 = __builtin_amdgcn_readfirstlane(threadIdx.x >> 6);
  const int j = blockIdx.x & 7, bp = blockIdx.x >> 5, hh = (blockIdx.x >> 3) & 3, h = 4 * j + hh;
  const bool early = bp & 1;

  if (w8 >= 4) {
    const int lw = w8 - 4;
    const int rot = (bp + 2 * hh + (j & 1)) & 7;
    const unsigned l4 = lane >> 4, l3 = lane >> 3;
    const unsigned vx0 = l3 * 1024 + (((lane & 7) ^ l4) << 4), vx1 = vx0 ^ 64;
    const unsigned pc = (lane & 15) ^ (l4 << 2);
    const unsigned vwq = l4 * 8192 + (pc << 4);
    const unsigned vv = l4 * 256 + (pc << 4);
    const unsigned vwo = l4 * 1024 + ((pc ^ (unsigned)(j << 1)) << 4);
    const unsigned vk = l4 * 256 + (((lane & 15) ^ l4) << 4);
    const char* xb = x + (size_t)(bp * 128 + lw * 32) * 1024;
    const char* wqb = wq + (size_t)(lw * 16) * 8192 + h * 256;
    const size_t kvoff = (size_t)((bp * 2 + (lw >> 1)) * 32 + h) * 16384 + (lw & 1) * 8192;
    const char* kb = kc + kvoff;
    const char* vb = vc + kvoff;
    const char* wob = wo + (size_t)(j * 512 + lw * 16) * 1024 + hh * 256;

    auto chunk = [&](int c, int sl) AI {
      const int cc = (c + rot) & 7;
      const unsigned slot = lds0 + sl * 32768;
#pragma unroll
      for (int q = 0; q < 4; ++q) {
        dma16(wqb + (size_t)(cc * 64 + q * 4) * 8192, vwq, slot + 16384 + (lw * 16 + q * 4) * 256);
        dma16(xb + q * 8192 + cc * 128, (q & 1) ? vx1 : vx0, slot + (lw * 32 + q * 8) * 128);
      }
    };
    unsigned sink;
    auto loadK = [&]() AI {
      touch(sink, wob + (size_t)(bp * 64) * 1024, ((lane & 31) >> 1) * 1024 + (lane & 1) * 128);
#pragma unroll
      for (int i = 0; i < 8; ++i) dma16nt(kb + i * 1024, vk ^ ((i & 3) << 6), lds0 + L_K + lw * 8192 + i * 1024);
    };
    auto loadV = [&]() AI {
#pragma unroll
      for (int i = 0; i < 8; ++i) dma16nt(vb + i * 1024, vv, lds0 + L_V + lw * 8192 + i * 1024);
    };
    auto loadWo = [&](int g) AI {
      const char* b = wob + (size_t)((hh ^ g) * 128) * 1024;
#pragma unroll
      for (int q = 0; q < 8; ++q)
        dma16(b + (size_t)((q >> 2) * 64 + (q & 3) * 4) * 1024, vwo, lds0 + g * 32768 + ((q >> 2) * 64 + lw * 16 + (q & 3) * 4) * 256);
    };
    chunk(0, 0); chunk(1, 1);
    wait_vm_bar<8>(); chunk(2, 2);
    if (early) {
      wait_vm_bar<0>(); loadK(); loadV(); chunk(3, 0);
      wait_vm_bar<0>(); chunk(4, 1); chunk(5, 2);
      wait_vm_bar<8>(); chunk(6, 0);
      wait_vm_bar<8>(); chunk(7, 1);
      wait_vm_bar<8>(); loadWo(2);
      wait_vm_bar<8>(); loadWo(0);
      wait_vm_bar<63>(); loadWo(1);
      wait_vm_bar<63>();
      wait_vm_bar<0>(); loadWo(3);
    } else {
      wait_vm_bar<8>(); chunk(3, 3); chunk(4, 4);
      wait_vm_bar<0>(); chunk(5, 0); chunk(6, 1);
      wait_vm_bar<63>(); chunk(7, 2);
      wait_vm_bar<63>(); loadK();
      wait_vm_bar<9>(); loadV();
      wait_vm_bar<8>(); loadWo(0);
      wait_vm_bar<8>();
      wait_vm_bar<0>(); loadWo(1); loadWo(2); loadWo(3);
    }
    wait_vm_bar<0>();
    asm volatile("" ::"v"(sink));
    asm volatile(".Lgqa_loader_end:");
    return;
  }

  unsigned codew;
  {
    u64 ip;
    int len;
    asm volatile("s_getpc_b64 %0\n1:\n\ts_mov_b32 %1, .Lgqa_loader_end-1b" : "=s"(ip), "=s"(len));
    const __amdgpu_buffer_rsrc_t cr = __builtin_amdgcn_make_buffer_rsrc((char*)ip, 0, len > 0 ? len : 0, 0x00020000);
    codew = __builtin_amdgcn_raw_buffer_load_b32(cr, lane < 2 ? (((blockIdx.x >> 3) * 4 + w8) * 2 + lane) * 128 : 0x7ffffff0, 0, 0);
  }
  const int w = w8;
  const int r = lane & 31, hf = lane >> 5;
  const int grp = bp * 4 + w;
  const int me = (grp * 8 + j) * 4 + hh;
  const u64 tag = ld_flag((const GAS u64*)(ws + WS_EPOCH + grp * 128)) + 1;
  const __amdgpu_buffer_rsrc_t wsr = __builtin_amdgcn_make_buffer_rsrc(ws, 0, 16u << 20, 0x00020000);
  const GAS u64* myflag = (const GAS u64*)(ws + WS_FLAG + (size_t)me * 512);
  { u64 warm = ld_flag(myflag + (lane & 3) * 16); asm volatile("" ::"v"(warm)); }
  __builtin_amdgcn_raw_buffer_store_b128(u32x4{~0u, ~0u, ~0u, ~0u}, wsr, lane * 16 + 1024, WS_P + (me ^ 4) * 8192, SC1);

  const unsigned q4 = (lane & 15) >> 2, p4 = lane & 3, blk = (lane >> 4) & 1;
  const unsigned trc = (8 * hf + q4) * 256 + 32 * blk + 8 * (((p4 & 1) << 1) | (p4 >> 1));
  unsigned trb[4];
#pragma unroll
  for (int c = 0; c < 4; ++c) trb[c] = trc + ((c ^ q4) << 6);
  unsigned xfb[4];
#pragma unroll
  for (int s = 0; s < 4; ++s) xfb[s] = (w * 32 + r) * 128 + (((2 * s + hf) ^ ((r >> 1) & 7)) << 4);
  const int kr = (r & 19) | ((r & 4) << 1) | ((r & 8) >> 1);
  const LAS char* kp = lds + L_K + (w >> 1) * 16384 + kr * 256;
  const LAS char* vp = lds + L_V + (w >> 1) * 16384;

  f32x16 acc[4], sc[2];
#pragma unroll
  for (int c = 0; c < 4; ++c)
#pragma unroll
    for (int i = 0; i < 16; ++i) acc[c][i] = 0.f;
#pragma unroll
  for (int t = 0; t < 2; ++t)
#pragma unroll
    for (int i = 0; i < 16; ++i) sc[t][i] = 0.f;

  bf16x8 A[8];
  u32x4 f[8];

  {
    bf16x8 B[8];
    auto stp = [](int m) AI { return m < 112 ? (m >> 2) & 3 : m & 3; };
    auto til = [](int m) AI { return m < 112 ? m & 3 : (m >> 2) & 3; };
    auto rd = [&](int m) AI {
      const int c = m >> 4, s = stp(m), t = til(m);
      const unsigned slot = c < 3 ? c * 32768u : early ? (c % 3) * 32768u : (c % 5) * 32768u;
      if (t == 0) B[(c & 1) * 4 + s] = *(const LAS bf16x8*)(lds + slot + xfb[s]);
      A[m & 7] = tr_frag(lds + slot + 16384 + s * 4096 + trb[t]);
    };
    bar_only();
#pragma unroll
    for (int m = 0; m < 6; ++m) rd(m);
    SB();
    sfor<0, 128>([&](auto ic) AI {
      constexpr int m = decltype(ic)::v, k = m & 15, c = m >> 4;
      acc[til(m)] = MFMA(A[m & 7], B[(c & 1) * 4 + stp(m)], acc[til(m)]);
      SB();
      if constexpr (c < 7) {
        if (k < 10) rd(m + 6);
        else if (k == 12) {
          if (c == 1 ? !early : c >= 5 ? early : true) lgkm_bar();
          SB();
          rd(m + 4); rd(m + 5);
        }
        else if (k == 13) { rd(m + 5); rd(m + 6); }
        else if (k > 13) rd(m + 6);
      } else {
        if (m + 6 < 128) rd(m + 6);
        if constexpr (k >= 5 && (k & 1)) f[(k - 5) >> 1] = pack8(acc[(k - 5) >> 2], ((k - 5) >> 1) & 1);
      }
      SB();
    });
  }
  f[6] = pack8(acc[3], 0);
  f[7] = pack8(acc[3], 1);
  lgkm_bar();
  constexpr float C = 0.08838834764831845f * 1.4426950408889634f;
  float m0, sum0 = 0.f;
  {
    bf16x8 kf[16];
    auto rdk = [&](int i) AI { kf[i] = *(const LAS bf16x8*)(kp + (i >> 3) * 8192 + (((2 * (i & 7) + hf) ^ (kr & 15)) << 4)); };
#pragma unroll
    for (int i = 0; i < 12; ++i) { rdk(i); SB(); }
    sfor<0, 16>([&](auto ic) AI {
      constexpr int m = decltype(ic)::v;
      sc[m >> 3] = MFMA(kf[m], BF(f[m & 7]), sc[m >> 3]);
      SB();
      if constexpr (m < 4) rdk(12 + m);
      if constexpr (m == 9) {
        m0 = sc[0][0];
#pragma unroll
        for (int i = 1; i < 16; ++i) m0 = fmaxf(m0, sc[0][i]);
        m0 *= C;
      }
      if constexpr (m >= 10) {
#pragma unroll
        for (int i = (m - 10) * 8 / 3; i < (m - 9) * 8 / 3; ++i) sc[0][i] = __builtin_amdgcn_exp2f(fmaf(sc[0][i], C, -m0));
      }
      SB();
    });
  }
#pragma unroll
  for (int i = 0; i < 16; ++i) sum0 += sc[0][i];
  pin(sum0);
  float inv;
  {
    float mx = sc[1][0];
#pragma unroll
    for (int i = 1; i < 16; ++i) mx = fmaxf(mx, sc[1][i]);
    mx = fmaxf(mx * C, m0);
    auto rr = __builtin_amdgcn_permlane32_swap(__float_as_uint(mx), __float_as_uint(mx), false, false);
    mx = fmaxf(__uint_as_float(rr[0]), __uint_as_float(rr[1]));
    const float a0 = __builtin_amdgcn_exp2f(m0 - mx);
    float sum = sum0 * a0;
#pragma unroll
    for (int i = 0; i < 16; ++i) sc[1][i] = __builtin_amdgcn_exp2f(fmaf(sc[1][i], C, -mx));
    f[0] = pack8s(sc[0], 0, a0);
    f[1] = pack8s(sc[0], 1, a0);
#pragma unroll
    for (int i = 0; i < 16; ++i) sum += sc[1][i];
    f[2] = pack8(sc[1], 0);
    f[3] = pack8(sc[1], 1);
    auto r2 = __builtin_amdgcn_permlane32_swap(__float_as_uint(sum), __float_as_uint(sum), false, false);
    inv = __builtin_amdgcn_rcpf(__uint_as_float(r2[0]) + __uint_as_float(r2[1]));
  }
  asm volatile("" : "+v"(f[0]), "+v"(f[1]), "+v"(f[2]), "+v"(f[3]), "+v"(inv));
  bar_only();
#pragma unroll
  for (int c = 0; c < 4; ++c)
#pragma unroll
    for (int i = 0; i < 16; ++i) acc[c][i] = 0.f;
  u32x4 of[4][8];
  {
    auto rd = [&](int m) AI { A[m & 7] = tr_frag(vp + (m & 3) * 4096 + trb[m >> 2]); };
    auto put = [&](int s) AI {
      of[0][s] = pack8s(acc[s >> 1], s & 1, inv);
      __builtin_amdgcn_raw_buffer_store_b128(of[0][s], wsr, lane * 16 + s * 1024, me * 8192, SC1);
    };
#pragma unroll
    for (int m = 0; m < 7; ++m) { rd(m); SB(); }
    sfor<0, 16>([&](auto ic) AI {
      constexpr int m = decltype(ic)::v;
      acc[m >> 2] = MFMA(A[m & 7], BF(f[m & 3]), acc[m >> 2]);
      SB();
      if constexpr (m + 7 < 16) rd(m + 7);
      if constexpr (m >= 5 && (m & 1)) put((m - 5) >> 1);
      SB();
    });
    put(6); SB(); put(7); SB();
  }
#pragma unroll
  for (int c = 0; c < 4; ++c)
#pragma unroll
    for (int i = 0; i < 16; ++i) acc[c][i] = 0.f;
  u64 poll;
  asm volatile("" ::"v"(codew));
  bar_only();
  const GAS u64* fq1 = myflag + (lane < 3 ? lane : 0) * 16;
  {
    auto rd = [&](int m) AI { A[m & 7] = tr_frag(lds + (m >> 2) * 4096 + trb[m & 3]); };
#pragma unroll
    for (int m = 0; m < 7; ++m) { rd(m); SB(); }
    sfor<0, 32>([&](auto ic) AI {
      constexpr int m = decltype(ic)::v;
      acc[m & 3] = MFMA(A[m & 7], BF(of[0][m >> 2]), acc[m & 3]);
      SB();
      if constexpr (m + 7 < 32) rd(m + 7);
      if constexpr (m == 16) {
        drain_vm();
        if (lane >= 1 && lane < 5) __hip_atomic_store((GAS u64*)(ws + WS_FLAG + (size_t)(me ^ lane) * 512) + (lane - 1) * 16, tag, RLX_AGENT);
        poll = ld_flag(fq1);
      }
      SB();
    });
  }
  pin(poll);
  if (!__all(poll == tag)) wait_flags(fq1, tag);
  asm volatile("" ::: "memory");
  auto fetch = [&](int i) AI {
    of[1 + (i >> 3)][i & 7] = __builtin_amdgcn_raw_buffer_load_b128(wsr, lane * 16 + (i & 7) * 1024, (me ^ (1 + (i >> 3))) * 8192, SC1);
  };
#pragma unroll
  for (int i = 0; i < 10; ++i) { fetch(i); SB(); }
  bar_only();
  {
    auto frg = [](int m) AI { return m < 48 ? m / 3 : m < 72 ? 16 + (m & 7) : m - 72; };
    auto til = [](int m) AI { return m < 48 ? 3 - m % 3 : m < 72 ? 3 - ((m - 48) >> 3) : 0; };
    auto rd = [&](int m) AI { A[m & 7] = tr_frag(lds + 32768 + frg(m) * 4096 + trb[til(m)]); };
#pragma unroll
    for (int m = 0; m < 7; ++m) { rd(m); SB(); }
    sfor<0, 96>([&](auto ic) AI {
      constexpr int m = decltype(ic)::v;
      acc[til(m)] = MFMA(A[m & 7], BF(of[1 + (frg(m) >> 3)][frg(m) & 7]), acc[til(m)]);
      SB();
      if constexpr (m + 7 < 96) rd(m + 7);
      if constexpr (m % 2 == 0 && 10 + m / 2 < 24) fetch(10 + m / 2);
      if constexpr (m == 40) poll = ld_flag(myflag + 48);
      if constexpr (m >= 56 && m < 80 && ((m & 7) == 1 || (m & 7) == 3)) {
        constexpr int d = 2 * (3 - ((m - 56) >> 3)) + ((m & 7) == 1 ? 1 : 0);
        __builtin_amdgcn_raw_buffer_store_b128(pack8(acc[d >> 1], d & 1), wsr, lane * 16 + d * 1024, WS_P + (me ^ (d << 2)) * 8192, SC1);
      }
      if constexpr (m == 92) {
        drain_vm();
        if (lane >= 2 && lane < 8) __hip_atomic_store((GAS u64*)(ws + WS_FLAG + (size_t)(me ^ (lane << 2)) * 512) + 48 + (lane - 1), tag, RLX_AGENT);
      }
      SB();
    });
    __builtin_amdgcn_raw_buffer_store_b128(not_ones(pack8(acc[0], 1)), wsr, lane * 16 + 1024, WS_P + (me ^ 4) * 8192, SC1);
  }
  pin(poll);
  if (!__all(poll == tag)) wait_flags(myflag + 48, tag);
  asm volatile("" ::: "memory");
  u32x4 pv[7];
  {
    const GAS u64* q = myflag + 48 + (lane < 7 ? lane : 0);
#pragma clang loop unroll(disable)
    for (unsigned spins = 0; spins < SPIN_CAP; ++spins) {
      const u64 v = ld_flag(q);
      pv[0] = __builtin_amdgcn_raw_buffer_load_b128(wsr, lane * 16 + 1024, WS_P + me * 8192, SC1);
      SB();
      if (__all(v == tag)) break;
      __builtin_amdgcn_s_sleep(1);
    }
    asm volatile("" ::: "memory");
#pragma unroll
    for (int d = 2; d < 8; ++d) pv[d - 1] = __builtin_amdgcn_raw_buffer_load_b128(wsr, lane * 16 + d * 1024, WS_P + me * 8192, SC1);
    SB();
    if (!no_ones(pv[0])) {
#pragma clang loop unroll(disable)
      for (unsigned spins = 0; spins < SPIN_CAP; ++spins) {
        asm volatile("" ::: "memory");
        pv[0] = __builtin_amdgcn_raw_buffer_load_b128(wsr, lane * 16 + 1024, WS_P + me * 8192, SC1);
        if (no_ones(pv[0])) break;
      }
    }
  }
  float o[8];
#pragma unroll
  for (int i = 0; i < 8; ++i) o[i] = acc[0][i];
#pragma unroll
  for (int d = 0; d < 7; ++d)
#pragma unroll
    for (int i = 0; i < 4; ++i) {
      o[2 * i] += __uint_as_float(pv[d][i] << 16);
      o[2 * i + 1] += __uint_as_float(pv[d][i] & 0xffff0000u);
    }
  __builtin_amdgcn_raw_buffer_store_b128(u32x4{pk2(o[0], o[1]), pk2(o[2], o[3]), pk2(o[4], o[5]), pk2(o[6], o[7])},
                                         __builtin_amdgcn_make_buffer_rsrc(out, 0, 1u << 20, 0x00020000),
                                         (bp * 128 + w * 32 + r) * 1024 + hh * 256 + j * 32 + hf * 16, 0, SC1);
  if (j == 0 && hh == 0 && lane == 0) __hip_atomic_store((GAS u64*)(ws + WS_EPOCH + grp * 128), tag, RLX_AGENT);
}

extern "C" void kernel_launch(void* const* d_in, const int* in_sizes, int n_in, void* d_out, int out_size, void* d_ws, size_t ws_size,
                              hipStream_t stream) {
  gqa_fused<<<256, 512, 0, stream>>>((const char*)d_in[0], (const char*)d_in[1], (const char*)d_in[2], (const char*)d_in[3],
                                     (const char*)d_in[4], (char*)d_out, (char*)d_ws);
}
```

```cpp
#include <hip/hip_runtime.h>
#include <hip/hip_bf16.h>

typedef unsigned long long u64;
typedef short s16x4 __attribute__((ext_vector_type(4)));
typedef __bf16 bf16x8 __attribute__((ext_vector_type(8)));
typedef __bf16 bf16x2 __attribute__((ext_vector_type(2)));
typedef float f32x2 __attribute__((ext_vector_type(2)));
typedef float f32x16 __attribute__((ext_vector_type(16)));
typedef unsigned u32x4 __attribute__((ext_vector_type(4)));
#define LAS __attribute__((address_space(3)))
#define GAS __attribute__((address_space(1)))
#define DI __device__ __forceinline__
#define AI __attribute__((always_inline))
#define RLX_AGENT __ATOMIC_RELAXED, __HIP_MEMORY_SCOPE_AGENT
#define SB() __builtin_amdgcn_sched_barrier(0)
#define MFMA(a, b, c) __builtin_amdgcn_mfma_f32_32x32x16_bf16((a), (b), (c), 0, 0, 0)
#define BF(x) __builtin_bit_cast(bf16x8, x)
constexpr int SC1 = 16;

constexpr unsigned WS_P     = 8u << 20;
constexpr unsigned WS_FLAG  = 16u << 20;
constexpr unsigned WS_EPOCH = WS_FLAG + (512u << 10);
constexpr unsigned L_K = 3u * 32768, L_V = 4u * 32768, LDS_BYTES = 160u << 10;
constexpr unsigned SPIN_CAP = 1u << 20;

DI void dma16(const char* sbase, unsigned voff, unsigned lds) {
  asm volatile("s_mov_b32 m0, %2\n\ts_nop 0\n\tglobal_load_lds_dwordx4 %0, %1" ::"v"(voff), "s"(sbase), "s"(lds) : "memory");
}
DI void dma16nt(const char* sbase, unsigned voff, unsigned lds) {
  asm volatile("s_mov_b32 m0, %2\n\ts_nop 0\n\tglobal_load_lds_dwordx4 %0, %1 nt" ::"v"(voff), "s"(sbase), "s"(lds) : "memory");
}
DI void touch(unsigned& sink, const char* sbase, unsigned voff) {
  asm volatile("global_load_dword %0, %1, %2" : "=v"(sink) : "v"(voff), "s"(sbase) : "memory");
}
DI void pin(float& v) { asm volatile("" : "+v"(v)); }
DI void pin(u64& v) { asm volatile("" : "+v"(v)); }
DI void drain_vm() { asm volatile("s_waitcnt vmcnt(0)" ::: "memory"); }
template <int N> DI void wait_vm_bar() { asm volatile("s_waitcnt vmcnt(%0)\n\ts_barrier" ::"i"(N) : "memory"); }
DI void bar_only() { SB(); asm volatile("s_barrier" ::: "memory"); SB(); }
DI void lgkm_bar() { SB(); asm volatile("s_waitcnt lgkmcnt(0)\n\ts_barrier" ::: "memory"); SB(); }
DI u64 ld_flag(const GAS u64* q) { return __hip_atomic_load(q, RLX_AGENT); }

DI u64 rtime() { u64 t; asm volatile("s_memrealtime %0\n\ts_waitcnt lgkmcnt(0)" : "=s"(t)::"memory"); return t; }
DI bf16x8 tr_frag(const LAS char* p) {
  s16x4 lo = __builtin_amdgcn_ds_read_tr16_b64_v4i16((LAS s16x4*)p);
  s16x4 hi = __builtin_amdgcn_ds_read_tr16_b64_v4i16((LAS s16x4*)(p + 1024));
  return __builtin_bit_cast(bf16x8, __builtin_shufflevector(lo, hi, 0, 1, 2, 3, 4, 5, 6, 7));
}
DI unsigned pk2(float a, float b) { f32x2 v = {a, b}; return __builtin_bit_cast(unsigned, __builtin_convertvector(v, bf16x2)); }
DI u32x4 pack8(const f32x16& a, int s) {
  return u32x4{pk2(a[8 * s], a[8 * s + 1]), pk2(a[8 * s + 2], a[8 * s + 3]), pk2(a[8 * s + 4], a[8 * s + 5]), pk2(a[8 * s + 6], a[8 * s + 7])};
}
DI u32x4 pack8s(const f32x16& a, int s, float k) {
  float t[8];
#pragma unroll
  for (int i = 0; i < 8; ++i) t[i] = a[8 * s + i] * k;
  SB();
  return u32x4{pk2(t[0], t[1]), pk2(t[2], t[3]), pk2(t[4], t[5]), pk2(t[6], t[7])};
}
DI u32x4 not_ones(u32x4 v) {
  return u32x4{min(v[0], 0xfffffffeu), min(v[1], 0xfffffffeu), min(v[2], 0xfffffffeu), min(v[3], 0xfffffffeu)};
}
DI bool no_ones(u32x4 v) { return __all(max(max(v[0], v[1]), max(v[2], v[3])) != ~0u); }
template <int I> struct IC { static constexpr int v = I; };
template <int B, int E, class F> DI void sfor(F&& f) { if constexpr (B < E) { f(IC<B>{}); sfor<B + 1, E>(f); } }

DI void wait_flags(const GAS u64* q, u64 tag) {
#pragma clang loop unroll(disable)
  for (unsigned spins = 0; spins < SPIN_CAP; ++spins) {
    if (__all(ld_flag(q) == tag)) break;
    __builtin_amdgcn_s_sleep(1);
  }
  asm volatile("" ::: "memory");
}

__global__ __launch_bounds__(512, 2) void gqa_fused(const char* __restrict__ x, const char* __restrict__ kc, const char* __restrict__ vc,
                                                   const char* __restrict__ wq, const char* __restrict__ wo, char* __restrict__ out, char* ws) {
  __shared__ __attribute__((aligned(16))) char lds_raw[LDS_BYTES];
  const LAS char* lds = (const LAS char*)lds_raw;
  const unsigned lds0 = (unsigned)(size_t)lds;
  const int lane = threadIdx.x & 63;
  const int w8 = __builtin_amdgcn_readfirstlane(threadIdx.x >> 6);
  const int j = blockIdx.x & 7, bp = blockIdx.x >> 5, hh = (blockIdx.x >> 3) & 3, h = 4 * j + hh;
  const u64 t00 = rtime();
  u64 tp = t00;
  const bool early = bp & 1;

  if (w8 >= 4) {
    const int lw = w8 - 4;
    const int rot = (bp + 2 * hh + (j & 1)) & 7;
    const unsigned l4 = lane >> 4, l3 = lane >> 3;
    const unsigned vx0 = l3 * 1024 + (((lane & 7) ^ l4) << 4), vx1 = vx0 ^ 64;
    const unsigned pc = (lane & 15) ^ (l4 << 2);
    const unsigned vwq = l4 * 8192 + (pc << 4);
    const unsigned vv = l4 * 256 + (pc << 4);
    const unsigned vwo = l4 * 1024 + ((pc ^ (unsigned)(j << 1)) << 4);
    const unsigned vk = l4 * 256 + (((lane & 15) ^ l4) << 4);
    const char* xb = x + (size_t)(bp * 128 + lw * 32) * 1024;
    const char* wqb = wq + (size_t)(lw * 16) * 8192 + h * 256;
    const size_t kvoff = (size_t)((bp * 2 + (lw >> 1)) * 32 + h) * 16384 + (lw & 1) * 8192;
    const char* kb = kc + kvoff;
    const char* vb = vc + kvoff;
    const char* wob = wo + (size_t)(j * 512 + lw * 16) * 1024 + hh * 256;

    auto chunk = [&](int c, int sl) AI {
      const int cc = (c + rot) & 7;
      const unsigned slot = lds0 + sl * 32768;
#pragma unroll
      for (int q = 0; q < 4; ++q) {
        dma16(wqb + (size_t)(cc * 64 + q * 4) * 8192, vwq, slot + 16384 + (lw * 16 + q * 4) * 256);
        dma16(xb + q * 8192 + cc * 128, (q & 1) ? vx1 : vx0, slot + (lw * 32 + q * 8) * 128);
      }
    };
    unsigned sink;
    auto loadK = [&]() AI {
      touch(sink, wob + (size_t)(bp * 64) * 1024, ((lane & 31) >> 1) * 1024 + (lane & 1) * 128);
#pragma unroll
      for (int i = 0; i < 8; ++i) dma16nt(kb + i * 1024, vk ^ ((i & 3) << 6), lds0 + L_K + lw * 8192 + i * 1024);
    };
    auto loadV = [&]() AI {
#pragma unroll
      for (int i = 0; i < 8; ++i) dma16nt(vb + i * 1024, vv, lds0 + L_V + lw * 8192 + i * 1024);
    };
    auto loadWo = [&](int g) AI {
      const char* b = wob + (size_t)((hh ^ g) * 128) * 1024;
#pragma unroll
      for (int q = 0; q < 8; ++q)
        dma16(b + (size_t)((q >> 2) * 64 + (q & 3) * 4) * 1024, vwo, lds0 + g * 32768 + ((q >> 2) * 64 + lw * 16 + (q & 3) * 4) * 256);
    };
    asm volatile("" ::"s"(xb), "s"(wqb), "v"(vwq), "v"(vx0));
    tp = rtime();
    chunk(0, 0); chunk(1, 1);
    wait_vm_bar<8>(); chunk(2, 2);
    if (early) {
      wait_vm_bar<0>(); loadK(); loadV(); chunk(3, 0);
      wait_vm_bar<0>(); chunk(4, 1); chunk(5, 2);
      wait_vm_bar<8>(); chunk(6, 0);
      wait_vm_bar<8>(); chunk(7, 1);
      wait_vm_bar<8>(); loadWo(2);
      wait_vm_bar<8>(); loadWo(0);
      wait_vm_bar<63>(); loadWo(1);
      wait_vm_bar<63>();
      wait_vm_bar<0>(); loadWo(3);
    } else {
      wait_vm_bar<8>(); chunk(3, 3); chunk(4, 4);
      wait_vm_bar<0>(); chunk(5, 0); chunk(6, 1);
      wait_vm_bar<63>(); chunk(7, 2);
      wait_vm_bar<63>(); loadK();
      wait_vm_bar<9>(); loadV();
      wait_vm_bar<8>(); loadWo(0);
      wait_vm_bar<8>();
      wait_vm_bar<0>(); loadWo(1); loadWo(2); loadWo(3);
    }
    wait_vm_bar<0>();
    asm volatile("" ::"v"(sink));
    asm volatile(".Lgqa_loader_end:");
    { u64 until = t00 + 100 * (tp - t00); for (int i = 0; i < (1 << 20) && rtime() < until; ++i) __builtin_amdgcn_s_sleep(4); }
    return;
  }

  unsigned codew;
  {
    u64 ip;
    int len;
    asm volatile("s_getpc_b64 %0\n1:\n\ts_mov_b32 %1, .Lgqa_loader_end-1b" : "=s"(ip), "=s"(len));
    const __amdgpu_buffer_rsrc_t cr = __builtin_amdgcn_make_buffer_rsrc((char*)ip, 0, len > 0 ? len : 0, 0x00020000);
    codew = __builtin_amdgcn_raw_buffer_load_b32(cr, lane < 2 ? (((blockIdx.x >> 3) * 4 + w8) * 2 + lane) * 128 : 0x7ffffff0, 0, 0);
  }
  const int w = w8;
  const int r = lane & 31, hf = lane >> 5;
  const int grp = bp * 4 + w;
  const int me = (grp * 8 + j) * 4 + hh;
  const u64 tag = ld_flag((const GAS u64*)(ws + WS_EPOCH + grp * 128)) + 1;
  const __amdgpu_buffer_rsrc_t wsr = __builtin_amdgcn_make_buffer_rsrc(ws, 0, 16u << 20, 0x00020000);
  const GAS u64* myflag = (const GAS u64*)(ws + WS_FLAG + (size_t)me * 512);
  { u64 warm = ld_flag(myflag + (lane & 3) * 16); asm volatile("" ::"v"(warm)); }
  __builtin_amdgcn_raw_buffer_store_b128(u32x4{~0u, ~0u, ~0u, ~0u}, wsr, lane * 16 + 1024, WS_P + (me ^ 4) * 8192, SC1);

  const unsigned q4 = (lane & 15) >> 2, p4 = lane & 3, blk = (lane >> 4) & 1;
  const unsigned trc = (8 * hf + q4) * 256 + 32 * blk + 8 * (((p4 & 1) << 1) | (p4 >> 1));
  unsigned trb[4];
#pragma unroll
  for (int c = 0; c < 4; ++c) trb[c] = trc + ((c ^ q4) << 6);
  unsigned xfb[4];
#pragma unroll
  for (int s = 0; s < 4; ++s) xfb[s] = (w * 32 + r) * 128 + (((2 * s + hf) ^ ((r >> 1) & 7)) << 4);
  const int kr = (r & 19) | ((r & 4) << 1) | ((r & 8) >> 1);
  const LAS char* kp = lds + L_K + (w >> 1) * 16384 + kr * 256;
  const LAS char* vp = lds + L_V + (w >> 1) * 16384;

  f32x16 acc[4], sc[2];
#pragma unroll
  for (int c = 0; c < 4; ++c)
#pragma unroll
    for (int i = 0; i < 16; ++i) acc[c][i] = 0.f;
#pragma unroll
  for (int t = 0; t < 2; ++t)
#pragma unroll
    for (int i = 0; i < 16; ++i) sc[t][i] = 0.f;

  bf16x8 A[8];
  u32x4 f[8];

  {
    bf16x8 B[8];
    auto stp = [](int m) AI { return m < 112 ? (m >> 2) & 3 : m & 3; };
    auto til = [](int m) AI { return m < 112 ? m & 3 : (m >> 2) & 3; };
    auto rd = [&](int m) AI {
      const int c = m >> 4, s = stp(m), t = til(m);
      const unsigned slot = c < 3 ? c * 32768u : early ? (c % 3) * 32768u : (c % 5) * 32768u;
      if (t == 0) B[(c & 1) * 4 + s] = *(const LAS bf16x8*)(lds + slot + xfb[s]);
      A[m & 7] = tr_frag(lds + slot + 16384 + s * 4096 + trb[t]);
    };
    bar_only();
#pragma unroll
    for (int m = 0; m < 6; ++m) rd(m);
    SB();
    sfor<0, 128>([&](auto ic) AI {
      constexpr int m = decltype(ic)::v, k = m & 15, c = m >> 4;
      acc[til(m)] = MFMA(A[m & 7], B[(c & 1) * 4 + stp(m)], acc[til(m)]);
      SB();
      if constexpr (c < 7) {
        if (k < 10) rd(m + 6);
        else if (k == 12) {
          if (c == 1 ? !early : c >= 5 ? early : true) lgkm_bar();
          SB();
          rd(m + 4); rd(m + 5);
        }
        else if (k == 13) { rd(m + 5); rd(m + 6); }
        else if (k > 13) rd(m + 6);
      } else {
        if (m + 6 < 128) rd(m + 6);
        if constexpr (k >= 5 && (k & 1)) f[(k - 5) >> 1] = pack8(acc[(k - 5) >> 2], ((k - 5) >> 1) & 1);
      }
      SB();
    });
  }
  f[6] = pack8(acc[3], 0);
  f[7] = pack8(acc[3], 1);
  lgkm_bar();
  constexpr float C = 0.08838834764831845f * 1.4426950408889634f;
  float m0, sum0 = 0.f;
  {
    bf16x8 kf[16];
    auto rdk = [&](int i) AI { kf[i] = *(const LAS bf16x8*)(kp + (i >> 3) * 8192 + (((2 * (i & 7) + hf) ^ (kr & 15)) << 4)); };
#pragma unroll
    for (int i = 0; i < 12; ++i) { rdk(i); SB(); }
    sfor<0, 16>([&](auto ic) AI {
      constexpr int m = decltype(ic)::v;
      sc[m >> 3] = MFMA(kf[m], BF(f[m & 7]), sc[m >> 3]);
      SB();
      if constexpr (m < 4) rdk(12 + m);
      if constexpr (m == 9) {
        m0 = sc[0][0];
#pragma unroll
        for (int i = 1; i < 16; ++i) m0 = fmaxf(m0, sc[0][i]);
        m0 *= C;
      }
      if constexpr (m >= 10) {
#pragma unroll
        for (int i = (m - 10) * 8 / 3; i < (m - 9) * 8 / 3; ++i) sc[0][i] = __builtin_amdgcn_exp2f(fmaf(sc[0][i], C, -m0));
      }
      SB();
    });
  }
#pragma unroll
  for (int i = 0; i < 16; ++i) sum0 += sc[0][i];
  pin(sum0);
  float inv;
  {
    float mx = sc[1][0];
#pragma unroll
    for (int i = 1; i < 16; ++i) mx = fmaxf(mx, sc[1][i]);
    mx = fmaxf(mx * C, m0);
    auto rr = __builtin_amdgcn_permlane32_swap(__float_as_uint(mx), __float_as_uint(mx), false, false);
    mx = fmaxf(__uint_as_float(rr[0]), __uint_as_float(rr[1]));
    const float a0 = __builtin_amdgcn_exp2f(m0 - mx);
    float sum = sum0 * a0;
#pragma unroll
    for (int i = 0; i < 16; ++i) sc[1][i] = __builtin_amdgcn_exp2f(fmaf(sc[1][i], C, -mx));
    f[0] = pack8s(sc[0], 0, a0);
    f[1] = pack8s(sc[0], 1, a0);
#pragma unroll
    for (int i = 0; i < 16; ++i) sum += sc[1][i];
    f[2] = pack8(sc[1], 0);
    f[3] = pack8(sc[1], 1);
    auto r2 = __builtin_amdgcn_permlane32_swap(__float_as_uint(sum), __float_as_uint(sum), false, false);
    inv = __builtin_amdgcn_rcpf(__uint_as_float(r2[0]) + __uint_as_float(r2[1]));
  }
  asm volatile("" : "+v"(f[0]), "+v"(f[1]), "+v"(f[2]), "+v"(f[3]), "+v"(inv));
  bar_only();
#pragma unroll
  for (int c = 0; c < 4; ++c)
#pragma unroll
    for (int i = 0; i < 16; ++i) acc[c][i] = 0.f;
  u32x4 of[4][8];
  {
    auto rd = [&](int m) AI { A[m & 7] = tr_frag(vp + (m & 3) * 4096 + trb[m >> 2]); };
    auto put = [&](int s) AI {
      of[0][s] = pack8s(acc[s >> 1], s & 1, inv);
      __builtin_amdgcn_raw_buffer_store_b128(of[0][s], wsr, lane * 16 + s * 1024, me * 8192, SC1);
    };
#pragma unroll
    for (int m = 0; m < 7; ++m) { rd(m); SB(); }
    sfor<0, 16>([&](auto ic) AI {
      constexpr int m = decltype(ic)::v;
      acc[m >> 2] = MFMA(A[m & 7], BF(f[m & 3]), acc[m >> 2]);
      SB();
      if constexpr (m + 7 < 16) rd(m + 7);
      if constexpr (m >= 5 && (m & 1)) put((m - 5) >> 1);
      SB();
    });
    put(6); SB(); put(7); SB();
  }
#pragma unroll
  for (int c = 0; c < 4; ++c)
#pragma unroll
    for (int i = 0; i < 16; ++i) acc[c][i] = 0.f;
  u64 poll;
  asm volatile("" ::"v"(codew));
  bar_only();
  const GAS u64* fq1 = myflag + (lane < 3 ? lane : 0) * 16;
  {
    auto rd = [&](int m) AI { A[m & 7] = tr_frag(lds + (m >> 2) * 4096 + trb[m & 3]); };
#pragma unroll
    for (int m = 0; m < 7; ++m) { rd(m); SB(); }
    sfor<0, 32>([&](auto ic) AI {
      constexpr int m = decltype(ic)::v;
      acc[m & 3] = MFMA(A[m & 7], BF(of[0][m >> 2]), acc[m & 3]);
      SB();
      if constexpr (m + 7 < 32) rd(m + 7);
      if constexpr (m == 16) {
        drain_vm();
        if (lane >= 1 && lane < 5) __hip_atomic_store((GAS u64*)(ws + WS_FLAG + (size_t)(me ^ lane) * 512) + (lane - 1) * 16, tag, RLX_AGENT);
        poll = ld_flag(fq1);
      }
      SB();
    });
  }
  pin(poll);
  if (!__all(poll == tag)) wait_flags(fq1, tag);
  asm volatile("" ::: "memory");
  auto fetch = [&](int i) AI {
    of[1 + (i >> 3)][i & 7] = __builtin_amdgcn_raw_buffer_load_b128(wsr, lane * 16 + (i & 7) * 1024, (me ^ (1 + (i >> 3))) * 8192, SC1);
  };
#pragma unroll
  for (int i = 0; i < 10; ++i) { fetch(i); SB(); }
  bar_only();
  {
    auto frg = [](int m) AI { return m < 48 ? m / 3 : m < 72 ? 16 + (m & 7) : m - 72; };
    auto til = [](int m) AI { return m < 48 ? 3 - m % 3 : m < 72 ? 3 - ((m - 48) >> 3) : 0; };
    auto rd = [&](int m) AI { A[m & 7] = tr_frag(lds + 32768 + frg(m) * 4096 + trb[til(m)]); };
#pragma unroll
    for (int m = 0; m < 7; ++m) { rd(m); SB(); }
    sfor<0, 96>([&](auto ic) AI {
      constexpr int m = decltype(ic)::v;
      acc[til(m)] = MFMA(A[m & 7], BF(of[1 + (frg(m) >> 3)][frg(m) & 7]), acc[til(m)]);
      SB();
      if constexpr (m + 7 < 96) rd(m + 7);
      if constexpr (m % 2 == 0 && 10 + m / 2 < 24) fetch(10 + m / 2);
      if constexpr (m == 40) poll = ld_flag(myflag + 48);
      if constexpr (m >= 56 && m < 80 && ((m & 7) == 1 || (m & 7) == 3)) {
        constexpr int d = 2 * (3 - ((m - 56) >> 3)) + ((m & 7) == 1 ? 1 : 0);
        __builtin_amdgcn_raw_buffer_store_b128(pack8(acc[d >> 1], d & 1), wsr, lane * 16 + d * 1024, WS_P + (me ^ (d << 2)) * 8192, SC1);
      }
      if constexpr (m == 92) {
        drain_vm();
        if (lane >= 2 && lane < 8) __hip_atomic_store((GAS u64*)(ws + WS_FLAG + (size_t)(me ^ (lane << 2)) * 512) + 48 + (lane - 1), tag, RLX_AGENT);
      }
      SB();
    });
    __builtin_amdgcn_raw_buffer_store_b128(not_ones(pack8(acc[0], 1)), wsr, lane * 16 + 1024, WS_P + (me ^ 4) * 8192, SC1);
  }
  pin(poll);
  if (!__all(poll == tag)) wait_flags(myflag + 48, tag);
  asm volatile("" ::: "memory");
  u32x4 pv[7];
  {
    const GAS u64* q = myflag + 48 + (lane < 7 ? lane : 0);
#pragma clang loop unroll(disable)
    for (unsigned spins = 0; spins < SPIN_CAP; ++spins) {
      const u64 v = ld_flag(q);
      pv[0] = __builtin_amdgcn_raw_buffer_load_b128(wsr, lane * 16 + 1024, WS_P + me * 8192, SC1);
      SB();
      if (__all(v == tag)) break;
      __builtin_amdgcn_s_sleep(1);
    }
    asm volatile("" ::: "memory");
#pragma unroll
    for (int d = 2; d < 8; ++d) pv[d - 1] = __builtin_amdgcn_raw_buffer_load_b128(wsr, lane * 16 + d * 1024, WS_P + me * 8192, SC1);
    SB();
    if (!no_ones(pv[0])) {
#pragma clang loop unroll(disable)
      for (unsigned spins = 0; spins < SPIN_CAP; ++spins) {
        asm volatile("" ::: "memory");
        pv[0] = __builtin_amdgcn_raw_buffer_load_b128(wsr, lane * 16 + 1024, WS_P + me * 8192, SC1);
        if (no_ones(pv[0])) break;
      }
    }
  }
  float o[8];
#pragma unroll
  for (int i = 0; i < 8; ++i) o[i] = acc[0][i];
#pragma unroll
  for (int d = 0; d < 7; ++d)
#pragma unroll
    for (int i = 0; i < 4; ++i) {
      o[2 * i] += __uint_as_float(pv[d][i] << 16);
      o[2 * i + 1] += __uint_as_float(pv[d][i] & 0xffff0000u);
    }
  __builtin_amdgcn_raw_buffer_store_b128(u32x4{pk2(o[0], o[1]), pk2(o[2], o[3]), pk2(o[4], o[5]), pk2(o[6], o[7])},
                                         __builtin_amdgcn_make_buffer_rsrc(out, 0, 1u << 20, 0x00020000),
                                         (bp * 128 + w * 32 + r) * 1024 + hh * 256 + j * 32 + hf * 16, 0, SC1);
  if (j == 0 && hh == 0 && lane == 0) __hip_atomic_store((GAS u64*)(ws + WS_EPOCH + grp * 128), tag, RLX_AGENT);
}

extern "C" void kernel_launch(void* const* d_in, const int* in_sizes, int n_in, void* d_out, int out_size, void* d_ws, size_t ws_size,
                              hipStream_t stream) {
  gqa_fused<<<256, 512, 0, stream>>>((const char*)d_in[0], (const char*)d_in[1], (const char*)d_in[2], (const char*)d_in[3],
                                     (const char*)d_in[4], (char*)d_out, (char*)d_ws);
}
```

```cpp
#include <hip/hip_runtime.h>
#include <hip/hip_bf16.h>

typedef unsigned long long u64;
typedef short s16x4 __attribute__((ext_vector_type(4)));
typedef __bf16 bf16x8 __attribute__((ext_vector_type(8)));
typedef __bf16 bf16x2 __attribute__((ext_vector_type(2)));
typedef float f32x2 __attribute__((ext_vector_type(2)));
typedef float f32x16 __attribute__((ext_vector_type(16)));
typedef unsigned u32x4 __attribute__((ext_vector_type(4)));
#define LAS __attribute__((address_space(3)))
#define GAS __attribute__((address_space(1)))
#define DI __device__ __forceinline__
#define AI __attribute__((always_inline))
#define RLX_AGENT __ATOMIC_RELAXED, __HIP_MEMORY_SCOPE_AGENT
#define SB() __builtin_amdgcn_sched_barrier(0)
#define MFMA(a, b, c) __builtin_amdgcn_mfma_f32_32x32x16_bf16((a), (b), (c), 0, 0, 0)
#define BF(x) __builtin_bit_cast(bf16x8, x)
constexpr int SC1 = 16;

constexpr unsigned WS_P     = 8u << 20;
constexpr unsigned WS_FLAG  = 16u << 20;
constexpr unsigned WS_EPOCH = WS_FLAG + (512u << 10);
constexpr unsigned L_K = 3u * 32768, L_V = 4u * 32768, LDS_BYTES = 160u << 10;
constexpr unsigned SPIN_CAP = 1u << 20;

DI void dma16(const char* sbase, unsigned voff, unsigned lds) {
  asm volatile("s_mov_b32 m0, %2\n\ts_nop 0\n\tglobal_load_lds_dwordx4 %0, %1" ::"v"(voff), "s"(sbase), "s"(lds) : "memory");
}
DI void dma16nt(const char* sbase, unsigned voff, unsigned lds) {
  asm volatile("s_mov_b32 m0, %2\n\ts_nop 0\n\tglobal_load_lds_dwordx4 %0, %1 nt" ::"v"(voff), "s"(sbase), "s"(lds) : "memory");
}
DI void touch(unsigned& sink, const char* sbase, unsigned voff) {
  asm volatile("global_load_dword %0, %1, %2" : "=v"(sink) : "v"(voff), "s"(sbase) : "memory");
}
DI void pin(float& v) { asm volatile("" : "+v"(v)); }
DI void pin(u64& v) { asm volatile("" : "+v"(v)); }
DI void drain_vm() { asm volatile("s_waitcnt vmcnt(0)" ::: "memory"); }
template <int N> DI void wait_vm_bar() { asm volatile("s_waitcnt vmcnt(%0)\n\ts_barrier" ::"i"(N) : "memory"); }
DI void bar_only() { SB(); asm volatile("s_barrier" ::: "memory"); SB(); }
DI void lgkm_bar() { SB(); asm volatile("s_waitcnt lgkmcnt(0)\n\ts_barrier" ::: "memory"); SB(); }
DI u64 ld_flag(const GAS u64* q) { return __hip_atomic_load(q, RLX_AGENT); }

DI u64 rtime() { u64 t; asm volatile("s_memrealtime %0\n\ts_waitcnt lgkmcnt(0)" : "=s"(t)::"memory"); return t; }
DI bf16x8 tr_frag(const LAS char* p) {
  s16x4 lo = __builtin_amdgcn_ds_read_tr16_b64_v4i16((LAS s16x4*)p);
  s16x4 hi = __builtin_amdgcn_ds_read_tr16_b64_v4i16((LAS s16x4*)(p + 1024));
  return __builtin_bit_cast(bf16x8, __builtin_shufflevector(lo, hi, 0, 1, 2, 3, 4, 5, 6, 7));
}
DI unsigned pk2(float a, float b) { f32x2 v = {a, b}; return __builtin_bit_cast(unsigned, __builtin_convertvector(v, bf16x2)); }
DI u32x4 pack8(const f32x16& a, int s) {
  return u32x4{pk2(a[8 * s], a[8 * s + 1]), pk2(a[8 * s + 2], a[8 * s + 3]), pk2(a[8 * s + 4], a[8 * s + 5]), pk2(a[8 * s + 6], a[8 * s + 7])};
}
DI u32x4 pack8s(const f32x16& a, int s, float k) {
  float t[8];
#pragma unroll
  for (int i = 0; i < 8; ++i) t[i] = a[8 * s + i] * k;
  SB();
  return u32x4{pk2(t[0], t[1]), pk2(t[2], t[3]), pk2(t[4], t[5]), pk2(t[6], t[7])};
}
DI u32x4 not_ones(u32x4 v) {
  return u32x4{min(v[0], 0xfffffffeu), min(v[1], 0xfffffffeu), min(v[2], 0xfffffffeu), min(v[3], 0xfffffffeu)};
}
DI bool no_ones(u32x4 v) { return __all(max(max(v[0], v[1]), max(v[2], v[3])) != ~0u); }
template <int I> struct IC { static constexpr int v = I; };
template <int B, int E, class F> DI void sfor(F&& f) { if constexpr (B < E) { f(IC<B>{}); sfor<B + 1, E>(f); } }

DI void wait_flags(const GAS u64* q, u64 tag) {
#pragma clang loop unroll(disable)
  for (unsigned spins = 0; spins < SPIN_CAP; ++spins) {
    if (__all(ld_flag(q) == tag)) break;
    __builtin_amdgcn_s_sleep(1);
  }
  asm volatile("" ::: "memory");
}

__global__ __launch_bounds__(512, 2) void gqa_fused(const char* __restrict__ x, const char* __restrict__ kc, const char* __restrict__ vc,
                                                   const char* __restrict__ wq, const char* __restrict__ wo, char* __restrict__ out, char* ws) {
  __shared__ __attribute__((aligned(16))) char lds_raw[LDS_BYTES];
  const LAS char* lds = (const LAS char*)lds_raw;
  const unsigned lds0 = (unsigned)(size_t)lds;
  const int lane = threadIdx.x & 63;
  const int w8 = __builtin_amdgcn_readfirstlane(threadIdx.x >> 6);
  const int j = blockIdx.x & 7, bp = blockIdx.x >> 5, hh = (blockIdx.x >> 3) & 3, h = 4 * j + hh;
  const u64 t00 = rtime();
  u64 tp = t00;
  const bool early = bp & 1;

  if (w8 >= 4) {
    const int lw = w8 - 4;
    const int rot = (bp + 2 * hh + (j & 1)) & 7;
    const unsigned l4 = lane >> 4, l3 = lane >> 3;
    const unsigned vx0 = l3 * 1024 + (((lane & 7) ^ l4) << 4), vx1 = vx0 ^ 64;
    const unsigned pc = (lane & 15) ^ (l4 << 2);
    const unsigned vwq = l4 * 8192 + (pc << 4);
    const unsigned vv = l4 * 256 + (pc << 4);
    const unsigned vwo = l4 * 1024 + ((pc ^ (unsigned)(j << 1)) << 4);
    const unsigned vk = l4 * 256 + (((lane & 15) ^ l4) << 4);
    const char* xb = x + (size_t)(bp * 128 + lw * 32) * 1024;
    const char* wqb = wq + (size_t)(lw * 16) * 8192 + h * 256;
    const size_t kvoff = (size_t)((bp * 2 + (lw >> 1)) * 32 + h) * 16384 + (lw & 1) * 8192;
    const char* kb = kc + kvoff;
    const char* vb = vc + kvoff;
    const char* wob = wo + (size_t)(j * 512 + lw * 16) * 1024 + hh * 256;

    auto chunk = [&](int c, int sl) AI {
      const int cc = (c + rot) & 7;
      const unsigned slot = lds0 + sl * 32768;
#pragma unroll
      for (int q = 0; q < 4; ++q) {
        dma16(wqb + (size_t)(cc * 64 + q * 4) * 8192, vwq, slot + 16384 + (lw * 16 + q * 4) * 256);
        dma16(xb + q * 8192 + cc * 128, (q & 1) ? vx1 : vx0, slot + (lw * 32 + q * 8) * 128);
      }
    };
    unsigned sink;
    auto loadK = [&]() AI {
      touch(sink, wob + (size_t)(bp * 64) * 1024, ((lane & 31) >> 1) * 1024 + (lane & 1) * 128);
#pragma unroll
      for (int i = 0; i < 8; ++i) dma16nt(kb + i * 1024, vk ^ ((i & 3) << 6), lds0 + L_K + lw * 8192 + i * 1024);
    };
    auto loadV = [&]() AI {
#pragma unroll
      for (int i = 0; i < 8; ++i) dma16nt(vb + i * 1024, vv, lds0 + L_V + lw * 8192 + i * 1024);
    };
    auto loadWo = [&](int g) AI {
      const char* b = wob + (size_t)((hh ^ g) * 128) * 1024;
#pragma unroll
      for (int q = 0; q < 8; ++q)
        dma16(b + (size_t)((q >> 2) * 64 + (q & 3) * 4) * 1024, vwo, lds0 + g * 32768 + ((q >> 2) * 64 + lw * 16 + (q & 3) * 4) * 256);
    };
    chunk(0, 0); chunk(1, 1);
    wait_vm_bar<8>(); tp = rtime(); chunk(2, 2);
    if (early) {
      wait_vm_bar<0>(); loadK(); loadV(); chunk(3, 0);
      wait_vm_bar<0>(); chunk(4, 1); chunk(5, 2);
      wait_vm_bar<8>(); chunk(6, 0);
      wait_vm_bar<8>(); chunk(7, 1);
      wait_vm_bar<8>(); loadWo(2);
      wait_vm_bar<8>(); loadWo(0);
      wait_vm_bar<63>(); loadWo(1);
      wait_vm_bar<63>();
      wait_vm_bar<0>(); loadWo(3);
    } else {
      wait_vm_bar<8>(); chunk(3, 3); chunk(4, 4);
      wait_vm_bar<0>(); chunk(5, 0); chunk(6, 1);
      wait_vm_bar<63>(); chunk(7, 2);
      wait_vm_bar<63>(); loadK();
      wait_vm_bar<9>(); loadV();
      wait_vm_bar<8>(); loadWo(0);
      wait_vm_bar<8>();
      wait_vm_bar<0>(); loadWo(1); loadWo(2); loadWo(3);
    }
    wait_vm_bar<0>();
    asm volatile("" ::"v"(sink));
    asm volatile(".Lgqa_loader_end:");
    { u64 until = t00 + 100 * (tp - t00); for (int i = 0; i < (1 << 20) && rtime() < until; ++i) __builtin_amdgcn_s_sleep(4); }
    return;
  }

  unsigned codew;
  {
    u64 ip;
    int len;
    asm volatile("s_getpc_b64 %0\n1:\n\ts_mov_b32 %1, .Lgqa_loader_end-1b" : "=s"(ip), "=s"(len));
    const __amdgpu_buffer_rsrc_t cr = __builtin_amdgcn_make_buffer_rsrc((char*)ip, 0, len > 0 ? len : 0, 0x00020000);
    codew = __builtin_amdgcn_raw_buffer_load_b32(cr, lane < 2 ? (((blockIdx.x >> 3) * 4 + w8) * 2 + lane) * 128 : 0x7ffffff0, 0, 0);
  }
  const int w = w8;
  const int r = lane & 31, hf = lane >> 5;
  const int grp = bp * 4 + w;
  const int me = (grp * 8 + j) * 4 + hh;
  u64 tag, warm;
  const __amdgpu_buffer_rsrc_t wsr = __builtin_amdgcn_make_buffer_rsrc(ws, 0, 16u << 20, 0x00020000);
  const GAS u64* myflag = (const GAS u64*)(ws + WS_FLAG + (size_t)me * 512);

  const unsigned q4 = (lane & 15) >> 2, p4 = lane & 3, blk = (lane >> 4) & 1;
  const unsigned trc = (8 * hf + q4) * 256 + 32 * blk + 8 * (((p4 & 1) << 1) | (p4 >> 1));
  unsigned trb[4];
#pragma unroll
  for (int c = 0; c < 4; ++c) trb[c] = trc + ((c ^ q4) << 6);
  unsigned xfb[4];
#pragma unroll
  for (int s = 0; s < 4; ++s) xfb[s] = (w * 32 + r) * 128 + (((2 * s + hf) ^ ((r >> 1) & 7)) << 4);
  const int kr = (r & 19) | ((r & 4) << 1) | ((r & 8) >> 1);
  const LAS char* kp = lds + L_K + (w >> 1) * 16384 + kr * 256;
  const LAS char* vp = lds + L_V + (w >> 1) * 16384;

  f32x16 acc[4], sc[2];
#pragma unroll
  for (int c = 0; c < 4; ++c)
#pragma unroll
    for (int i = 0; i < 16; ++i) acc[c][i] = 0.f;
#pragma unroll
  for (int t = 0; t < 2; ++t)
#pragma unroll
    for (int i = 0; i < 16; ++i) sc[t][i] = 0.f;

  bf16x8 A[8];
  u32x4 f[8];

  {
    bf16x8 B[8];
    auto stp = [](int m) AI { return m < 112 ? (m >> 2) & 3 : m & 3; };
    auto til = [](int m) AI { return m < 112 ? m & 3 : (m >> 2) & 3; };
    auto rd = [&](int m) AI {
      const int c = m >> 4, s = stp(m), t = til(m);
      const unsigned slot = c < 3 ? c * 32768u : early ? (c % 3) * 32768u : (c % 5) * 32768u;
      if (t == 0) B[(c & 1) * 4 + s] = *(const LAS bf16x8*)(lds + slot + xfb[s]);
      A[m & 7] = tr_frag(lds + slot + 16384 + s * 4096 + trb[t]);
    };
    bar_only();
#pragma unroll
    for (int m = 0; m < 6; ++m) rd(m);
    SB();
    sfor<0, 128>([&](auto ic) AI {
      constexpr int m = decltype(ic)::v, k = m & 15, c = m >> 4;
      acc[til(m)] = MFMA(A[m & 7], B[(c & 1) * 4 + stp(m)], acc[til(m)]);
      SB();
      if constexpr (c < 7) {
        if (k < 10) rd(m + 6);
        else if (k == 12) {
          if (c == 1 ? !early : c >= 5 ? early : true) lgkm_bar();
          SB();
          if (c == 0) {
            tag = ld_flag((const GAS u64*)(ws + WS_EPOCH + grp * 128));
            warm = ld_flag(myflag + (lane & 3) * 16);
            __builtin_amdgcn_raw_buffer_store_b128(u32x4{~0u, ~0u, ~0u, ~0u}, wsr, lane * 16 + 1024, WS_P + (me ^ 4) * 8192, SC1);
          }
          SB();
          rd(m + 4); rd(m + 5);
        }
        else if (k == 13) { rd(m + 5); rd(m + 6); }
        else if (k > 13) rd(m + 6);
      } else {
        if (m + 6 < 128) rd(m + 6);
        if constexpr (k >= 5 && (k & 1)) f[(k - 5) >> 1] = pack8(acc[(k - 5) >> 2], ((k - 5) >> 1) & 1);
      }
      SB();
    });
  }
  f[6] = pack8(acc[3], 0);
  f[7] = pack8(acc[3], 1);
  lgkm_bar();
  constexpr float C = 0.08838834764831845f * 1.4426950408889634f;
  float m0, sum0 = 0.f;
  {
    bf16x8 kf[16];
    auto rdk = [&](int i) AI { kf[i] = *(const LAS bf16x8*)(kp + (i >> 3) * 8192 + (((2 * (i & 7) + hf) ^ (kr & 15)) << 4)); };
#pragma unroll
    for (int i = 0; i < 12; ++i) { rdk(i); SB(); }
    sfor<0, 16>([&](auto ic) AI {
      constexpr int m = decltype(ic)::v;
      sc[m >> 3] = MFMA(kf[m], BF(f[m & 7]), sc[m >> 3]);
      SB();
      if constexpr (m < 4) rdk(12 + m);
      if constexpr (m == 9) {
        m0 = sc[0][0];
#pragma unroll
        for (int i = 1; i < 16; ++i) m0 = fmaxf(m0, sc[0][i]);
        m0 *= C;
      }
      if constexpr (m >= 10) {
#pragma unroll
        for (int i = (m - 10) * 8 / 3; i < (m - 9) * 8 / 3; ++i) sc[0][i] = __builtin_amdgcn_exp2f(fmaf(sc[0][i], C, -m0));
      }
      SB();
    });
  }
#pragma unroll
  for (int i = 0; i < 16; ++i) sum0 += sc[0][i];
  pin(sum0);
  float inv;
  {
    float mx = sc[1][0];
#pragma unroll
    for (int i = 1; i < 16; ++i) mx = fmaxf(mx, sc[1][i]);
    mx = fmaxf(mx * C, m0);
    auto rr = __builtin_amdgcn_permlane32_swap(__float_as_uint(mx), __float_as_uint(mx), false, false);
    mx = fmaxf(__uint_as_float(rr[0]), __uint_as_float(rr[1]));
    const float a0 = __builtin_amdgcn_exp2f(m0 - mx);
    float sum = sum0 * a0;
#pragma unroll
    for (int i = 0; i < 16; ++i) sc[1][i] = __builtin_amdgcn_exp2f(fmaf(sc[1][i], C, -mx));
    f[0] = pack8s(sc[0], 0, a0);
    f[1] = pack8s(sc[0], 1, a0);
#pragma unroll
    for (int i = 0; i < 16; ++i) sum += sc[1][i];
    f[2] = pack8(sc[1], 0);
    f[3] = pack8(sc[1], 1);
    auto r2 = __builtin_amdgcn_permlane32_swap(__float_as_uint(sum), __float_as_uint(sum), false, false);
    inv = __builtin_amdgcn_rcpf(__uint_as_float(r2[0]) + __uint_as_float(r2[1]));
  }
  asm volatile("" : "+v"(f[0]), "+v"(f[1]), "+v"(f[2]), "+v"(f[3]), "+v"(inv));
  bar_only();
#pragma unroll
  for (int c = 0; c < 4; ++c)
#pragma unroll
    for (int i = 0; i < 16; ++i) acc[c][i] = 0.f;
  u32x4 of[4][8];
  {
    auto rd = [&](int m) AI { A[m & 7] = tr_frag(vp + (m & 3) * 4096 + trb[m >> 2]); };
    auto put = [&](int s) AI {
      of[0][s] = pack8s(acc[s >> 1], s & 1, inv);
      __builtin_amdgcn_raw_buffer_store_b128(of[0][s], wsr, lane * 16 + s * 1024, me * 8192, SC1);
    };
#pragma unroll
    for (int m = 0; m < 7; ++m) { rd(m); SB(); }
    sfor<0, 16>([&](auto ic) AI {
      constexpr int m = decltype(ic)::v;
      acc[m >> 2] = MFMA(A[m & 7], BF(f[m & 3]), acc[m >> 2]);
      SB();
      if constexpr (m + 7 < 16) rd(m + 7);
      if constexpr (m >= 5 && (m & 1)) put((m - 5) >> 1);
      SB();
    });
    put(6); SB(); put(7); SB();
  }
#pragma unroll
  for (int c = 0; c < 4; ++c)
#pragma unroll
    for (int i = 0; i < 16; ++i) acc[c][i] = 0.f;
  u64 poll;
  asm volatile("" : "+v"(tag) : "v"(codew), "v"(warm));
  tag += 1;
  bar_only();
  const GAS u64* fq1 = myflag + (lane < 3 ? lane : 0) * 16;
  {
    auto rd = [&](int m) AI { A[m & 7] = tr_frag(lds + (m >> 2) * 4096 + trb[m & 3]); };
#pragma unroll
    for (int m = 0; m < 7; ++m) { rd(m); SB(); }
    sfor<0, 32>([&](auto ic) AI {
      constexpr int m = decltype(ic)::v;
      acc[m & 3] = MFMA(A[m & 7], BF(of[0][m >> 2]), acc[m & 3]);
      SB();
      if constexpr (m + 7 < 32) rd(m + 7);
      if constexpr (m == 16) {
        drain_vm();
        if (lane >= 1 && lane < 5) __hip_atomic_store((GAS u64*)(ws + WS_FLAG + (size_t)(me ^ lane) * 512) + (lane - 1) * 16, tag, RLX_AGENT);
        poll = ld_flag(fq1);
      }
      SB();
    });
  }
  pin(poll);
  if (!__all(poll == tag)) wait_flags(fq1, tag);
  asm volatile("" ::: "memory");
  auto fetch = [&](int i) AI {
    of[1 + (i >> 3)][i & 7] = __builtin_amdgcn_raw_buffer_load_b128(wsr, lane * 16 + (i & 7) * 1024, (me ^ (1 + (i >> 3))) * 8192, SC1);
  };
#pragma unroll
  for (int i = 0; i < 10; ++i) { fetch(i); SB(); }
  bar_only();
  {
    auto frg = [](int m) AI { return m < 48 ? m / 3 : m < 72 ? 16 + (m & 7) : m - 72; };
    auto til = [](int m) AI { return m < 48 ? 3 - m % 3 : m < 72 ? 3 - ((m - 48) >> 3) : 0; };
    auto rd = [&](int m) AI { A[m & 7] = tr_frag(lds + 32768 + frg(m) * 4096 + trb[til(m)]); };
#pragma unroll
    for (int m = 0; m < 7; ++m) { rd(m); SB(); }
    sfor<0, 96>([&](auto ic) AI {
      constexpr int m = decltype(ic)::v;
      acc[til(m)] = MFMA(A[m & 7], BF(of[1 + (frg(m) >> 3)][frg(m) & 7]), acc[til(m)]);
      SB();
      if constexpr (m + 7 < 96) rd(m + 7);
      if constexpr (m % 2 == 0 && 10 + m / 2 < 24) fetch(10 + m / 2);
      if constexpr (m == 40) poll = ld_flag(myflag + 48);
      if constexpr (m >= 56 && m < 80 && ((m & 7) == 1 || (m & 7) == 3)) {
        constexpr int d = 2 * (3 - ((m - 56) >> 3)) + ((m & 7) == 1 ? 1 : 0);
        __builtin_amdgcn_raw_buffer_store_b128(pack8(acc[d >> 1], d & 1), wsr, lane * 16 + d * 1024, WS_P + (me ^ (d << 2)) * 8192, SC1);
      }
      if constexpr (m == 92) {
        drain_vm();
        if (lane >= 2 && lane < 8) __hip_atomic_store((GAS u64*)(ws + WS_FLAG + (size_t)(me ^ (lane << 2)) * 512) + 48 + (lane - 1), tag, RLX_AGENT);
      }
      SB();
    });
    __builtin_amdgcn_raw_buffer_store_b128(not_ones(pack8(acc[0], 1)), wsr, lane * 16 + 1024, WS_P + (me ^ 4) * 8192, SC1);
  }
  pin(poll);
  if (!__all(poll == tag)) wait_flags(myflag + 48, tag);
  asm volatile("" ::: "memory");
  u32x4 pv[7];
  {
    const GAS u64* q = myflag + 48 + (lane < 7 ? lane : 0);
#pragma clang loop unroll(disable)
    for (unsigned spins = 0; spins < SPIN_CAP; ++spins) {
      const u64 v = ld_flag(q);
      pv[0] = __builtin_amdgcn_raw_buffer_load_b128(wsr, lane * 16 + 1024, WS_P + me * 8192, SC1);
      SB();
      if (__all(v == tag)) break;
      __builtin_amdgcn_s_sleep(1);
    }
    asm volatile("" ::: "memory");
#pragma unroll
    for (int d = 2; d < 8; ++d) pv[d - 1] = __builtin_amdgcn_raw_buffer_load_b128(wsr, lane * 16 + d * 1024, WS_P + me * 8192, SC1);
    SB();
    if (!no_ones(pv[0])) {
#pragma clang loop unroll(disable)
      for (unsigned spins = 0; spins < SPIN_CAP; ++spins) {
        asm volatile("" ::: "memory");
        pv[0] = __builtin_amdgcn_raw_buffer_load_b128(wsr, lane * 16 + 1024, WS_P + me * 8192, SC1);
        if (no_ones(pv[0])) break;
      }
    }
  }
  float o[8];
#pragma unroll
  for (int i = 0; i < 8; ++i) o[i] = acc[0][i];
#pragma unroll
  for (int d = 0; d < 7; ++d)
#pragma unroll
    for (int i = 0; i < 4; ++i) {
      o[2 * i] += __uint_as_float(pv[d][i] << 16);
      o[2 * i + 1] += __uint_as_float(pv[d][i] & 0xffff0000u);
    }
  __builtin_amdgcn_raw_buffer_store_b128(u32x4{pk2(o[0], o[1]), pk2(o[2], o[3]), pk2(o[4], o[5]), pk2(o[6], o[7])},
                                         __builtin_amdgcn_make_buffer_rsrc(out, 0, 1u << 20, 0x00020000),
                                         (bp * 128 + w * 32 + r) * 1024 + hh * 256 + j * 32 + hf * 16, 0, SC1);
  if (j == 0 && hh == 0 && lane == 0) __hip_atomic_store((GAS u64*)(ws + WS_EPOCH + grp * 128), tag, RLX_AGENT);
}

extern "C" void kernel_launch(void* const* d_in, const int* in_sizes, int n_in, void* d_out, int out_size, void* d_ws, size_t ws_size,
                              hipStream_t stream) {
  gqa_fused<<<256, 512, 0, stream>>>((const char*)d_in[0], (const char*)d_in[1], (const char*)d_in[2], (const char*)d_in[3],
                                     (const char*)d_in[4], (char*)d_out, (char*)d_ws);
}
```

```cpp
#include <hip/hip_runtime.h>
#include <hip/hip_bf16.h>

typedef unsigned long long u64;
typedef short s16x4 __attribute__((ext_vector_type(4)));
typedef __bf16 bf16x8 __attribute__((ext_vector_type(8)));
typedef __bf16 bf16x2 __attribute__((ext_vector_type(2)));
typedef float f32x2 __attribute__((ext_vector_type(2)));
typedef float f32x16 __attribute__((ext_vector_type(16)));
typedef unsigned u32x4 __attribute__((ext_vector_type(4)));
#define LAS __attribute__((address_space(3)))
#define GAS __attribute__((address_space(1)))
#define DI __device__ __forceinline__
#define AI __attribute__((always_inline))
#define RLX_AGENT __ATOMIC_RELAXED, __HIP_MEMORY_SCOPE_AGENT
#define SB() __builtin_amdgcn_sched_barrier(0)
#define MFMA(a, b, c) __builtin_amdgcn_mfma_f32_32x32x16_bf16((a), (b), (c), 0, 0, 0)
#define BF(x) __builtin_bit_cast(bf16x8, x)
constexpr int SC1 = 16;

constexpr unsigned WS_P     = 8u << 20;
constexpr unsigned WS_FLAG  = 16u << 20;
constexpr unsigned WS_EPOCH = WS_FLAG + (512u << 10);
constexpr unsigned L_K = 3u * 32768, L_V = 4u * 32768, LDS_BYTES = 160u << 10;
constexpr unsigned SPIN_CAP = 1u << 20;

DI void dma16(const char* sbase, unsigned voff, unsigned lds) {
  asm volatile("s_mov_b32 m0, %2\n\ts_nop 0\n\tglobal_load_lds_dwordx4 %0, %1" ::"v"(voff), "s"(sbase), "s"(lds) : "memory");
}
DI void dma16nt(const char* sbase, unsigned voff, unsigned lds) {
  asm volatile("s_mov_b32 m0, %2\n\ts_nop 0\n\tglobal_load_lds_dwordx4 %0, %1 nt" ::"v"(voff), "s"(sbase), "s"(lds) : "memory");
}
DI void touch(unsigned& sink, const char* sbase, unsigned voff) {
  asm volatile("global_load_dword %0, %1, %2" : "=v"(sink) : "v"(voff), "s"(sbase) : "memory");
}
DI void pin(float& v) { asm volatile("" : "+v"(v)); }
DI void pin(u64& v) { asm volatile("" : "+v"(v)); }
DI void drain_vm() { asm volatile("s_waitcnt vmcnt(0)" ::: "memory"); }
template <int N> DI void wait_vm_bar() { asm volatile("s_waitcnt vmcnt(%0)\n\ts_barrier" ::"i"(N) : "memory"); }
DI void bar_only() { SB(); asm volatile("s_barrier" ::: "memory"); SB(); }
DI void lgkm_bar() { SB(); asm volatile("s_waitcnt lgkmcnt(0)\n\ts_barrier" ::: "memory"); SB(); }
DI u64 ld_flag(const GAS u64* q) { return __hip_atomic_load(q, RLX_AGENT); }

DI u64 rtime_() { u64 t; asm volatile("s_memrealtime %0\n\ts_waitcnt lgkmcnt(0)" : "=s"(t)::"memory"); return t; }
DI bf16x8 tr_frag(const LAS char* p) {
  s16x4 lo = __builtin_amdgcn_ds_read_tr16_b64_v4i16((LAS s16x4*)p);
  s16x4 hi = __builtin_amdgcn_ds_read_tr16_b64_v4i16((LAS s16x4*)(p + 1024));
  return __builtin_bit_cast(bf16x8, __builtin_shufflevector(lo, hi, 0, 1, 2, 3, 4, 5, 6, 7));
}
DI unsigned pk2(float a, float b) { f32x2 v = {a, b}; return __builtin_bit_cast(unsigned, __builtin_convertvector(v, bf16x2)); }
DI u32x4 pack8(const f32x16& a, int s) {
  return u32x4{pk2(a[8 * s], a[8 * s + 1]), pk2(a[8 * s + 2], a[8 * s + 3]), pk2(a[8 * s + 4], a[8 * s + 5]), pk2(a[8 * s + 6], a[8 * s + 7])};
}
DI u32x4 pack8s(const f32x16& a, int s, float k) {
  float t[8];
#pragma unroll
  for (int i = 0; i < 8; ++i) t[i] = a[8 * s + i] * k;
  SB();
  return u32x4{pk2(t[0], t[1]), pk2(t[2], t[3]), pk2(t[4], t[5]), pk2(t[6], t[7])};
}
DI u32x4 not_ones(u32x4 v) {
  return u32x4{min(v[0], 0xfffffffeu), min(v[1], 0xfffffffeu), min(v[2], 0xfffffffeu), min(v[3], 0xfffffffeu)};
}
DI bool no_ones(u32x4 v) { return __all(max(max(v[0], v[1]), max(v[2], v[3])) != ~0u); }
template <int I> struct IC { static constexpr int v = I; };
template <int B, int E, class F> DI void sfor(F&& f) { if constexpr (B < E) { f(IC<B>{}); sfor<B + 1, E>(f); } }

DI void wait_flags(const GAS u64* q, u64 tag) {
#pragma clang loop unroll(disable)
  for (unsigned spins = 0; spins < SPIN_CAP; ++spins) {
    if (__all(ld_flag(q) == tag)) break;
    __builtin_amdgcn_s_sleep(1);
  }
  asm volatile("" ::: "memory");
}

__global__ __launch_bounds__(512, 2) void gqa_fused(const char* __restrict__ x, const char* __restrict__ kc, const char* __restrict__ vc,
                                                   const char* __restrict__ wq, const char* __restrict__ wo, char* __restrict__ out, char* ws) {
  __shared__ __attribute__((aligned(16))) char lds_raw[LDS_BYTES];
  const LAS char* lds = (const LAS char*)lds_raw;
  const unsigned lds0 = (unsigned)(size_t)lds;
  const u64 t00 = rtime_();
  const int lane = threadIdx.x & 63;
  const int w8 = __builtin_amdgcn_readfirstlane(threadIdx.x >> 6);
  const int j = blockIdx.x & 7, bp = blockIdx.x >> 5, hh = (blockIdx.x >> 3) & 3, h = 4 * j + hh;
  const bool early = bp & 1;

  if (w8 >= 4) {
    const int lw = w8 - 4;
    const int rot = (bp + 2 * hh + (j & 1)) & 7;
    const unsigned l4 = lane >> 4, l3 = lane >> 3;
    const unsigned vx0 = l3 * 1024 + (((lane & 7) ^ l4) << 4), vx1 = vx0 ^ 64;
    const unsigned pc = (lane & 15) ^ (l4 << 2);
    const unsigned vwq = l4 * 8192 + (pc << 4);
    const unsigned vv = l4 * 256 + (pc << 4);
    const unsigned vwo = l4 * 1024 + ((pc ^ (unsigned)(j << 1)) << 4);
    const unsigned vk = l4 * 256 + (((lane & 15) ^ l4) << 4);
    const char* xb = x + (size_t)(bp * 128 + lw * 32) * 1024;
    const char* wqb = wq + (size_t)(lw * 16) * 8192 + h * 256;
    const size_t kvoff = (size_t)((bp * 2 + (lw >> 1)) * 32 + h) * 16384 + (lw & 1) * 8192;
    const char* kb = kc + kvoff;
    const char* vb = vc + kvoff;
    const char* wob = wo + (size_t)(j * 512 + lw * 16) * 1024 + hh * 256;

    auto chunk = [&](int c, int sl) AI {
      const int cc = (c + rot) & 7;
      const unsigned slot = lds0 + sl * 32768;
#pragma unroll
      for (int q = 0; q < 4; ++q) {
        dma16(wqb + (size_t)(cc * 64 + q * 4) * 8192, vwq, slot + 16384 + (lw * 16 + q * 4) * 256);
        dma16(xb + q * 8192 + cc * 128, (q & 1) ? vx1 : vx0, slot + (lw * 32 + q * 8) * 128);
      }
    };
    unsigned sink;
    auto loadK = [&]() AI {
      touch(sink, wob + (size_t)(bp * 64) * 1024, ((lane & 31) >> 1) * 1024 + (lane & 1) * 128);
#pragma unroll
      for (int i = 0; i < 8; ++i) dma16nt(kb + i * 1024, vk ^ ((i & 3) << 6), lds0 + L_K + lw * 8192 + i * 1024);
    };
    auto loadV = [&]() AI {
#pragma unroll
      for (int i = 0; i < 8; ++i) dma16nt(vb + i * 1024, vv, lds0 + L_V + lw * 8192 + i * 1024);
    };
    auto loadWo = [&](int g) AI {
      const char* b = wob + (size_t)((hh ^ g) * 128) * 1024;
#pragma unroll
      for (int q = 0; q < 8; ++q)
        dma16(b + (size_t)((q >> 2) * 64 + (q & 3) * 4) * 1024, vwo, lds0 + g * 32768 + ((q >> 2) * 64 + lw * 16 + (q & 3) * 4) * 256);
    };
    chunk(0, 0); chunk(1, 1);
    wait_vm_bar<8>(); chunk(2, 2);
    if (early) {
      wait_vm_bar<0>(); loadK(); loadV(); chunk(3, 0);
      wait_vm_bar<0>(); chunk(4, 1); chunk(5, 2);
      wait_vm_bar<8>(); chunk(6, 0);
      wait_vm_bar<8>(); chunk(7, 1);
      wait_vm_bar<8>(); loadWo(2);
      wait_vm_bar<8>(); loadWo(0);
      wait_vm_bar<63>(); loadWo(1);
      wait_vm_bar<63>();
      wait_vm_bar<0>(); loadWo(3);
    } else {
      wait_vm_bar<8>(); chunk(3, 3); chunk(4, 4);
      wait_vm_bar<0>(); chunk(5, 0); chunk(6, 1);
      wait_vm_bar<63>(); chunk(7, 2);
      wait_vm_bar<63>(); loadK();
      wait_vm_bar<9>(); loadV();
      wait_vm_bar<8>(); loadWo(0);
      wait_vm_bar<8>();
      wait_vm_bar<0>(); loadWo(1); loadWo(2); loadWo(3);
    }
    wait_vm_bar<0>();
    asm volatile("" ::"v"(sink));
    asm volatile(".Lgqa_loader_end:");
    return;
  }

  unsigned codew;
  {
    u64 ip;
    int len;
    asm volatile("s_getpc_b64 %0\n1:\n\ts_mov_b32 %1, .Lgqa_loader_end-1b" : "=s"(ip), "=s"(len));
    const __amdgpu_buffer_rsrc_t cr = __builtin_amdgcn_make_buffer_rsrc((char*)ip, 0, len > 0 ? len : 0, 0x00020000);
    codew = __builtin_amdgcn_raw_buffer_load_b32(cr, lane < 2 ? (((blockIdx.x >> 3) * 4 + w8) * 2 + lane) * 128 : 0x7ffffff0, 0, 0);
  }
  const int w = w8;
  const int r = lane & 31, hf = lane >> 5;
  const int grp = bp * 4 + w;
  const int me = (grp * 8 + j) * 4 + hh;
  const u64 tag = ld_flag((const GAS u64*)(ws + WS_EPOCH + grp * 128)) + 1;
  const __amdgpu_buffer_rsrc_t wsr = __builtin_amdgcn_make_buffer_rsrc(ws, 0, 16u << 20, 0x00020000);
  const GAS u64* myflag = (const GAS u64*)(ws + WS_FLAG + (size_t)me * 512);
  { u64 warm = ld_flag(myflag + (lane & 3) * 16); asm volatile("" ::"v"(warm)); }
  __builtin_amdgcn_raw_buffer_store_b128(u32x4{~0u, ~0u, ~0u, ~0u}, wsr, lane * 16 + 1024, WS_P + (me ^ 4) * 8192, SC1);

  const unsigned q4 = (lane & 15) >> 2, p4 = lane & 3, blk = (lane >> 4) & 1;
  const unsigned trc = (8 * hf + q4) * 256 + 32 * blk + 8 * (((p4 & 1) << 1) | (p4 >> 1));
  unsigned trb[4];
#pragma unroll
  for (int c = 0; c < 4; ++c) trb[c] = trc + ((c ^ q4) << 6);
  unsigned xfb[4];
#pragma unroll
  for (int s = 0; s < 4; ++s) xfb[s] = (w * 32 + r) * 128 + (((2 * s + hf) ^ ((r >> 1) & 7)) << 4);
  const int kr = (r & 19) | ((r & 4) << 1) | ((r & 8) >> 1);
  const LAS char* kp = lds + L_K + (w >> 1) * 16384 + kr * 256;
  const LAS char* vp = lds + L_V + (w >> 1) * 16384;

  f32x16 acc[4], sc[2];
#pragma unroll
  for (int c = 0; c < 4; ++c)
#pragma unroll
    for (int i = 0; i < 16; ++i) acc[c][i] = 0.f;
#pragma unroll
  for (int t = 0; t < 2; ++t)
#pragma unroll
    for (int i = 0; i < 16; ++i) sc[t][i] = 0.f;

  bf16x8 A[8];
  u32x4 f[8];

  {
    bf16x8 B[8];
    auto stp = [](int m) AI { return m < 112 ? (m >> 2) & 3 : m & 3; };
    auto til = [](int m) AI { return m < 112 ? m & 3 : (m >> 2) & 3; };
    auto rd = [&](int m) AI {
      const int c = m >> 4, s = stp(m), t = til(m);
      const unsigned slot = c < 3 ? c * 32768u : early ? (c % 3) * 32768u : (c % 5) * 32768u;
      if (t == 0) B[(c & 1) * 4 + s] = *(const LAS bf16x8*)(lds + slot + xfb[s]);
      A[m & 7] = tr_frag(lds + slot + 16384 + s * 4096 + trb[t]);
    };
    bar_only();
#pragma unroll
    for (int m = 0; m < 6; ++m) rd(m);
    SB();
    sfor<0, 128>([&](auto ic) AI {
      constexpr int m = decltype(ic)::v, k = m & 15, c = m >> 4;
      acc[til(m)] = MFMA(A[m & 7], B[(c & 1) * 4 + stp(m)], acc[til(m)]);
      SB();
      if constexpr (c < 7) {
        if (k < 10) rd(m + 6);
        else if (k == 12) {
          if (c == 1 ? !early : c >= 5 ? early : true) lgkm_bar();
          SB();
          rd(m + 4); rd(m + 5);
        }
        else if (k == 13) { rd(m + 5); rd(m + 6); }
        else if (k > 13) rd(m + 6);
      } else {
        if (m + 6 < 128) rd(m + 6);
        if constexpr (k >= 5 && (k & 1)) f[(k - 5) >> 1] = pack8(acc[(k - 5) >> 2], ((k - 5) >> 1) & 1);
      }
      SB();
    });
  }
  f[6] = pack8(acc[3], 0);
  f[7] = pack8(acc[3], 1);
  lgkm_bar();
  constexpr float C = 0.08838834764831845f * 1.4426950408889634f;
  float m0, sum0 = 0.f;
  {
    bf16x8 kf[16];
    auto rdk = [&](int i) AI { kf[i] = *(const LAS bf16x8*)(kp + (i >> 3) * 8192 + (((2 * (i & 7) + hf) ^ (kr & 15)) << 4)); };
#pragma unroll
    for (int i = 0; i < 12; ++i) { rdk(i); SB(); }
    sfor<0, 16>([&](auto ic) AI {
      constexpr int m = decltype(ic)::v;
      sc[m >> 3] = MFMA(kf[m], BF(f[m & 7]), sc[m >> 3]);
      SB();
      if constexpr (m < 4) rdk(12 + m);
      if constexpr (m == 9) {
        m0 = sc[0][0];
#pragma unroll
        for (int i = 1; i < 16; ++i) m0 = fmaxf(m0, sc[0][i]);
        m0 *= C;
      }
      if constexpr (m >= 10) {
#pragma unroll
        for (int i = (m - 10) * 8 / 3; i < (m - 9) * 8 / 3; ++i) sc[0][i] = __builtin_amdgcn_exp2f(fmaf(sc[0][i], C, -m0));
      }
      SB();
    });
  }
#pragma unroll
  for (int i = 0; i < 16; ++i) sum0 += sc[0][i];
  pin(sum0);
  float inv;
  {
    float mx = sc[1][0];
#pragma unroll
    for (int i = 1; i < 16; ++i) mx = fmaxf(mx, sc[1][i]);
    mx = fmaxf(mx * C, m0);
    auto rr = __builtin_amdgcn_permlane32_swap(__float_as_uint(mx), __float_as_uint(mx), false, false);
    mx = fmaxf(__uint_as_float(rr[0]), __uint_as_float(rr[1]));
    const float a0 = __builtin_amdgcn_exp2f(m0 - mx);
    float sum = sum0 * a0;
#pragma unroll
    for (int i = 0; i < 16; ++i) sc[1][i] = __builtin_amdgcn_exp2f(fmaf(sc[1][i], C, -mx));
    f[0] = pack8s(sc[0], 0, a0);
    f[1] = pack8s(sc[0], 1, a0);
#pragma unroll
    for (int i = 0; i < 16; ++i) sum += sc[1][i];
    f[2] = pack8(sc[1], 0);
    f[3] = pack8(sc[1], 1);
    auto r2 = __builtin_amdgcn_permlane32_swap(__float_as_uint(sum), __float_as_uint(sum), false, false);
    inv = __builtin_amdgcn_rcpf(__uint_as_float(r2[0]) + __uint_as_float(r2[1]));
  }
  asm volatile("" : "+v"(f[0]), "+v"(f[1]), "+v"(f[2]), "+v"(f[3]), "+v"(inv));
  bar_only();
#pragma unroll
  for (int c = 0; c < 4; ++c)
#pragma unroll
    for (int i = 0; i < 16; ++i) acc[c][i] = 0.f;
  u32x4 of[4][8];
  {
    auto rd = [&](int m) AI { A[m & 7] = tr_frag(vp + (m & 3) * 4096 + trb[m >> 2]); };
    auto put = [&](int s) AI {
      of[0][s] = pack8s(acc[s >> 1], s & 1, inv);
      __builtin_amdgcn_raw_buffer_store_b128(of[0][s], wsr, lane * 16 + s * 1024, me * 8192, SC1);
    };
#pragma unroll
    for (int m = 0; m < 7; ++m) { rd(m); SB(); }
    sfor<0, 16>([&](auto ic) AI {
      constexpr int m = decltype(ic)::v;
      acc[m >> 2] = MFMA(A[m & 7], BF(f[m & 3]), acc[m >> 2]);
      SB();
      if constexpr (m + 7 < 16) rd(m + 7);
      if constexpr (m >= 5 && (m & 1)) put((m - 5) >> 1);
      SB();
    });
    put(6); SB(); put(7); SB();
  }
#pragma unroll
  for (int c = 0; c < 4; ++c)
#pragma unroll
    for (int i = 0; i < 16; ++i) acc[c][i] = 0.f;
  u64 poll;
  asm volatile("" ::"v"(codew));
  bar_only();
  const GAS u64* fq1 = myflag + (lane < 3 ? lane : 0) * 16;
  {
    auto rd = [&](int m) AI { A[m & 7] = tr_frag(lds + (m >> 2) * 4096 + trb[m & 3]); };
#pragma unroll
    for (int m = 0; m < 7; ++m) { rd(m); SB(); }
    sfor<0, 32>([&](auto ic) AI {
      constexpr int m = decltype(ic)::v;
      acc[m & 3] = MFMA(A[m & 7], BF(of[0][m >> 2]), acc[m & 3]);
      SB();
      if constexpr (m + 7 < 32) rd(m + 7);
      if constexpr (m == 16) {
        drain_vm();
        if (lane >= 1 && lane < 5) __hip_atomic_store((GAS u64*)(ws + WS_FLAG + (size_t)(me ^ lane) * 512) + (lane - 1) * 16, tag, RLX_AGENT);
        poll = ld_flag(fq1);
      }
      SB();
    });
  }
  pin(poll);
  if (!__all(poll == tag)) wait_flags(fq1, tag);
  asm volatile("" ::: "memory");
  auto fetch = [&](int i) AI {
    of[1 + (i >> 3)][i & 7] = __builtin_amdgcn_raw_buffer_load_b128(wsr, lane * 16 + (i & 7) * 1024, (me ^ (1 + (i >> 3))) * 8192, SC1);
  };
#pragma unroll
  for (int i = 0; i < 10; ++i) { fetch(i); SB(); }
  bar_only();
  {
    auto frg = [](int m) AI { return m < 48 ? m / 3 : m < 72 ? 16 + (m & 7) : m - 72; };
    auto til = [](int m) AI { return m < 48 ? 3 - m % 3 : m < 72 ? 3 - ((m - 48) >> 3) : 0; };
    auto rd = [&](int m) AI { A[m & 7] = tr_frag(lds + 32768 + frg(m) * 4096 + trb[til(m)]); };
#pragma unroll
    for (int m = 0; m < 7; ++m) { rd(m); SB(); }
    sfor<0, 96>([&](auto ic) AI {
      constexpr int m = decltype(ic)::v;
      acc[til(m)] = MFMA(A[m & 7], BF(of[1 + (frg(m) >> 3)][frg(m) & 7]), acc[til(m)]);
      SB();
      if constexpr (m + 7 < 96) rd(m + 7);
      if constexpr (m % 2 == 0 && 10 + m / 2 < 24) fetch(10 + m / 2);
      if constexpr (m == 40) poll = ld_flag(myflag + 48);
      if constexpr (m >= 56 && m < 80 && ((m & 7) == 1 || (m & 7) == 3)) {
        constexpr int d = 2 * (3 - ((m - 56) >> 3)) + ((m & 7) == 1 ? 1 : 0);
        __builtin_amdgcn_raw_buffer_store_b128(pack8(acc[d >> 1], d & 1), wsr, lane * 16 + d * 1024, WS_P + (me ^ (d << 2)) * 8192, SC1);
      }
      if constexpr (m == 92) {
        drain_vm();
        if (lane >= 2 && lane < 8) __hip_atomic_store((GAS u64*)(ws + WS_FLAG + (size_t)(me ^ (lane << 2)) * 512) + 48 + (lane - 1), tag, RLX_AGENT);
      }
      SB();
    });
    __builtin_amdgcn_raw_buffer_store_b128(not_ones(pack8(acc[0], 1)), wsr, lane * 16 + 1024, WS_P + (me ^ 4) * 8192, SC1);
  }
  pin(poll);
  if (!__all(poll == tag)) wait_flags(myflag + 48, tag);
  asm volatile("" ::: "memory");
  u32x4 pv[7];
  {
    const GAS u64* q = myflag + 48 + (lane < 7 ? lane : 0);
#pragma clang loop unroll(disable)
    for (unsigned spins = 0; spins < SPIN_CAP; ++spins) {
      const u64 v = ld_flag(q);
      pv[0] = __builtin_amdgcn_raw_buffer_load_b128(wsr, lane * 16 + 1024, WS_P + me * 8192, SC1);
      SB();
      if (__all(v == tag)) break;
      __builtin_amdgcn_s_sleep(1);
    }
    asm volatile("" ::: "memory");
#pragma unroll
    for (int d = 2; d < 8; ++d) pv[d - 1] = __builtin_amdgcn_raw_buffer_load_b128(wsr, lane * 16 + d * 1024, WS_P + me * 8192, SC1);
    SB();
    if (!no_ones(pv[0])) {
#pragma clang loop unroll(disable)
      for (unsigned spins = 0; spins < SPIN_CAP; ++spins) {
        asm volatile("" ::: "memory");
        pv[0] = __builtin_amdgcn_raw_buffer_load_b128(wsr, lane * 16 + 1024, WS_P + me * 8192, SC1);
        if (no_ones(pv[0])) break;
      }
    }
  }
  float o[8];
#pragma unroll
  for (int i = 0; i < 8; ++i) o[i] = acc[0][i];
#pragma unroll
  for (int d = 0; d < 7; ++d)
#pragma unroll
    for (int i = 0; i < 4; ++i) {
      o[2 * i] += __uint_as_float(pv[d][i] << 16);
      o[2 * i + 1] += __uint_as_float(pv[d][i] & 0xffff0000u);
    }
  __builtin_amdgcn_raw_buffer_store_b128(u32x4{pk2(o[0], o[1]), pk2(o[2], o[3]), pk2(o[4], o[5]), pk2(o[6], o[7])},
                                         __builtin_amdgcn_make_buffer_rsrc(out, 0, 1u << 20, 0x00020000),
                                         (bp * 128 + w * 32 + r) * 1024 + hh * 256 + j * 32 + hf * 16, 0, SC1);
  if (j == 0 && hh == 0 && lane == 0) __hip_atomic_store((GAS u64*)(ws + WS_EPOCH + grp * 128), tag, RLX_AGENT);
  { asm volatile("s_waitcnt vmcnt(0)" ::: "memory"); u64 until = t00 + 50 * (rtime_() - t00); for (int i = 0; i < (1 << 20) && rtime_() < until; ++i) __builtin_amdgcn_s_sleep(4); }
}

extern "C" void kernel_launch(void* const* d_in, const int* in_sizes, int n_in, void* d_out, int out_size, void* d_ws, size_t ws_size,
                              hipStream_t stream) {
  gqa_fused<<<256, 512, 0, stream>>>((const char*)d_in[0], (const char*)d_in[1], (const char*)d_in[2], (const char*)d_in[3],
                                     (const char*)d_in[4], (char*)d_out, (char*)d_ws);
}
```

```cpp
#include <hip/hip_runtime.h>
#include <hip/hip_bf16.h>

typedef unsigned long long u64;
typedef short s16x4 __attribute__((ext_vector_type(4)));
typedef __bf16 bf16x8 __attribute__((ext_vector_type(8)));
typedef __bf16 bf16x2 __attribute__((ext_vector_type(2)));
typedef float f32x2 __attribute__((ext_vector_type(2)));
typedef float f32x16 __attribute__((ext_vector_type(16)));
typedef unsigned u32x4 __attribute__((ext_vector_type(4)));
#define LAS __attribute__((address_space(3)))
#define GAS __attribute__((address_space(1)))
#define DI __device__ __forceinline__
#define AI __attribute__((always_inline))
#define RLX_AGENT __ATOMIC_RELAXED, __HIP_MEMORY_SCOPE_AGENT
#define SB() __builtin_amdgcn_sched_barrier(0)
#define MFMA(a, b, c) __builtin_amdgcn_mfma_f32_32x32x16_bf16((a), (b), (c), 0, 0, 0)
#define BF(x) __builtin_bit_cast(bf16x8, x)
constexpr int SC1 = 16;

constexpr unsigned WS_P     = 8u << 20;
constexpr unsigned WS_FLAG  = 16u << 20;
constexpr unsigned WS_EPOCH = WS_FLAG + (512u << 10);
constexpr unsigned L_K = 3u * 32768, L_V = 4u * 32768, LDS_BYTES = 160u << 10;
constexpr unsigned SPIN_CAP = 1u << 20;

DI void dma16(const char* sbase, unsigned voff, unsigned lds) {
  asm volatile("s_mov_b32 m0, %2\n\ts_nop 0\n\tglobal_load_lds_dwordx4 %0, %1" ::"v"(voff), "s"(sbase), "s"(lds) : "memory");
}
DI void dma16nt(const char* sbase, unsigned voff, unsigned lds) {
  asm volatile("s_mov_b32 m0, %2\n\ts_nop 0\n\tglobal_load_lds_dwordx4 %0, %1 nt" ::"v"(voff), "s"(sbase), "s"(lds) : "memory");
}
DI void touch(unsigned& sink, const char* sbase, unsigned voff) {
  asm volatile("global_load_dword %0, %1, %2" : "=v"(sink) : "v"(voff), "s"(sbase) : "memory");
}
DI void pin(float& v) { asm volatile("" : "+v"(v)); }
DI void pin(u64& v) { asm volatile("" : "+v"(v)); }
DI void drain_vm() { asm volatile("s_waitcnt vmcnt(0)" ::: "memory"); }
template <int N> DI void wait_vm_bar() { asm volatile("s_waitcnt vmcnt(%0)\n\ts_barrier" ::"i"(N) : "memory"); }
DI void bar_only() { SB(); asm volatile("s_barrier" ::: "memory"); SB(); }
DI void lgkm_bar() { SB(); asm volatile("s_waitcnt lgkmcnt(0)\n\ts_barrier" ::: "memory"); SB(); }
DI u64 ld_flag(const GAS u64* q) { return __hip_atomic_load(q, RLX_AGENT); }

DI bf16x8 tr_frag(const LAS char* p) {
  s16x4 lo = __builtin_amdgcn_ds_read_tr16_b64_v4i16((LAS s16x4*)p);
  s16x4 hi = __builtin_amdgcn_ds_read_tr16_b64_v4i16((LAS s16x4*)(p + 1024));
  return __builtin_bit_cast(bf16x8, __builtin_shufflevector(lo, hi, 0, 1, 2, 3, 4, 5, 6, 7));
}
DI unsigned pk2(float a, float b) { f32x2 v = {a, b}; return __builtin_bit_cast(unsigned, __builtin_convertvector(v, bf16x2)); }
DI u32x4 pack8(const f32x16& a, int s) {
  return u32x4{pk2(a[8 * s], a[8 * s + 1]), pk2(a[8 * s + 2], a[8 * s + 3]), pk2(a[8 * s + 4], a[8 * s + 5]), pk2(a[8 * s + 6], a[8 * s + 7])};
}
DI u32x4 pack8s(const f32x16& a, int s, float k) {
  float t[8];
#pragma unroll
  for (int i = 0; i < 8; ++i) t[i] = a[8 * s + i] * k;
  SB();
  return u32x4{pk2(t[0], t[1]), pk2(t[2], t[3]), pk2(t[4], t[5]), pk2(t[6], t[7])};
}
DI u32x4 not_ones(u32x4 v) {
  return u32x4{min(v[0], 0xfffffffeu), min(v[1], 0xfffffffeu), min(v[2], 0xfffffffeu), min(v[3], 0xfffffffeu)};
}
DI bool no_ones(u32x4 v) { return __all(max(max(v[0], v[1]), max(v[2], v[3])) != ~0u); }
template <int I> struct IC { static constexpr int v = I; };
template <int B, int E, class F> DI void sfor(F&& f) { if constexpr (B < E) { f(IC<B>{}); sfor<B + 1, E>(f); } }

DI void wait_flags(const GAS u64* q, u64 tag) {
#pragma clang loop unroll(disable)
  for (unsigned spins = 0; spins < SPIN_CAP; ++spins) {
    if (__all(ld_flag(q) == tag)) break;
    __builtin_amdgcn_s_sleep(1);
  }
  asm volatile("" ::: "memory");
}

__global__ __launch_bounds__(512, 2) void gqa_fused(const char* __restrict__ x, const char* __restrict__ kc, const char* __restrict__ vc,
                                                   const char* __restrict__ wq, const char* __restrict__ wo, char* __restrict__ out, char* ws) {
  __shared__ __attribute__((aligned(16))) char lds_raw[LDS_BYTES];
  const LAS char* lds = (const LAS char*)lds_raw;
  const unsigned lds0 = (unsigned)(size_t)lds;
  const int lane = threadIdx.x & 63;
  const int w8 = __builtin_amdgcn_readfirstlane(threadIdx.x >> 6);
  const int j = blockIdx.x & 7, bp = blockIdx.x >> 5, hh = (blockIdx.x >> 3) & 3, h = 4 * j + hh;
  const bool early = bp < 4;

  if (w8 >= 4) {
    const int lw = w8 - 4;
    const int rot = (bp + 2 * hh + (j & 1)) & 7;
    const unsigned l4 = lane >> 4, l3 = lane >> 3;
    const unsigned vx0 = l3 * 1024 + (((lane & 7) ^ l4) << 4), vx1 = vx0 ^ 64;
    const unsigned pc = (lane & 15) ^ (l4 << 2);
    const unsigned vwq = l4 * 8192 + (pc << 4);
    const unsigned vv = l4 * 256 + (pc << 4);
    const unsigned vwo = l4 * 1024 + ((pc ^ (unsigned)(j << 1)) << 4);
    const unsigned vk = l4 * 256 + (((lane & 15) ^ l4) << 4);
    const char* xb = x + (size_t)(bp * 128 + lw * 32) * 1024;
    const char* wqb = wq + (size_t)(lw * 16) * 8192 + h * 256;
    const size_t kvoff = (size_t)((bp * 2 + (lw >> 1)) * 32 + h) * 16384 + (lw & 1) * 8192;
    const char* kb = kc + kvoff;
    const char* vb = vc + kvoff;
    const char* wob = wo + (size_t)(j * 512 + lw * 16) * 1024 + hh * 256;

    auto chunk = [&](int c, int sl) AI {
      const int cc = (c + rot) & 7;
      const unsigned slot = lds0 + sl * 32768;
#pragma unroll
      for (int q = 0; q < 4; ++q) {
        dma16(wqb + (size_t)(cc * 64 + q * 4) * 8192, vwq, slot + 16384 + (lw * 16 + q * 4) * 256);
        dma16(xb + q * 8192 + cc * 128, (q & 1) ? vx1 : vx0, slot + (lw * 32 + q * 8) * 128);
      }
    };
    unsigned sink;
    auto loadK = [&]() AI {
      touch(sink, wob + (size_t)(bp * 64) * 1024, ((lane & 31) >> 1) * 1024 + (lane & 1) * 128);
#pragma unroll
      for (int i = 0; i < 8; ++i) dma16nt(kb + i * 1024, vk ^ ((i & 3) << 6), lds0 + L_K + lw * 8192 + i * 1024);
    };
    auto loadV = [&]() AI {
#pragma unroll
      for (int i = 0; i < 8; ++i) dma16nt(vb + i * 1024, vv, lds0 + L_V + lw * 8192 + i * 1024);
    };
    auto loadWo = [&](int g) AI {
      const char* b = wob + (size_t)((hh ^ g) * 128) * 1024;
#pragma unroll
      for (int q = 0; q < 8; ++q)
        dma16(b + (size_t)((q >> 2) * 64 + (q & 3) * 4) * 1024, vwo, lds0 + g * 32768 + ((q >> 2) * 64 + lw * 16 + (q & 3) * 4) * 256);
    };
    chunk(0, 0); chunk(1, 1);
    wait_vm_bar<8>(); chunk(2, 2);
    if (early) {
      wait_vm_bar<0>(); loadK(); loadV(); chunk(3, 0);
      wait_vm_bar<0>(); chunk(4, 1); chunk(5, 2);
      wait_vm_bar<8>(); chunk(6, 0);
      wait_vm_bar<8>(); chunk(7, 1);
      wait_vm_bar<8>(); loadWo(2);
      wait_vm_bar<8>(); loadWo(0);
      wait_vm_bar<63>(); loadWo(1);
      wait_vm_bar<63>();
      wait_vm_bar<0>(); loadWo(3);
    } else {
      wait_vm_bar<8>(); chunk(3, 3); chunk(4, 4);
      wait_vm_bar<0>(); chunk(5, 0); chunk(6, 1);
      wait_vm_bar<63>(); chunk(7, 2);
      wait_vm_bar<63>(); loadK();
      wait_vm_bar<9>(); loadV();
      wait_vm_bar<8>(); loadWo(0);
      wait_vm_bar<8>();
      wait_vm_bar<0>(); loadWo(1); loadWo(2); loadWo(3);
    }
    wait_vm_bar<0>();
    asm volatile("" ::"v"(sink));
    asm volatile(".Lgqa_loader_end:");
    return;
  }

  unsigned codew;
  {
    u64 ip;
    int len;
    asm volatile("s_getpc_b64 %0\n1:\n\ts_mov_b32 %1, .Lgqa_loader_end-1b" : "=s"(ip), "=s"(len));
    const __amdgpu_buffer_rsrc_t cr = __builtin_amdgcn_make_buffer_rsrc((char*)ip, 0, len > 0 ? len : 0, 0x00020000);
    codew = __builtin_amdgcn_raw_buffer_load_b32(cr, lane < 2 ? (((blockIdx.x >> 3) * 4 + w8) * 2 + lane) * 128 : 0x7ffffff0, 0, 0);
  }
  const int w = w8;
  const int r = lane & 31, hf = lane >> 5;
  const int grp = bp * 4 + w;
  const int me = (grp * 8 + j) * 4 + hh;
  const u64 tag = ld_flag((const GAS u64*)(ws + WS_EPOCH + grp * 128)) + 1;
  const __amdgpu_buffer_rsrc_t wsr = __builtin_amdgcn_make_buffer_rsrc(ws, 0, 16u << 20, 0x00020000);
  const GAS u64* myflag = (const GAS u64*)(ws + WS_FLAG + (size_t)me * 512);
  { u64 warm = ld_flag(myflag + (lane & 3) * 16); asm volatile("" ::"v"(warm)); }
  __builtin_amdgcn_raw_buffer_store_b128(u32x4{~0u, ~0u, ~0u, ~0u}, wsr, lane * 16 + 1024, WS_P + (me ^ 4) * 8192, SC1);

  const unsigned q4 = (lane & 15) >> 2, p4 = lane & 3, blk = (lane >> 4) & 1;
  const unsigned trc = (8 * hf + q4) * 256 + 32 * blk + 8 * (((p4 & 1) << 1) | (p4 >> 1));
  unsigned trb[4];
#pragma unroll
  for (int c = 0; c < 4; ++c) trb[c] = trc + ((c ^ q4) << 6);
  unsigned xfb[4];
#pragma unroll
  for (int s = 0; s < 4; ++s) xfb[s] = (w * 32 + r) * 128 + (((2 * s + hf) ^ ((r >> 1) & 7)) << 4);
  const int kr = (r & 19) | ((r & 4) << 1) | ((r & 8) >> 1);
  const LAS char* kp = lds + L_K + (w >> 1) * 16384 + kr * 256;
  const LAS char* vp = lds + L_V + (w >> 1) * 16384;

  f32x16 acc[4], sc[2];
#pragma unroll
  for (int c = 0; c < 4; ++c)
#pragma unroll
    for (int i = 0; i < 16; ++i) acc[c][i] = 0.f;
#pragma unroll
  for (int t = 0; t < 2; ++t)
#pragma unroll
    for (int i = 0; i < 16; ++i) sc[t][i] = 0.f;

  bf16x8 A[8];
  u32x4 f[8];

  {
    bf16x8 B[8];
    auto stp = [](int m) AI { return m < 112 ? (m >> 2) & 3 : m & 3; };
    auto til = [](int m) AI { return m < 112 ? m & 3 : (m >> 2) & 3; };
    auto rd = [&](int m) AI {
      const int c = m >> 4, s = stp(m), t = til(m);
      const unsigned slot = c < 3 ? c * 32768u : early ? (c % 3) * 32768u : (c % 5) * 32768u;
      if (t == 0) B[(c & 1) * 4 + s] = *(const LAS bf16x8*)(lds + slot + xfb[s]);
      A[m & 7] = tr_frag(lds + slot + 16384 + s * 4096 + trb[t]);
    };
    bar_only();
#pragma unroll
    for (int m = 0; m < 6; ++m) rd(m);
    SB();
    sfor<0, 128>([&](auto ic) AI {
      constexpr int m = decltype(ic)::v, k = m & 15, c = m >> 4;
      acc[til(m)] = MFMA(A[m & 7], B[(c & 1) * 4 + stp(m)], acc[til(m)]);
      SB();
      if constexpr (c < 7) {
        if (k < 10) rd(m + 6);
        else if (k == 12) {
          if (c == 1 ? !early : c >= 5 ? early : true) lgkm_bar();
          SB();
          rd(m + 4); rd(m + 5);
        }
        else if (k == 13) { rd(m + 5); rd(m + 6); }
        else if (k > 13) rd(m + 6);
      } else {
        if (m + 6 < 128) rd(m + 6);
        if constexpr (k >= 5 && (k & 1)) f[(k - 5) >> 1] = pack8(acc[(k - 5) >> 2], ((k - 5) >> 1) & 1);
      }
      SB();
    });
  }
  f[6] = pack8(acc[3], 0);
  f[7] = pack8(acc[3], 1);
  lgkm_bar();
  constexpr float C = 0.08838834764831845f * 1.4426950408889634f;
  float m0, sum0 = 0.f;
  {
    bf16x8 kf[16];
    auto rdk = [&](int i) AI { kf[i] = *(const LAS bf16x8*)(kp + (i >> 3) * 8192 + (((2 * (i & 7) + hf) ^ (kr & 15)) << 4)); };
#pragma unroll
    for (int i = 0; i < 12; ++i) { rdk(i); SB(); }
    sfor<0, 16>([&](auto ic) AI {
      constexpr int m = decltype(ic)::v;
      sc[m >> 3] = MFMA(kf[m], BF(f[m & 7]), sc[m >> 3]);
      SB();
      if constexpr (m < 4) rdk(12 + m);
      if constexpr (m == 9) {
        m0 = sc[0][0];
#pragma unroll
        for (int i = 1; i < 16; ++i) m0 = fmaxf(m0, sc[0][i]);
        m0 *= C;
      }
      if constexpr (m >= 10) {
#pragma unroll
        for (int i = (m - 10) * 8 / 3; i < (m - 9) * 8 / 3; ++i) sc[0][i] = __builtin_amdgcn_exp2f(fmaf(sc[0][i], C, -m0));
      }
      SB();
    });
  }
#pragma unroll
  for (int i = 0; i < 16; ++i) sum0 += sc[0][i];
  pin(sum0);
  float inv;
  {
    float mx = sc[1][0];
#pragma unroll
    for (int i = 1; i < 16; ++i) mx = fmaxf(mx, sc[1][i]);
    mx = fmaxf(mx * C, m0);
    auto rr = __builtin_amdgcn_permlane32_swap(__float_as_uint(mx), __float_as_uint(mx), false, false);
    mx = fmaxf(__uint_as_float(rr[0]), __uint_as_float(rr[1]));
    const float a0 = __builtin_amdgcn_exp2f(m0 - mx);
    float sum = sum0 * a0;
#pragma unroll
    for (int i = 0; i < 16; ++i) sc[1][i] = __builtin_amdgcn_exp2f(fmaf(sc[1][i], C, -mx));
    f[0] = pack8s(sc[0], 0, a0);
    f[1] = pack8s(sc[0], 1, a0);
#pragma unroll
    for (int i = 0; i < 16; ++i) sum += sc[1][i];
    f[2] = pack8(sc[1], 0);
    f[3] = pack8(sc[1], 1);
    auto r2 = __builtin_amdgcn_permlane32_swap(__float_as_uint(sum), __float_as_uint(sum), false, false);
    inv = __builtin_amdgcn_rcpf(__uint_as_float(r2[0]) + __uint_as_float(r2[1]));
  }
  asm volatile("" : "+v"(f[0]), "+v"(f[1]), "+v"(f[2]), "+v"(f[3]), "+v"(inv));
  bar_only();
#pragma unroll
  for (int c = 0; c < 4; ++c)
#pragma unroll
    for (int i = 0; i < 16; ++i) acc[c][i] = 0.f;
  u32x4 of[4][8];
  {
    auto rd = [&](int m) AI { A[m & 7] = tr_frag(vp + (m & 3) * 4096 + trb[m >> 2]); };
    auto put = [&](int s) AI {
      of[0][s] = pack8s(acc[s >> 1], s & 1, inv);
      __builtin_amdgcn_raw_buffer_store_b128(of[0][s], wsr, lane * 16 + s * 1024, me * 8192, SC1);
    };
#pragma unroll
    for (int m = 0; m < 7; ++m) { rd(m); SB(); }
    sfor<0, 16>([&](auto ic) AI {
      constexpr int m = decltype(ic)::v;
      acc[m >> 2] = MFMA(A[m & 7], BF(f[m & 3]), acc[m >> 2]);
      SB();
      if constexpr (m + 7 < 16) rd(m + 7);
      if constexpr (m >= 5 && (m & 1)) put((m - 5) >> 1);
      SB();
    });
    put(6); SB(); put(7); SB();
  }
#pragma unroll
  for (int c = 0; c < 4; ++c)
#pragma unroll
    for (int i = 0; i < 16; ++i) acc[c][i] = 0.f;
  u64 poll;
  asm volatile("" ::"v"(codew));
  bar_only();
  const GAS u64* fq1 = myflag + (lane < 3 ? lane : 0) * 16;
  {
    auto rd = [&](int m) AI { A[m & 7] = tr_frag(lds + (m >> 2) * 4096 + trb[m & 3]); };
#pragma unroll
    for (int m = 0; m < 7; ++m) { rd(m); SB(); }
    sfor<0, 32>([&](auto ic) AI {
      constexpr int m = decltype(ic)::v;
      acc[m & 3] = MFMA(A[m & 7], BF(of[0][m >> 2]), acc[m & 3]);
      SB();
      if constexpr (m + 7 < 32) rd(m + 7);
      if constexpr (m == 16) {
        drain_vm();
        if (lane >= 1 && lane < 5) __hip_atomic_store((GAS u64*)(ws + WS_FLAG + (size_t)(me ^ lane) * 512) + (lane - 1) * 16, tag, RLX_AGENT);
        poll = ld_flag(fq1);
      }
      SB();
    });
  }
  pin(poll);
  if (!__all(poll == tag)) wait_flags(fq1, tag);
  asm volatile("" ::: "memory");
  auto fetch = [&](int i) AI {
    of[1 + (i >> 3)][i & 7] = __builtin_amdgcn_raw_buffer_load_b128(wsr, lane * 16 + (i & 7) * 1024, (me ^ (1 + (i >> 3))) * 8192, SC1);
  };
#pragma unroll
  for (int i = 0; i < 10; ++i) { fetch(i); SB(); }
  bar_only();
  {
    auto frg = [](int m) AI { return m < 48 ? m / 3 : m < 72 ? 16 + (m & 7) : m - 72; };
    auto til = [](int m) AI { return m < 48 ? 3 - m % 3 : m < 72 ? 3 - ((m - 48) >> 3) : 0; };
    auto rd = [&](int m) AI { A[m & 7] = tr_frag(lds + 32768 + frg(m) * 4096 + trb[til(m)]); };
#pragma unroll
    for (int m = 0; m < 7; ++m) { rd(m); SB(); }
    sfor<0, 96>([&](auto ic) AI {
      constexpr int m = decltype(ic)::v;
      acc[til(m)] = MFMA(A[m & 7], BF(of[1 + (frg(m) >> 3)][frg(m) & 7]), acc[til(m)]);
      SB();
      if constexpr (m + 7 < 96) rd(m + 7);
      if constexpr (m % 2 == 0 && 10 + m / 2 < 24) fetch(10 + m / 2);
      if constexpr (m == 40) poll = ld_flag(myflag + 48);
      if constexpr (m >= 56 && m < 80 && ((m & 7) == 1 || (m & 7) == 3)) {
        constexpr int d = 2 * (3 - ((m - 56) >> 3)) + ((m & 7) == 1 ? 1 : 0);
        __builtin_amdgcn_raw_buffer_store_b128(pack8(acc[d >> 1], d & 1), wsr, lane * 16 + d * 1024, WS_P + (me ^ (d << 2)) * 8192, SC1);
      }
      if constexpr (m == 92) {
        drain_vm();
        if (lane >= 2 && lane < 8) __hip_atomic_store((GAS u64*)(ws + WS_FLAG + (size_t)(me ^ (lane << 2)) * 512) + 48 + (lane - 1), tag, RLX_AGENT);
      }
      SB();
    });
    __builtin_amdgcn_raw_buffer_store_b128(not_ones(pack8(acc[0], 1)), wsr, lane * 16 + 1024, WS_P + (me ^ 4) * 8192, SC1);
  }
  pin(poll);
  if (!__all(poll == tag)) wait_flags(myflag + 48, tag);
  asm volatile("" ::: "memory");
  u32x4 pv[7];
  {
    const GAS u64* q = myflag + 48 + (lane < 7 ? lane : 0);
#pragma clang loop unroll(disable)
    for (unsigned spins = 0; spins < SPIN_CAP; ++spins) {
      const u64 v = ld_flag(q);
      pv[0] = __builtin_amdgcn_raw_buffer_load_b128(wsr, lane * 16 + 1024, WS_P + me * 8192, SC1);
      SB();
      if (__all(v == tag)) break;
      __builtin_amdgcn_s_sleep(1);
    }
    asm volatile("" ::: "memory");
#pragma unroll
    for (int d = 2; d < 8; ++d) pv[d - 1] = __builtin_amdgcn_raw_buffer_load_b128(wsr, lane * 16 + d * 1024, WS_P + me * 8192, SC1);
    SB();
    if (!no_ones(pv[0])) {
#pragma clang loop unroll(disable)
      for (unsigned spins = 0; spins < SPIN_CAP; ++spins) {
        asm volatile("" ::: "memory");
        pv[0] = __builtin_amdgcn_raw_buffer_load_b128(wsr, lane * 16 + 1024, WS_P + me * 8192, SC1);
        if (no_ones(pv[0])) break;
      }
    }
  }
  float o[8];
#pragma unroll
  for (int i = 0; i < 8; ++i) o[i] = acc[0][i];
#pragma unroll
  for (int d = 0; d < 7; ++d)
#pragma unroll
    for (int i = 0; i < 4; ++i) {
      o[2 * i] += __uint_as_float(pv[d][i] << 16);
      o[2 * i + 1] += __uint_as_float(pv[d][i] & 0xffff0000u);
    }
  __builtin_amdgcn_raw_buffer_store_b128(u32x4{pk2(o[0], o[1]), pk2(o[2], o[3]), pk2(o[4], o[5]), pk2(o[6], o[7])},
                                         __builtin_amdgcn_make_buffer_rsrc(out, 0, 1u << 20, 0x00020000),
                                         (bp * 128 + w * 32 + r) * 1024 + hh * 256 + j * 32 + hf * 16, 0, SC1);
  if (j == 0 && hh == 0 && lane == 0) __hip_atomic_store((GAS u64*)(ws + WS_EPOCH + grp * 128), tag, RLX_AGENT);
}

extern "C" void kernel_launch(void* const* d_in, const int* in_sizes, int n_in, void* d_out, int out_size, void* d_ws, size_t ws_size,
                              hipStream_t stream) {
  gqa_fused<<<256, 512, 0, stream>>>((const char*)d_in[0], (const char*)d_in[1], (const char*)d_in[2], (const char*)d_in[3],
                                     (const char*)d_in[4], (char*)d_out, (char*)d_ws);
}
```
